```python
import torch

def _softmax(x, axis=-1):
    return torch.softmax(x, dim=axis)

def _gelu(x):
    return torch.nn.functional.gelu(x, approximate='tanh')

def _silu(x):
    return torch.nn.functional.silu(x)

def _sigmoid(x):
    return torch.sigmoid(x)

def _layernorm(x, axis=-1, eps=1e-05):
    m = x.mean(dim=axis, keepdim=True)
    v = x.var(dim=axis, keepdim=True, unbiased=False)
    return (x - m) / torch.sqrt(v + eps)

def _scatter_add(tgt, idx, src, axis=0):
    return tgt.clone().index_add_(axis, idx, src)

def _segment_sum(src, seg, n):
    out = torch.zeros((n,) + src.shape[1:], dtype=src.dtype)
    return out.index_add_(0, seg, src)

def _pad(x, pw):
    flat = tuple((v for pair in reversed(pw) for v in pair))
    return torch.nn.functional.pad(x, flat)
kernel_name = 'recurrent_scan__memory_aug_recurrent_mem_tr__swiglu_ffn__prompt'

def setup_inputs():

    _w = lambda a: (lambda t: t.to(torch.bfloat16) if t.is_floating_point() else t)(torch.as_tensor(a))

    def _A_setup():
        B, S, Dm = (1, 2048, 448)
        H, Dh = (16, 256)
        M, seg = (8, 32)
        seg = min(seg, S)
        S = S // seg * seg
        return {'x': _w((lambda *s: torch.randn(*s, dtype=torch.bfloat16) * 0.05)(B, S, Dm)), 'mem0': _w((lambda *s: torch.randn(*s, dtype=torch.bfloat16) * 0.2)(B, M, Dm)), 'Wq': _w((lambda *s: torch.randn(*s, dtype=torch.bfloat16) * 1.6)(Dm, H, Dh) * Dm ** (-0.5)), 'Wk': _w((lambda *s: torch.randn(*s, dtype=torch.bfloat16) * 1.6)(Dm, H, Dh) * Dm ** (-0.5)), 'Wv': _w((lambda *s: torch.randn(*s, dtype=torch.bfloat16) * 0.2)(Dm, H, Dh) * Dm ** (-0.5)), 'Wo': _w((lambda *s: torch.randn(*s, dtype=torch.bfloat16) * 0.2)(H, Dh, Dm) * (H * Dh) ** (-0.5)), 'seg_len': seg}
    _A_in = _A_setup()

    def _B_setup():
        B, S, D, F = (1, 2048, 448, 1792)
        return {'x': _w((lambda *s: torch.randn(*s, dtype=torch.bfloat16) * 0.05)(B, S, D)), 'w_gate': _w((lambda *s: torch.randn(*s, dtype=torch.bfloat16) * 0.1)(D, F)), 'w_up': _w((lambda *s: torch.randn(*s, dtype=torch.bfloat16) * 0.1)(D, F)), 'w_down': _w((lambda *s: torch.randn(*s, dtype=torch.bfloat16) * 0.1)(F, D))}
    _B_in = _B_setup()
    return {'A_x': _A_in['x'], 'A_mem0': _A_in['mem0'], 'A_Wq': _A_in['Wq'], 'A_Wk': _A_in['Wk'], 'A_Wv': _A_in['Wv'], 'A_Wo': _A_in['Wo'], 'A_seg_len': _A_in['seg_len'], 'B_x': _B_in['x'], 'B_w_gate': _B_in['w_gate'], 'B_w_up': _B_in['w_up'], 'B_w_down': _B_in['w_down']}

def reference(A_x, A_mem0, A_Wq, A_Wk, A_Wv, A_Wo, A_seg_len, B_x, B_w_gate, B_w_up, B_w_down):

    def _A_ref(x, mem0, Wq, Wk, Wv, Wo, seg_len):
        B, S, Dm = x.shape
        H, Dh = (Wq.shape[1], Wq.shape[2])
        M = mem0.shape[1]
        scale = Dh ** (-0.5)
        n_seg = S // seg_len
        L = M + seg_len
        idx = torch.arange(L)
        mask = (idx[:, None] < M) | (idx[:, None] >= idx[None, :])
        mem = mem0
        outs = []
        for s in range(n_seg):
            xs = x[:, s * seg_len:(s + 1) * seg_len, :].clone()
            h = torch.concatenate([mem, xs], axis=1)
            q = torch.einsum('btd,dhe->bhte', h, Wq)
            k = torch.einsum('btd,dhe->bhte', h, Wk)
            v = torch.einsum('btd,dhe->bhte', h, Wv)
            scores = torch.einsum('bhqe,bhke->bhqk', q, k) * scale
            scores = torch.where(mask[None, None], scores, -1000000000.0)
            p = _softmax(scores, axis=-1)
            o = torch.einsum('bhqk,bhke->bhqe', p, v)
            o = torch.einsum('bhte,hed->btd', o, Wo)
            mem = o[:, :M, :].clone()
            outs.append(o[:, M:, :])
        return torch.concatenate(outs, axis=1)

    def _B_ref(x, w_gate, w_up, w_down):
        g = _silu(x @ w_gate)
        u = x @ w_up
        return g * u @ w_down
    x = A_x
    xn = _layernorm(x, axis=-1)
    s = _A_ref(xn, A_mem0, A_Wq, A_Wk, A_Wv, A_Wo, A_seg_len)
    g = _silu(_B_ref(xn, B_w_gate, B_w_up, B_w_down))
    return x + s * g
```

```cpp
#include <hip/hip_runtime.h>
#include <hip/hip_bf16.h>
#define LN_MODE 0
typedef unsigned short u16;
__device__ __forceinline__ float b2f(u16 v) { return __uint_as_float((unsigned)v << 16); }
__device__ __forceinline__ u16 f2b(float f) { __bf16 b = (__bf16)f; return __builtin_bit_cast(u16, b); }
__device__ __forceinline__ float rb(float f) { return b2f(f2b(f)); }
constexpr int S = 2048, D = 448, H = 16, E = 256, M = 8, SEG = 32, F = 1792, NW = 2 * H * D;

__global__ void ln_k(const u16* x, u16* xn) {
  const int row = blockIdx.x, lane = threadIdx.x;
  float v[7]; float s = 0;
  for (int j = 0; j < 7; ++j) { v[j] = b2f(x[row * D + lane + 64 * j]); s += v[j]; }
  for (int o = 32; o; o >>= 1) s += __shfl_xor(s, o);
  const float mean = s / D; float q = 0;
  for (int j = 0; j < 7; ++j) { float d = v[j] - mean; q += d * d; }
  for (int o = 32; o; o >>= 1) q += __shfl_xor(q, o);
  const float var = q / D;
  if (LN_MODE == 1) {
    const float m = rb(mean), den = rb(sqrtf(rb(rb(var) + 1e-5f)));
    for (int j = 0; j < 7; ++j) xn[row * D + lane + 64 * j] = f2b(rb(v[j] - m) / den);
  } else {
    const float r = 1.0f / sqrtf(var + 1e-5f);
    for (int j = 0; j < 7; ++j) xn[row * D + lane + 64 * j] = f2b((v[j] - mean) * r);
  }
}

__global__ void gemm_k(const u16* A, const u16* B, u16* C, int Mr, int N, int K, int sam, int sak, int sbk, int sbn, int ldc, int za, int zb, int zc, float alpha) {
  __shared__ float As[32][33], Bs[32][33];
  A += (size_t)blockIdx.z * za; B += (size_t)blockIdx.z * zb; C += (size_t)blockIdx.z * zc;
  const int tx = threadIdx.x & 15, ty = threadIdx.x >> 4, m0 = blockIdx.y * 32, n0 = blockIdx.x * 32;
  float c[2][2] = {{0, 0}, {0, 0}};
  for (int k0 = 0; k0 < K; k0 += 32) {
    for (int i = threadIdx.x; i < 1024; i += 256) {
      int r = i >> 5, q = i & 31;
      As[r][q] = (m0 + r < Mr) ? b2f(A[(size_t)(m0 + r) * sam + (size_t)(k0 + q) * sak]) : 0.f;
      if (sbn == 1) Bs[r][q] = b2f(B[(size_t)(k0 + r) * sbk + (n0 + q)]);
      else Bs[q][r] = b2f(B[(size_t)(k0 + q) * sbk + (size_t)(n0 + r) * sbn]);
    }
    __syncthreads();
    for (int k = 0; k < 32; ++k) {
      float a0 = As[ty][k], a1 = As[ty + 16][k], b0 = Bs[k][tx], b1 = Bs[k][tx + 16];
      c[0][0] += a0 * b0; c[0][1] += a0 * b1; c[1][0] += a1 * b0; c[1][1] += a1 * b1;
    }
    __syncthreads();
  }
  for (int i = 0; i < 2; ++i) for (int j = 0; j < 2; ++j) if (m0 + ty + 16 * i < Mr) C[(size_t)(m0 + ty + 16 * i) * ldc + n0 + tx + 16 * j] = f2b(alpha * c[i][j]);
}

__global__ void attn_k(const u16* xn, const u16* KV, const u16* mem, const u16* KVm, u16* memo, u16* sout, int s) {
  __shared__ float q[D], P[H][40];
  const int i = blockIdx.x, t = threadIdx.x;
  const u16* qr = i < M ? mem + i * D : xn + (size_t)(s * SEG + i - M) * D;
  for (int d = t; d < D; d += 256) q[d] = b2f(qr[d]);
  __syncthreads();
  for (int p = t; p < H * 40; p += 256) {
    const int h = p / 40, j = p % 40;
    const u16* kr = (j < M ? KVm + (size_t)j * NW : KV + (size_t)(s * SEG + j - M) * NW) + h * D;
    float a = 0; for (int d = 0; d < D; ++d) a += q[d] * b2f(kr[d]);
    P[h][j] = (i < M || i >= j) ? a : -1e9f;
  }
  __syncthreads();
  if (t < H) {
    float m = -3e38f; for (int j = 0; j < 40; ++j) m = fmaxf(m, P[t][j]);
    float z = 0; for (int j = 0; j < 40; ++j) { P[t][j] = __expf(P[t][j] - m); z += P[t][j]; }
    for (int j = 0; j < 40; ++j) P[t][j] = rb(P[t][j] / z);
  }
  __syncthreads();
  for (int d = t; d < D; d += 256) {
    float a = 0;
    for (int h = 0; h < H; ++h) for (int j = 0; j < 40; ++j) {
      const u16* vr = (j < M ? KVm + (size_t)j * NW : KV + (size_t)(s * SEG + j - M) * NW) + (H + h) * D;
      a += P[h][j] * b2f(vr[d]);
    }
    if (i < M) memo[i * D + d] = f2b(a); else sout[(size_t)(s * SEG + i - M) * D + d] = f2b(a);
  }
}

__global__ void glu_k(const u16* g, const u16* u, u16* hgu, int n) {
  int i = blockIdx.x * 256 + threadIdx.x; if (i >= n) return;
  float a = b2f(g[i]); float sg = rb(a / (1.f + __expf(-a)));
  hgu[i] = f2b(sg * b2f(u[i]));
}
__global__ void fin_k(const u16* x, const u16* sv, const u16* dn, u16* out, int n) {
  int i = blockIdx.x * 256 + threadIdx.x; if (i >= n) return;
  float a = b2f(dn[i]); float sg = rb(a / (1.f + __expf(-a)));
  out[i] = f2b(b2f(x[i]) + rb(b2f(sv[i]) * sg));
}

extern "C" void kernel_launch(void* const* d_in, const int* in_sizes, int n_in, void* d_out, int out_size, void* d_ws, size_t ws_size, hipStream_t stream) {
  const u16* x = (const u16*)d_in[0]; const u16* mem0 = (const u16*)d_in[1];
  const u16* Wq = (const u16*)d_in[2]; const u16* Wk = (const u16*)d_in[3]; const u16* Wv = (const u16*)d_in[4]; const u16* Wo = (const u16*)d_in[5];
  const u16* wg = (const u16*)d_in[8]; const u16* wu = (const u16*)d_in[9]; const u16* wd = (const u16*)d_in[10];
  u16* ws = (u16*)d_ws;
  u16* xn = ws; u16* W1 = xn + S * D; u16* KV = W1 + (size_t)D * NW; u16* KVm = KV + (size_t)S * NW; u16* mem = KVm + M * NW;
  u16* sv = mem + 65 * M * D; u16* g = sv + S * D; u16* u = g + S * F; u16* hg = u + S * F; u16* dn = hg + S * F;
  ln_k<<<S, 64, 0, stream>>>(x, xn);
  gemm_k<<<dim3(D / 32, D / 32, H), 256, 0, stream>>>(Wk, Wq, W1, D, D, E, H * E, 1, 1, H * E, NW, E, E, D, 0.0625f);
  gemm_k<<<dim3(D / 32, D / 32, H), 256, 0, stream>>>(Wv, Wo, W1 + H * D, D, D, E, H * E, 1, D, 1, NW, E, E * D, D, 1.0f);
  gemm_k<<<dim3(NW / 32, S / 32, 1), 256, 0, stream>>>(xn, W1, KV, S, NW, D, D, 1, NW, 1, NW, 0, 0, 0, 1.0f);
  for (int s = 0; s < S / SEG; ++s) {
    const u16* m = s == 0 ? mem0 : mem + s * M * D;
    gemm_k<<<dim3(NW / 32, 1, 1), 256, 0, stream>>>(m, W1, KVm, M, NW, D, D, 1, NW, 1, NW, 0, 0, 0, 1.0f);
    attn_k<<<40, 256, 0, stream>>>(xn, KV, m, KVm, mem + (s + 1) * M * D, sv, s);
  }
  gemm_k<<<dim3(F / 32, S / 32, 1), 256, 0, stream>>>(xn, wg, g, S, F, D, D, 1, F, 1, F, 0, 0, 0, 1.0f);
  gemm_k<<<dim3(F / 32, S / 32, 1), 256, 0, stream>>>(xn, wu, u, S, F, D, D, 1, F, 1, F, 0, 0, 0, 1.0f);
  glu_k<<<S * F / 256, 256, 0, stream>>>(g, u, hg, S * F);
  gemm_k<<<dim3(D / 32, S / 32, 1), 256, 0, stream>>>(hg, wd, dn, S, D, F, F, 1, D, 1, D, 0, 0, 0, 1.0f);
  fin_k<<<S * D / 256, 256, 0, stream>>>(x, sv, dn, (u16*)d_out, S * D);
}
```

```cpp
#include <hip/hip_runtime.h>

typedef unsigned short u16;
typedef unsigned v4u __attribute__((ext_vector_type(4)));
typedef unsigned v2u __attribute__((ext_vector_type(2)));
typedef float f32x4 __attribute__((ext_vector_type(4)));
typedef float f32x2 __attribute__((ext_vector_type(2)));
typedef __bf16 bf16x8 __attribute__((ext_vector_type(8)));
typedef __bf16 bf16x2 __attribute__((ext_vector_type(2)));
typedef short s16x4 __attribute__((ext_vector_type(4)));
typedef __amdgpu_buffer_rsrc_t rsrc_t;
#define LAS __attribute__((address_space(3)))

constexpr int S = 2048, D = 448, H = 16, E = 256, M = 8, SEG = 32, F = 1792, NSEG = 64;
constexpr unsigned W_RING = 0;
constexpr unsigned W_FLAG = 2u << 20;
constexpr unsigned W_DIAG = W_FLAG + 65536;
constexpr unsigned W_XF   = 3u << 20;
constexpr unsigned W_WF   = W_XF + 64 * 28 * 1024;
constexpr unsigned ex(unsigned o) { return ((o >> 13) << 14) | (o & 8191); }
constexpr unsigned W_KQF  = 32u << 20;
constexpr unsigned W_VOF  = W_KQF + (1u << 20);
constexpr unsigned W_HF   = 160u << 20;
constexpr unsigned W_PF   = W_HF + ex(112 * 1024);
constexpr unsigned W_KQM  = W_HF + ex(160 * 1024);
constexpr unsigned W_MBF  = W_HF + ex(288 * 1024);
constexpr unsigned W_END  = 224u << 20;
__device__ __forceinline__ unsigned seg1(int s, unsigned far) { return ((unsigned)s << 21) + (((s & 1) << 13) ^ far); }
__device__ __forceinline__ unsigned seg2(int s, unsigned far) { return ((unsigned)s << 20) + (((s & 2) << 12) ^ far); }
__device__ __forceinline__ unsigned seg_out(int s, unsigned far) { return seg2(s, s + 1 < NSEG ? far ^ 0x2000 : far); }
constexpr unsigned OOB = 0x40000000u;
constexpr int FL_CHAIN = 0, FL_KQ = 64, FL_VO = 92, FL_H = 120, FL_P = 148, FL_PROBE = 164, FL_CTL = 228, FL_SCH = 229;
constexpr int L_MEM = 0, L_TR = 7680, L_RED = 9728, L_TAB = 10752;

__device__ __forceinline__ unsigned ringA(int tier, unsigned first, unsigned o) {
  const unsigned n = first + (o >> 12);
  return W_RING + (n >> 3) * 524288 + ((n >> 1) & 3) * 32768 + ((2 * tier + (n & 1)) << 12) + (o & 4095);
}
constexpr unsigned P_MEM = 0, P_SMM = 2, P_SMT = 4, P_MB = 6;
__device__ __forceinline__ unsigned memA(unsigned o) { return ringA(!((o >> 10) & 1), P_MEM, (o >> 11) * 2048 + ((o >> 7) & 7) * 256 + (o & 127)); }
__device__ __forceinline__ unsigned scoA(unsigned first, unsigned o) { return ringA((o >> 9) & 1, first, (o >> 10) * 512 + (o & 511)); }
__device__ __forceinline__ unsigned mbA(unsigned o) { return ringA((o >> 12) & 1, P_MB, (o >> 13) * 4096 + (o & 4095)); }

__device__ __forceinline__ void chain_setup_a(unsigned* lds, int q) {
  const int tid = threadIdx.x, lane = tid & 63, w = tid >> 6, g = lane >> 4, c = lane & 15;
  const int x = q >> 5, l = q & 31, h = q >> 2, lc = q & 3;
  const bool hasB = l < 28;
  const int ct = hasB ? l : 0, hb = 8 * x + 2 * w;
  const int own0 = w < 3 ? 2 * w : 6, own1 = w < 3 ? 2 * w + 1 : 0;
  const int i8 = c & 7;
  const int key = (c & 8) | ((c & 2) << 1) | ((c & 4) >> 1) | (c & 1);
  const unsigned oS = q * 256 + (i8 >> 2) * 128 + (i8 & 1) * 16 + g * 32 + ((((i8 >> 1) ^ lc) & 1) * 8);
  const unsigned oT = (2 * h + (lc & 1)) * 512 + (g >> 1) * 256 + (g & 1) * 64 + c * 4;
  const unsigned oB = ct * 4096 + hb * 256;
  unsigned* t = lds + L_TAB / 4 + tid;
  int r = 0;
  auto put = [&](unsigned val) { t[256 * r++] = val; };
  put(memA(own0 * 1024 + lane * 16)); put(memA(own1 * 1024 + lane * 16));
  put(lane * 16);
  put(L_TR + (w * 16 + c) * 40 + g * 8);
  put(L_TR + (8 * g + (c >> 2)) * 40 + (c & 3) * 8);
  put((lane & 2) ? 0xEEEEu : 0x4444u);
  put(hasB ? mbA(oB + lane * 4) : OOB);
  put(hasB && lane < 32 ? W_MBF + ex(oB + lane * 16) : OOB);

  if (w == 0) {
    put(c < 8 ? scoA(P_SMM, oS) : OOB); put(scoA(P_SMM, q * 256 + lane * 4));
    put(L_MEM + 7 * lc * 256 + (4 * (i8 >> 1) + (i8 & 1)) * 16 + g * 4);
  } else if (w == 2) {
    put(W_KQM + ex(q * 2048 + lane * 16)); put(0); put(lane == 0 ? W_FLAG + (FL_CHAIN + q) * 256 : OOB);
  } else if (w == 3) {
    put(lc < 2 ? scoA(P_SMT, oT) : OOB); put(0); put(0);
    put(lc < 2 ? W_KQF + (2 * h + lc) * 32768 + (g * 16 + key) * 16 : OOB);
    put(W_FLAG + (FL_KQ + min((14 * h) / 8 + (lane & 3), (14 * h + 13) / 8)) * 256);
  }
  for (int i = 7168 / 4 + tid; i < L_TAB / 4; i += 256) lds[i] = 0;
  __syncthreads();
}

__device__ __forceinline__ void chain_setup_b(unsigned* lds, int y, int l, const unsigned* mem0) {
  const int tid = threadIdx.x, lane = tid & 63, w = tid >> 6, g = lane >> 4, c = lane & 15;
  const int b = 28 * y + l, ct = b >> 1, rh = b & 1;
  const int i4 = (lane >> 2) & 3, vq = lane & 3;
  const int d32 = lane & 31, oi4 = ((d32 >> 4) << 1) | ((d32 >> 2) & 1), op = ((d32 & 3) << 1) | ((d32 >> 3) & 1);
  const unsigned aMW = lane < 32 ? memA(ct * 256 + rh * 128 + d32 * 4) : OOB;
  unsigned* t = lds + L_TAB / 4 + tid;
  int r = 0;
  auto put = [&](unsigned val) { t[256 * r++] = val; };
  for (int j = 0; j < 2; ++j) put(scoA(P_SMM, j * 512 + (4 * w + g) * 1024 + (i4 >> 1) * 256 + rh * 128 + (i4 & 1) * 16 + vq * 32));
  for (int j = 0; j < 2; ++j) put(scoA(P_SMT, j * 512 + (4 * w + g) * 1024 + rh * 256 + (i4 & 1) * 128 + (i4 >> 1) * 64 + vq * 16));
  put(mbA(ct * 4096 + w * 1024 + g * 256 + c * 4));
  put(W_VOF + ex((ct * 16 + 4 * w + (g >> 1)) * 1024) + ((g & 1) * 16 + c) * 16);
  put(L_RED + (w >> 1) * 512 + (g * 8 + (c >> 1)) * 16 + (w & 1) * 8 + (c & 1) * 4);
  put(W_FLAG + (FL_VO + ct) * 256);
  put((lane & 1) ? 0xEEEEu : 0x4444u);

  put(aMW);
  put(L_RED + (oi4 * 8 + op) * 16);
  if (tid < 2) lds[7424 / 4 + tid] = 0;
  put(mem0[((4 * rh + oi4) * D + 16 * ct) / 2 + op]);
  __syncthreads();
}


__device__ __forceinline__ rsrc_t mk(const void* p, unsigned bytes) { return __builtin_amdgcn_make_buffer_rsrc((void*)p, 0, bytes, 0x00020000); }
__device__ __forceinline__ v4u ld4(rsrc_t r, unsigned off) { return __builtin_amdgcn_raw_buffer_load_b128(r, off, 0, 0); }
__device__ __forceinline__ v4u ld4c(rsrc_t r, unsigned off) { return __builtin_amdgcn_raw_buffer_load_b128(r, off, 0, 16); }
__device__ __forceinline__ void st4c(rsrc_t r, unsigned off, v4u v) { __builtin_amdgcn_raw_buffer_store_b128(v, r, off, 0, 16); }
__device__ __forceinline__ void st1c(rsrc_t r, unsigned off, unsigned v) { __builtin_amdgcn_raw_buffer_store_b32(v, r, off, 0, 16); }
__device__ __forceinline__ f32x4 mma(v4u a, v4u b, f32x4 c) {
  return __builtin_amdgcn_mfma_f32_16x16x32_bf16(__builtin_bit_cast(bf16x8, a), __builtin_bit_cast(bf16x8, b), c, 0, 0, 0);
}
__device__ __forceinline__ unsigned pk2(float a, float b) { return __builtin_bit_cast(unsigned, __builtin_convertvector((f32x2){a, b}, bf16x2)); }
__device__ __forceinline__ v4u pk8(f32x4 a, f32x4 b) { return (v4u){pk2(a[0], a[1]), pk2(a[2], a[3]), pk2(b[0], b[1]), pk2(b[2], b[3])}; }
__device__ __forceinline__ float lo16(unsigned u) { return __uint_as_float(u << 16); }
__device__ __forceinline__ float hi16(unsigned u) { return __uint_as_float(u & 0xffff0000u); }
__device__ __forceinline__ float rb(float f) { return lo16(pk2(f, f)); }
__device__ __forceinline__ float silu(float a) { return a * __builtin_amdgcn_rcpf(1.f + __builtin_amdgcn_exp2f(-1.44269504f * a)); }
__device__ __forceinline__ void drain() { asm volatile("s_waitcnt vmcnt(0)" ::: "memory"); }
__device__ __forceinline__ void bar() { asm volatile("s_waitcnt lgkmcnt(0)\n\ts_barrier" ::: "memory"); }
__device__ __forceinline__ void dma(rsrc_t r, unsigned dst, unsigned off) {
  unsigned keep;
  asm volatile("s_mov_b32 %0, m0\n\ts_mov_b32 m0, %2\n\ts_nop 0\n\tbuffer_load_dwordx4 %1, %3, 0 offen lds\n\ts_mov_b32 m0, %0"
               : "=&s"(keep) : "v"(off), "s"(dst), "s"(r) : "memory");
}
__device__ __forceinline__ v4u lds4(unsigned a) { return *(LAS const v4u*)a; }
__device__ __forceinline__ v4u ldst(unsigned a, unsigned rows4) {
  const v2u p = __builtin_bit_cast(v2u, __builtin_amdgcn_ds_read_tr16_b64_v4i16((LAS s16x4*)a));
  const v2u q = __builtin_bit_cast(v2u, __builtin_amdgcn_ds_read_tr16_b64_v4i16((LAS s16x4*)(a + rows4)));
  return (v4u){p.x, p.y, q.x, q.y};
}
__device__ __forceinline__ v4u rows4(unsigned x) {
  const auto p = __builtin_amdgcn_permlane16_swap(x, x, false, false);
  const auto e = __builtin_amdgcn_permlane32_swap(p[0], p[0], false, false), o = __builtin_amdgcn_permlane32_swap(p[1], p[1], false, false);
  return (v4u){e[0], o[0], e[1], o[1]};
}
template <bool MAX>
__device__ __forceinline__ float over_rows(float x) {
  const auto p = __builtin_amdgcn_permlane16_swap(__float_as_uint(x), __float_as_uint(x), false, false);
  x = MAX ? fmaxf(__uint_as_float(p[0]), __uint_as_float(p[1])) : __uint_as_float(p[0]) + __uint_as_float(p[1]);
  const auto q = __builtin_amdgcn_permlane32_swap(__float_as_uint(x), __float_as_uint(x), false, false);
  return MAX ? fmaxf(__uint_as_float(q[0]), __uint_as_float(q[1])) : __uint_as_float(q[0]) + __uint_as_float(q[1]);
}
constexpr int HINT = 1 << 16;
__device__ __forceinline__ unsigned flagA(int idx) { return W_FLAG + (idx & (HINT - 1)) * 256 + (idx >> 16) * 128; }
__device__ __forceinline__ unsigned wait_flags(rsrc_t ws, int idx, unsigned need, int& budget) {
  unsigned v;
  for (;;) {
    asm volatile("" ::: "memory");
    v = __builtin_amdgcn_raw_buffer_load_b32(ws, flagA(idx), 0, 16);
    if (__builtin_amdgcn_ballot_w64(v < need) == 0 || budget <= 0) break;
    --budget;
    __builtin_amdgcn_s_sleep(1);
  }
  __builtin_amdgcn_fence(__ATOMIC_ACQUIRE, "wavefront");
  asm volatile("" ::: "memory");
  return v;
}
__device__ __forceinline__ unsigned far_half(rsrc_t ws, int& budget) {
  wait_flags(ws, FL_CTL, 1, budget);
  return ~(__builtin_amdgcn_readfirstlane(__builtin_amdgcn_raw_buffer_load_b32(ws, W_FLAG + FL_CTL * 256, 0, 16)) << 12) & 0x2000u;
}
constexpr int BUDGET = 100000;
__device__ __forceinline__ unsigned long long ws_base(const void* p) { return ((unsigned long long)p + 0x1fffffull) & ~0x1fffffull; }

struct Args {
  const u16 *x, *mem0, *wq, *wk, *wv, *wo, *wg, *wu, *wd;
  u16* out;
  unsigned char* ws;
};

__device__ __forceinline__ void warm_code(unsigned bytes) {
  unsigned long long pc;
  asm volatile("s_getpc_b64 %0" : "=s"(pc));
  asm volatile("buffer_load_dwordx4 a[252:255], %0, %1, 0 offen" :: "v"((blockIdx.x >> 3) * 4096 + threadIdx.x * 16), "s"(mk((const void*)(pc & ~63ull), bytes))
               : "a252", "a253", "a254", "a255");
}

constexpr int PRE_LN = 147456;
__device__ __forceinline__ void pre_block0(const Args& a, rsrc_t ws) {
  constexpr int mat = 0;
  const int tid = threadIdx.x, lane = tid & 63, w = __builtin_amdgcn_readfirstlane(tid >> 6), g = lane >> 4, c = lane & 15;
  const int blk = blockIdx.x, h = (blk & 7) * 2 + ((blk >> 6) & 1), mh = 16 * mat + h, kb = (blk >> 5) & 1, nb = (blk >> 3) & 3;
  const rsrc_t ra = mk(mat ? a.wv : a.wk, D * H * E * 2), rbw = mk(mat ? a.wo : a.wq, D * H * E * 2), rx = mk(a.x, S * D * 2);
  const int r8 = 2 * w + (lane >> 5), pp = lane & 31;
  const unsigned sw = (pp ^ r8) << 4, wb = w * 1024;
  const unsigned sa = ((224 * kb + 8 * (w >> 1) + (r8 & 3)) * H + h) * (E * 2) + sw;
  const unsigned sb = mat ? (h * E + 4 * w + g) * (D * 2) + nb * 224 + ((c ^ (2 * g + 8 * (w >> 1))) << 4) : ((112 * nb + r8) * H + h) * (E * 2) + sw;
  const unsigned xo = (blk * 8 + r8) * (D * 2) + pp * 16;
  v4u v0, v1;
  asm volatile("buffer_load_dwordx4 %[v0], %[x0], %[rx], 0 offen\n buffer_load_dwordx4 %[v1], %[x1], %[rx], 0 offen\n"
  "s_lshl_b32 s56, %[rk], 18\n"
  "s_add_u32 s57, s56, 262144\n"
  "s_cmp_eq_u32 s57, 1835008\n"
  "s_cselect_b32 s57, 0, s57\n"
  "s_lshl_b32 s41, %[rn], 15\n"
  "s_add_u32 s41, s41, %[wb]\n"
  "s_mul_i32 s42, %[rn], 524288\n"
  "s_add_u32 m0, s41, 0\n"
  "s_add_u32 s40, s42, 0\n"
  "buffer_load_dwordx4 %[sb0], %[rb], s40 offen lds\n"
  "s_add_u32 m0, s41, 4096\n"
  "s_add_u32 s40, s42, 65536\n"
  "buffer_load_dwordx4 %[sb1], %[rb], s40 offen lds\n"
  "s_add_u32 m0, s41, 8192\n"
  "s_add_u32 s40, s42, 131072\n"
  "buffer_load_dwordx4 %[sb0], %[rb], s40 offen lds\n"
  "s_add_u32 m0, s41, 12288\n"
  "s_add_u32 s40, s42, 196608\n"
  "buffer_load_dwordx4 %[sb1], %[rb], s40 offen lds\n"
  "s_add_u32 m0, s41, 16384\n"
  "s_add_u32 s40, s42, 262144\n"
  "buffer_load_dwordx4 %[sb0], %[rb], s40 offen lds\n"
  "s_add_u32 m0, s41, 20480\n"
  "s_add_u32 s40, s42, 327680\n"
  "buffer_load_dwordx4 %[sb1], %[rb], s40 offen lds\n"
  "s_mul_i32 s40, %[rn], 57344\n"
  "s_sub_u32 s41, s41, s40\n"
  "s_mul_i32 s40, %[rn], 917504\n"
  "s_sub_u32 s42, s42, s40\n"
  "s_add_u32 m0, %[wb], 65536\n"
  "s_add_u32 s40, s56, 0\n"
  "buffer_load_dwordx4 %[sa0], %[ra], s40 offen lds\n"
  "s_add_u32 m0, %[wb], 69632\n"
  "s_add_u32 s40, s56, 131072\n"
  "buffer_load_dwordx4 %[sa1], %[ra], s40 offen lds\n"
  "s_add_u32 m0, %[wb], 73728\n"
  "s_add_u32 s40, s56, 32768\n"
  "buffer_load_dwordx4 %[sa0], %[ra], s40 offen lds\n"
  "s_add_u32 m0, %[wb], 77824\n"
  "s_add_u32 s40, s56, 163840\n"
  "buffer_load_dwordx4 %[sa1], %[ra], s40 offen lds\n"
  "s_add_u32 m0, s41, 24576\n"
  "s_add_u32 s40, s42, 393216\n"
  "buffer_load_dwordx4 %[sb0], %[rb], s40 offen lds\n"
  "s_add_u32 m0, s41, 28672\n"
  "s_add_u32 s40, s42, 458752\n"
  "buffer_load_dwordx4 %[sb1], %[rb], s40 offen lds\n"
  "s_add_u32 m0, s41, 32768\n"
  "s_add_u32 s40, s42, 524288\n"
  "buffer_load_dwordx4 %[sb0], %[rb], s40 offen lds\n"
  "s_add_u32 m0, s41, 36864\n"
  "s_add_u32 s40, s42, 589824\n"
  "buffer_load_dwordx4 %[sb1], %[rb], s40 offen lds\n"
  "s_add_u32 m0, s41, 40960\n"
  "s_add_u32 s40, s42, 655360\n"
  "buffer_load_dwordx4 %[sb0], %[rb], s40 offen lds\n"
  "s_add_u32 m0, s41, 45056\n"
  "s_add_u32 s40, s42, 720896\n"
  "buffer_load_dwordx4 %[sb1], %[rb], s40 offen lds\n"
  "s_add_u32 m0, s41, 49152\n"
  "s_add_u32 s40, s42, 786432\n"
  "buffer_load_dwordx4 %[sb0], %[rb], s40 offen lds\n"
  "s_add_u32 m0, s41, 53248\n"
  "s_add_u32 s40, s42, 851968\n"
  "buffer_load_dwordx4 %[sb1], %[rb], s40 offen lds\n"
  "s_add_u32 m0, %[wb], 81920\n"
  "s_add_u32 s40, s57, 0\n"
  "buffer_load_dwordx4 %[sa0], %[ra], s40 offen lds\n"
  "s_add_u32 m0, %[wb], 86016\n"
  "s_add_u32 s40, s57, 131072\n"
  "buffer_load_dwordx4 %[sa1], %[ra], s40 offen lds\n"
  "s_add_u32 m0, %[wb], 90112\n"
  "s_add_u32 s40, s57, 32768\n"
  "buffer_load_dwordx4 %[sa0], %[ra], s40 offen lds\n"
  "s_add_u32 m0, %[wb], 94208\n"
  "s_add_u32 s40, s57, 163840\n"
  "buffer_load_dwordx4 %[sa1], %[ra], s40 offen lds\n"
  "s_waitcnt vmcnt(22)\n"
  : [v0] "=&v"(v0), [v1] "=&v"(v1)
  : [x0] "v"(xo), [x1] "v"(pp < 24 ? xo + 512 : OOB), [rx] "s"(rx), [ra] "s"(ra), [rb] "s"(rbw), [wb] "s"(wb),
    [rk] "s"(2 * nb), [rn] "s"(kb), [sa0] "v"(sa), [sa1] "v"(sa ^ 128), [sb0] "v"(sb), [sb1] "v"(sb ^ 128) : "s40", "s41", "s42", "s56", "s57", "s58", "scc", "memory");
  float f[16], s = 0.f;
#pragma unroll
  for (int e = 0; e < 4; ++e) { f[2 * e] = lo16(v0[e]); f[2 * e + 1] = hi16(v0[e]); f[8 + 2 * e] = lo16(v1[e]); f[9 + 2 * e] = hi16(v1[e]); }
#pragma unroll
  for (int e = 0; e < 16; ++e) s += f[e];
#pragma unroll
  for (int o = 1; o < 32; o <<= 1) s += __shfl_xor(s, o);
  const float mean = s / D;
  float q0 = 0.f, q1 = 0.f;
#pragma unroll
  for (int e = 0; e < 8; ++e) { q0 += (f[e] - mean) * (f[e] - mean); q1 += (f[8 + e] - mean) * (f[8 + e] - mean); }
  float q = pp < 24 ? q0 + q1 : q0;
#pragma unroll
  for (int o = 1; o < 32; o <<= 1) q += __shfl_xor(q, o);
  const float m = rb(mean), rden = 1.f / rb(sqrtf(rb(rb(q / D) + 1e-5f)));
#pragma unroll
  for (int e = 0; e < 4; ++e) {
    const unsigned d0 = pk2(f[2 * e] - m, f[2 * e + 1] - m), d1 = pk2(f[8 + 2 * e] - m, f[9 + 2 * e] - m);
    v0[e] = pk2(lo16(d0) * rden, hi16(d0) * rden); v1[e] = pk2(lo16(d1) * rden, hi16(d1) * rden);
  }
  *(LAS v4u*)(PRE_LN + (pp * 8 + r8) * 16) = v0;
  *(LAS v4u*)(PRE_LN + ((pp + 32) * 8 + r8) * 16) = v1;
  const unsigned xf = W_XF + (blk >> 1) * 14336 + (tid >> 3) * 256 + (blk & 1) * 128 + (tid & 7) * 16;
  asm volatile(
  "s_mov_b32 s42, 0x3db8aa3b\n"
  "s_lshl_b32 s56, %[rk], 18\n"
  "s_add_u32 s57, s56, 262144\n"
  "s_cmp_eq_u32 s57, 1835008\n"
  "s_cselect_b32 s57, 0, s57\n"
  "s_add_u32 s58, s57, 262144\n"
  "s_cmp_eq_u32 s58, 1835008\n"
  "s_cselect_b32 s58, 0, s58\n"
  "s_add_u32 s59, s58, 262144\n"
  "s_cmp_eq_u32 s59, 1835008\n"
  "s_cselect_b32 s59, 0, s59\n"
  "s_add_u32 s60, s59, 262144\n"
  "s_cmp_eq_u32 s60, 1835008\n"
  "s_cselect_b32 s60, 0, s60\n"
  "s_add_u32 s61, s60, 262144\n"
  "s_cmp_eq_u32 s61, 1835008\n"
  "s_cselect_b32 s61, 0, s61\n"
  "s_add_u32 s62, s61, 262144\n"
  "s_cmp_eq_u32 s62, 1835008\n"
  "s_cselect_b32 s62, 0, s62\n"
  "v_xor_b32 v176, 0, %[la]\n"
  "v_add_u32 v180, 0x10000, v176\n"
  "v_add_u32 v184, 0x20000, v176\n"
  "v_add_u32 v188, %[lb], v176\n"
  "v_xor_b32 v177, 64, %[la]\n"
  "v_add_u32 v181, 0x10000, v177\n"
  "v_add_u32 v185, 0x20000, v177\n"
  "v_add_u32 v189, %[lb], v177\n"
  "v_xor_b32 v178, 128, %[la]\n"
  "v_add_u32 v182, 0x10000, v178\n"
  "v_add_u32 v186, 0x20000, v178\n"
  "v_add_u32 v190, %[lb], v178\n"
  "v_xor_b32 v179, 192, %[la]\n"
  "v_add_u32 v183, 0x10000, v179\n"
  "v_add_u32 v187, 0x20000, v179\n"
  "v_add_u32 v191, %[lb], v179\n"
  "s_waitcnt vmcnt(4) lgkmcnt(0)\n"
  "s_barrier\n"
  "ds_read_b128 v[192:195], %[ll]\n"
  "ds_read_b128 v[196:199], %[ll] offset:4096\n"
  "ds_read_b128 v[0:3], v188 offset:0\n"
  "ds_read_b128 v[32:35], v188 offset:8192\n"
  "ds_read_b128 v[4:7], v189 offset:0\n"
  "ds_read_b128 v[36:39], v189 offset:8192\n"
  "s_waitcnt lgkmcnt(4)\n"
  "ds_read_b128 v[8:11], v190 offset:0\n"
  "ds_read_b128 v[40:43], v190 offset:8192\n"
  "ds_read_b128 v[12:15], v191 offset:0\n"
  "ds_read_b128 v[44:47], v191 offset:8192\n"
  "s_waitcnt lgkmcnt(4)\n"
  "ds_read_b128 v[16:19], v188 offset:256\n"
  "ds_read_b128 v[48:51], v188 offset:8448\n"
  "ds_read_b128 v[20:23], v189 offset:256\n"
  "ds_read_b128 v[52:55], v189 offset:8448\n"
  "s_waitcnt lgkmcnt(4)\n"
  "ds_read_b128 v[24:27], v190 offset:256\n"
  "ds_read_b128 v[56:59], v190 offset:8448\n"
  "ds_read_b128 v[28:31], v191 offset:256\n"
  "ds_read_b128 v[60:63], v191 offset:8448\n"
  "s_waitcnt lgkmcnt(4)\n"
  "s_cmp_eq_u32 %[wb], 3072\n"
  "s_cbranch_scc1 L3_%=\n"
  "ds_read_b128 v[96:99], v180 offset:0\n"
  "ds_read_b128 v[100:103], v180 offset:8192\n"
  "ds_read_b128 v[104:107], v181 offset:0\n"
  "ds_read_b128 v[108:111], v181 offset:8192\n"
  "s_waitcnt lgkmcnt(3)\n"
  "v_mfma_f32_16x16x32_bf16 v[64:67], v[96:99], v[0:3], 0\n"
  "ds_read_b128 v[112:115], v182 offset:0\n"
  "v_mfma_f32_16x16x32_bf16 v[72:75], v[96:99], v[32:35], 0\n"
  "s_waitcnt lgkmcnt(3)\n"
  "v_mfma_f32_16x16x32_bf16 v[68:71], v[100:103], v[0:3], 0\n"
  "ds_read_b128 v[116:119], v182 offset:8192\n"
  "v_mfma_f32_16x16x32_bf16 v[76:79], v[100:103], v[32:35], 0\n"
  "s_add_u32 m0, %[wb], 98304\n"
  "s_add_u32 s40, s58, 0\n"
  "buffer_load_dwordx4 %[sa0], %[ra], s40 offen lds\n"
  "s_waitcnt lgkmcnt(3)\n"
  "v_mfma_f32_16x16x32_bf16 v[64:67], v[104:107], v[4:7], v[64:67]\n"
  "ds_read_b128 v[96:99], v183 offset:0\n"
  "v_mfma_f32_16x16x32_bf16 v[72:75], v[104:107], v[36:39], v[72:75]\n"
  "s_waitcnt lgkmcnt(3)\n"
  "v_mfma_f32_16x16x32_bf16 v[68:71], v[108:111], v[4:7], v[68:71]\n"
  "ds_read_b128 v[100:103], v183 offset:8192\n"
  "v_mfma_f32_16x16x32_bf16 v[76:79], v[108:111], v[36:39], v[76:79]\n"
  "s_add_u32 m0, %[wb], 102400\n"
  "s_add_u32 s40, s58, 131072\n"
  "buffer_load_dwordx4 %[sa1], %[ra], s40 offen lds\n"
  "s_waitcnt lgkmcnt(3)\n"
  "v_mfma_f32_16x16x32_bf16 v[64:67], v[112:115], v[8:11], v[64:67]\n"
  "ds_read_b128 v[104:107], v180 offset:256\n"
  "v_mfma_f32_16x16x32_bf16 v[72:75], v[112:115], v[40:43], v[72:75]\n"
  "s_waitcnt lgkmcnt(3)\n"
  "v_mfma_f32_16x16x32_bf16 v[68:71], v[116:119], v[8:11], v[68:71]\n"
  "ds_read_b128 v[108:111], v180 offset:8448\n"
  "v_mfma_f32_16x16x32_bf16 v[76:79], v[116:119], v[40:43], v[76:79]\n"
  "s_add_u32 m0, %[wb], 106496\n"
  "s_add_u32 s40, s58, 32768\n"
  "buffer_load_dwordx4 %[sa0], %[ra], s40 offen lds\n"
  "s_waitcnt lgkmcnt(3)\n"
  "v_mfma_f32_16x16x32_bf16 v[64:67], v[96:99], v[12:15], v[64:67]\n"
  "ds_read_b128 v[112:115], v181 offset:256\n"
  "v_mfma_f32_16x16x32_bf16 v[72:75], v[96:99], v[44:47], v[72:75]\n"
  "s_waitcnt lgkmcnt(3)\n"
  "v_mfma_f32_16x16x32_bf16 v[68:71], v[100:103], v[12:15], v[68:71]\n"
  "ds_read_b128 v[116:119], v181 offset:8448\n"
  "v_mfma_f32_16x16x32_bf16 v[76:79], v[100:103], v[44:47], v[76:79]\n"
  "s_add_u32 m0, %[wb], 110592\n"
  "s_add_u32 s40, s58, 163840\n"
  "buffer_load_dwordx4 %[sa1], %[ra], s40 offen lds\n"
  "s_waitcnt lgkmcnt(3)\n"
  "v_mfma_f32_16x16x32_bf16 v[64:67], v[104:107], v[16:19], v[64:67]\n"
  "ds_read_b128 v[96:99], v182 offset:256\n"
  "v_mfma_f32_16x16x32_bf16 v[72:75], v[104:107], v[48:51], v[72:75]\n"
  "s_waitcnt lgkmcnt(3)\n"
  "v_mfma_f32_16x16x32_bf16 v[68:71], v[108:111], v[16:19], v[68:71]\n"
  "ds_read_b128 v[100:103], v182 offset:8448\n"
  "v_mfma_f32_16x16x32_bf16 v[76:79], v[108:111], v[48:51], v[76:79]\n"
  "s_add_u32 m0, %[wb], 114688\n"
  "s_add_u32 s40, s59, 0\n"
  "buffer_load_dwordx4 %[sa0], %[ra], s40 offen lds\n"
  "s_waitcnt lgkmcnt(3)\n"
  "v_mfma_f32_16x16x32_bf16 v[64:67], v[112:115], v[20:23], v[64:67]\n"
  "ds_read_b128 v[104:107], v183 offset:256\n"
  "v_mfma_f32_16x16x32_bf16 v[72:75], v[112:115], v[52:55], v[72:75]\n"
  "s_waitcnt lgkmcnt(3)\n"
  "v_mfma_f32_16x16x32_bf16 v[68:71], v[116:119], v[20:23], v[68:71]\n"
  "ds_read_b128 v[108:111], v183 offset:8448\n"
  "v_mfma_f32_16x16x32_bf16 v[76:79], v[116:119], v[52:55], v[76:79]\n"
  "s_add_u32 m0, %[wb], 118784\n"
  "s_add_u32 s40, s59, 131072\n"
  "buffer_load_dwordx4 %[sa1], %[ra], s40 offen lds\n"
  "s_waitcnt vmcnt(6)\n"
  "s_barrier\n"
  "s_waitcnt lgkmcnt(3)\n"
  "v_mfma_f32_16x16x32_bf16 v[64:67], v[96:99], v[24:27], v[64:67]\n"
  "ds_read_b128 v[112:115], v180 offset:16384\n"
  "v_mfma_f32_16x16x32_bf16 v[72:75], v[96:99], v[56:59], v[72:75]\n"
  "s_waitcnt lgkmcnt(3)\n"
  "v_mfma_f32_16x16x32_bf16 v[68:71], v[100:103], v[24:27], v[68:71]\n"
  "ds_read_b128 v[116:119], v180 offset:24576\n"
  "v_mfma_f32_16x16x32_bf16 v[76:79], v[100:103], v[56:59], v[76:79]\n"
  "s_add_u32 m0, %[wb], 122880\n"
  "s_add_u32 s40, s59, 32768\n"
  "buffer_load_dwordx4 %[sa0], %[ra], s40 offen lds\n"
  "s_waitcnt lgkmcnt(3)\n"
  "v_mfma_f32_16x16x32_bf16 v[64:67], v[104:107], v[28:31], v[64:67]\n"
  "ds_read_b128 v[96:99], v181 offset:16384\n"
  "v_mfma_f32_16x16x32_bf16 v[72:75], v[104:107], v[60:63], v[72:75]\n"
  "s_waitcnt lgkmcnt(3)\n"
  "v_mfma_f32_16x16x32_bf16 v[68:71], v[108:111], v[28:31], v[68:71]\n"
  "ds_read_b128 v[100:103], v181 offset:24576\n"
  "v_mfma_f32_16x16x32_bf16 v[76:79], v[108:111], v[60:63], v[76:79]\n"
  "s_add_u32 m0, %[wb], 126976\n"
  "s_add_u32 s40, s59, 163840\n"
  "buffer_load_dwordx4 %[sa1], %[ra], s40 offen lds\n"
  "s_waitcnt lgkmcnt(3)\n"
  "v_mfma_f32_16x16x32_bf16 v[80:83], v[112:115], v[0:3], 0\n"
  "ds_read_b128 v[104:107], v182 offset:16384\n"
  "v_mfma_f32_16x16x32_bf16 v[88:91], v[112:115], v[32:35], 0\n"
  "s_waitcnt lgkmcnt(3)\n"
  "v_mfma_f32_16x16x32_bf16 v[84:87], v[116:119], v[0:3], 0\n"
  "ds_read_b128 v[108:111], v182 offset:24576\n"
  "v_mfma_f32_16x16x32_bf16 v[92:95], v[116:119], v[32:35], 0\n"
  "v_mul_f32 v64, s42, v64\n"
  "v_mul_f32 v65, s42, v65\n"
  "s_add_u32 m0, %[wb], 131072\n"
  "s_add_u32 s40, s60, 0\n"
  "buffer_load_dwordx4 %[sa0], %[ra], s40 offen lds\n"
  "s_waitcnt lgkmcnt(3)\n"
  "v_mfma_f32_16x16x32_bf16 v[80:83], v[96:99], v[4:7], v[80:83]\n"
  "ds_read_b128 v[112:115], v183 offset:16384\n"
  "v_cvt_pk_bf16_f32 v120, v64, v65\n"
  "v_mul_f32 v66, s42, v66\n"
  "v_mfma_f32_16x16x32_bf16 v[88:91], v[96:99], v[36:39], v[88:91]\n"
  "v_mul_f32 v67, s42, v67\n"
  "v_cvt_pk_bf16_f32 v121, v66, v67\n"
  "s_waitcnt lgkmcnt(3)\n"
  "v_mfma_f32_16x16x32_bf16 v[84:87], v[100:103], v[4:7], v[84:87]\n"
  "ds_read_b128 v[116:119], v183 offset:24576\n"
  "v_mul_f32 v68, s42, v68\n"
  "v_mul_f32 v69, s42, v69\n"
  "v_mfma_f32_16x16x32_bf16 v[92:95], v[100:103], v[36:39], v[92:95]\n"
  "v_cvt_pk_bf16_f32 v122, v68, v69\n"
  "v_mul_f32 v70, s42, v70\n"
  "s_add_u32 m0, %[wb], 135168\n"
  "s_add_u32 s40, s60, 131072\n"
  "buffer_load_dwordx4 %[sa1], %[ra], s40 offen lds\n"
  "s_waitcnt lgkmcnt(3)\n"
  "v_mfma_f32_16x16x32_bf16 v[80:83], v[104:107], v[8:11], v[80:83]\n"
  "ds_read_b128 v[96:99], v180 offset:16640\n"
  "v_mul_f32 v71, s42, v71\n"
  "v_cvt_pk_bf16_f32 v123, v70, v71\n"
  "v_mfma_f32_16x16x32_bf16 v[88:91], v[104:107], v[40:43], v[88:91]\n"
  "v_mul_f32 v72, s42, v72\n"
  "v_mul_f32 v73, s42, v73\n"
  "s_waitcnt lgkmcnt(3)\n"
  "v_mfma_f32_16x16x32_bf16 v[84:87], v[108:111], v[8:11], v[84:87]\n"
  "ds_read_b128 v[100:103], v180 offset:24832\n"
  "v_cvt_pk_bf16_f32 v124, v72, v73\n"
  "v_mul_f32 v74, s42, v74\n"
  "v_mfma_f32_16x16x32_bf16 v[92:95], v[108:111], v[40:43], v[92:95]\n"
  "v_mul_f32 v75, s42, v75\n"
  "v_cvt_pk_bf16_f32 v125, v74, v75\n"
  "s_add_u32 m0, %[wb], 139264\n"
  "s_add_u32 s40, s60, 32768\n"
  "buffer_load_dwordx4 %[sa0], %[ra], s40 offen lds\n"
  "s_waitcnt lgkmcnt(3)\n"
  "v_mfma_f32_16x16x32_bf16 v[80:83], v[112:115], v[12:15], v[80:83]\n"
  "ds_read_b128 v[104:107], v181 offset:16640\n"
  "v_mul_f32 v76, s42, v76\n"
  "v_mul_f32 v77, s42, v77\n"
  "v_mfma_f32_16x16x32_bf16 v[88:91], v[112:115], v[44:47], v[88:91]\n"
  "v_cvt_pk_bf16_f32 v126, v76, v77\n"
  "v_mul_f32 v78, s42, v78\n"
  "s_waitcnt lgkmcnt(3)\n"
  "v_mfma_f32_16x16x32_bf16 v[84:87], v[116:119], v[12:15], v[84:87]\n"
  "ds_read_b128 v[108:111], v181 offset:24832\n"
  "v_mul_f32 v79, s42, v79\n"
  "v_cvt_pk_bf16_f32 v127, v78, v79\n"
  "v_mfma_f32_16x16x32_bf16 v[92:95], v[116:119], v[44:47], v[92:95]\n"
  "s_add_u32 m0, %[wb], 143360\n"
  "s_add_u32 s40, s60, 163840\n"
  "buffer_load_dwordx4 %[sa1], %[ra], s40 offen lds\n"
  "s_waitcnt lgkmcnt(3)\n"
  "v_mfma_f32_16x16x32_bf16 v[80:83], v[96:99], v[16:19], v[80:83]\n"
  "ds_read_b128 v[112:115], v182 offset:16640\n"
  "v_mfma_f32_16x16x32_bf16 v[88:91], v[96:99], v[48:51], v[88:91]\n"
  "s_waitcnt lgkmcnt(3)\n"
  "v_mfma_f32_16x16x32_bf16 v[84:87], v[100:103], v[16:19], v[84:87]\n"
  "ds_read_b128 v[116:119], v182 offset:24832\n"
  "v_mfma_f32_16x16x32_bf16 v[92:95], v[100:103], v[48:51], v[92:95]\n"
  "s_add_u32 m0, %[wb], 0\n"
  "s_add_u32 s40, s61, 0\n"
  "buffer_load_dwordx4 %[sa0], %[ra], s40 offen lds\n"
  "s_waitcnt lgkmcnt(3)\n"
  "v_mfma_f32_16x16x32_bf16 v[80:83], v[104:107], v[20:23], v[80:83]\n"
  "ds_read_b128 v[96:99], v183 offset:16640\n"
  "v_mfma_f32_16x16x32_bf16 v[88:91], v[104:107], v[52:55], v[88:91]\n"
  "s_waitcnt lgkmcnt(3)\n"
  "v_mfma_f32_16x16x32_bf16 v[84:87], v[108:111], v[20:23], v[84:87]\n"
  "ds_read_b128 v[100:103], v183 offset:24832\n"
  "v_mfma_f32_16x16x32_bf16 v[92:95], v[108:111], v[52:55], v[92:95]\n"
  "s_add_u32 m0, %[wb], 4096\n"
  "s_add_u32 s40, s61, 131072\n"
  "buffer_load_dwordx4 %[sa1], %[ra], s40 offen lds\n"
  "s_waitcnt vmcnt(10)\n"
  "s_barrier\n"
  "s_waitcnt lgkmcnt(3)\n"
  "v_mfma_f32_16x16x32_bf16 v[80:83], v[112:115], v[24:27], v[80:83]\n"
  "ds_read_b128 v[104:107], v180 offset:32768\n"
  "v_mfma_f32_16x16x32_bf16 v[88:91], v[112:115], v[56:59], v[88:91]\n"
  "s_waitcnt lgkmcnt(3)\n"
  "v_mfma_f32_16x16x32_bf16 v[84:87], v[116:119], v[24:27], v[84:87]\n"
  "ds_read_b128 v[108:111], v180 offset:40960\n"
  "v_mfma_f32_16x16x32_bf16 v[92:95], v[116:119], v[56:59], v[92:95]\n"
  "s_add_u32 m0, %[wb], 8192\n"
  "s_add_u32 s40, s61, 32768\n"
  "buffer_load_dwordx4 %[sa0], %[ra], s40 offen lds\n"
  "s_waitcnt lgkmcnt(3)\n"
  "v_mfma_f32_16x16x32_bf16 v[80:83], v[96:99], v[28:31], v[80:83]\n"
  "ds_read_b128 v[112:115], v181 offset:32768\n"
  "v_mfma_f32_16x16x32_bf16 v[88:91], v[96:99], v[60:63], v[88:91]\n"
  "s_waitcnt lgkmcnt(3)\n"
  "v_mfma_f32_16x16x32_bf16 v[84:87], v[100:103], v[28:31], v[84:87]\n"
  "ds_read_b128 v[116:119], v181 offset:40960\n"
  "v_mfma_f32_16x16x32_bf16 v[92:95], v[100:103], v[60:63], v[92:95]\n"
  "s_add_u32 m0, %[wb], 12288\n"
  "s_add_u32 s40, s61, 163840\n"
  "buffer_load_dwordx4 %[sa1], %[ra], s40 offen lds\n"
  "s_waitcnt lgkmcnt(3)\n"
  "v_mfma_f32_16x16x32_bf16 v[64:67], v[104:107], v[0:3], 0\n"
  "ds_read_b128 v[96:99], v182 offset:32768\n"
  "v_mfma_f32_16x16x32_bf16 v[72:75], v[104:107], v[32:35], 0\n"
  "s_waitcnt lgkmcnt(3)\n"
  "v_mfma_f32_16x16x32_bf16 v[68:71], v[108:111], v[0:3], 0\n"
  "ds_read_b128 v[100:103], v182 offset:40960\n"
  "v_mfma_f32_16x16x32_bf16 v[76:79], v[108:111], v[32:35], 0\n"
  "v_mul_f32 v80, s42, v80\n"
  "v_mul_f32 v81, s42, v81\n"
  "s_add_u32 m0, %[wb], 16384\n"
  "s_add_u32 s40, s62, 0\n"
  "buffer_load_dwordx4 %[sa0], %[ra], s40 offen lds\n"
  "s_waitcnt lgkmcnt(3)\n"
  "v_mfma_f32_16x16x32_bf16 v[64:67], v[112:115], v[4:7], v[64:67]\n"
  "ds_read_b128 v[104:107], v183 offset:32768\n"
  "v_cvt_pk_bf16_f32 v128, v80, v81\n"
  "v_mul_f32 v82, s42, v82\n"
  "v_mfma_f32_16x16x32_bf16 v[72:75], v[112:115], v[36:39], v[72:75]\n"
  "v_mul_f32 v83, s42, v83\n"
  "v_cvt_pk_bf16_f32 v129, v82, v83\n"
  "s_waitcnt lgkmcnt(3)\n"
  "v_mfma_f32_16x16x32_bf16 v[68:71], v[116:119], v[4:7], v[68:71]\n"
  "ds_read_b128 v[108:111], v183 offset:40960\n"
  "v_mul_f32 v84, s42, v84\n"
  "v_mul_f32 v85, s42, v85\n"
  "v_mfma_f32_16x16x32_bf16 v[76:79], v[116:119], v[36:39], v[76:79]\n"
  "v_cvt_pk_bf16_f32 v130, v84, v85\n"
  "v_mul_f32 v86, s42, v86\n"
  "s_add_u32 m0, %[wb], 20480\n"
  "s_add_u32 s40, s62, 131072\n"
  "buffer_load_dwordx4 %[sa1], %[ra], s40 offen lds\n"
  "s_waitcnt lgkmcnt(3)\n"
  "v_mfma_f32_16x16x32_bf16 v[64:67], v[96:99], v[8:11], v[64:67]\n"
  "ds_read_b128 v[112:115], v180 offset:33024\n"
  "v_mul_f32 v87, s42, v87\n"
  "v_cvt_pk_bf16_f32 v131, v86, v87\n"
  "v_mfma_f32_16x16x32_bf16 v[72:75], v[96:99], v[40:43], v[72:75]\n"
  "v_mul_f32 v88, s42, v88\n"
  "v_mul_f32 v89, s42, v89\n"
  "s_waitcnt lgkmcnt(3)\n"
  "v_mfma_f32_16x16x32_bf16 v[68:71], v[100:103], v[8:11], v[68:71]\n"
  "ds_read_b128 v[116:119], v180 offset:41216\n"
  "v_cvt_pk_bf16_f32 v132, v88, v89\n"
  "v_mul_f32 v90, s42, v90\n"
  "v_mfma_f32_16x16x32_bf16 v[76:79], v[100:103], v[40:43], v[76:79]\n"
  "v_mul_f32 v91, s42, v91\n"
  "v_cvt_pk_bf16_f32 v133, v90, v91\n"
  "s_add_u32 m0, %[wb], 24576\n"
  "s_add_u32 s40, s62, 32768\n"
  "buffer_load_dwordx4 %[sa0], %[ra], s40 offen lds\n"
  "s_waitcnt lgkmcnt(3)\n"
  "v_mfma_f32_16x16x32_bf16 v[64:67], v[104:107], v[12:15], v[64:67]\n"
  "ds_read_b128 v[96:99], v181 offset:33024\n"
  "v_mul_f32 v92, s42, v92\n"
  "v_mul_f32 v93, s42, v93\n"
  "v_mfma_f32_16x16x32_bf16 v[72:75], v[104:107], v[44:47], v[72:75]\n"
  "v_cvt_pk_bf16_f32 v134, v92, v93\n"
  "v_mul_f32 v94, s42, v94\n"
  "s_waitcnt lgkmcnt(3)\n"
  "v_mfma_f32_16x16x32_bf16 v[68:71], v[108:111], v[12:15], v[68:71]\n"
  "ds_read_b128 v[100:103], v181 offset:41216\n"
  "v_mul_f32 v95, s42, v95\n"
  "v_cvt_pk_bf16_f32 v135, v94, v95\n"
  "v_mfma_f32_16x16x32_bf16 v[76:79], v[108:111], v[44:47], v[76:79]\n"
  "s_add_u32 m0, %[wb], 28672\n"
  "s_add_u32 s40, s62, 163840\n"
  "buffer_load_dwordx4 %[sa1], %[ra], s40 offen lds\n"
  "s_waitcnt lgkmcnt(3)\n"
  "v_mfma_f32_16x16x32_bf16 v[64:67], v[112:115], v[16:19], v[64:67]\n"
  "ds_read_b128 v[104:107], v182 offset:33024\n"
  "v_mfma_f32_16x16x32_bf16 v[72:75], v[112:115], v[48:51], v[72:75]\n"
  "s_waitcnt lgkmcnt(3)\n"
  "v_mfma_f32_16x16x32_bf16 v[68:71], v[116:119], v[16:19], v[68:71]\n"
  "ds_read_b128 v[108:111], v182 offset:41216\n"
  "v_mfma_f32_16x16x32_bf16 v[76:79], v[116:119], v[48:51], v[76:79]\n"
  "buffer_store_dwordx4 v[192:195], %[x0], %[ws], 0 offen sc1\n"
  "s_waitcnt lgkmcnt(3)\n"
  "v_mfma_f32_16x16x32_bf16 v[64:67], v[96:99], v[20:23], v[64:67]\n"
  "ds_read_b128 v[112:115], v183 offset:33024\n"
  "v_mfma_f32_16x16x32_bf16 v[72:75], v[96:99], v[52:55], v[72:75]\n"
  "s_waitcnt lgkmcnt(3)\n"
  "v_mfma_f32_16x16x32_bf16 v[68:71], v[100:103], v[20:23], v[68:71]\n"
  "ds_read_b128 v[116:119], v183 offset:41216\n"
  "v_mfma_f32_16x16x32_bf16 v[76:79], v[100:103], v[52:55], v[76:79]\n"
  "buffer_store_dwordx4 v[196:199], %[x1], %[ws], 0 offen sc1\n"
  "s_waitcnt vmcnt(14)\n"
  "s_barrier\n"
  "s_waitcnt lgkmcnt(3)\n"
  "v_mfma_f32_16x16x32_bf16 v[64:67], v[104:107], v[24:27], v[64:67]\n"
  "ds_read_b128 v[96:99], v180 offset:49152\n"
  "v_mfma_f32_16x16x32_bf16 v[72:75], v[104:107], v[56:59], v[72:75]\n"
  "s_waitcnt lgkmcnt(3)\n"
  "v_mfma_f32_16x16x32_bf16 v[68:71], v[108:111], v[24:27], v[68:71]\n"
  "ds_read_b128 v[100:103], v180 offset:57344\n"
  "v_mfma_f32_16x16x32_bf16 v[76:79], v[108:111], v[56:59], v[76:79]\n"
  "s_lshr_b32 s40, s56, 8\n"
  "buffer_store_dwordx4 v[120:123], %[o0], %[ws], s40 offen sc1\n"
  "s_waitcnt lgkmcnt(3)\n"
  "v_mfma_f32_16x16x32_bf16 v[64:67], v[112:115], v[28:31], v[64:67]\n"
  "ds_read_b128 v[104:107], v181 offset:49152\n"
  "v_mfma_f32_16x16x32_bf16 v[72:75], v[112:115], v[60:63], v[72:75]\n"
  "s_waitcnt lgkmcnt(3)\n"
  "v_mfma_f32_16x16x32_bf16 v[68:71], v[116:119], v[28:31], v[68:71]\n"
  "ds_read_b128 v[108:111], v181 offset:57344\n"
  "v_mfma_f32_16x16x32_bf16 v[76:79], v[116:119], v[60:63], v[76:79]\n"
  "s_lshr_b32 s40, s56, 8\n"
  "s_add_u32 s40, s40, 14336\n"
  "buffer_store_dwordx4 v[124:127], %[o0], %[ws], s40 offen sc1\n"
  "s_waitcnt lgkmcnt(3)\n"
  "v_mfma_f32_16x16x32_bf16 v[80:83], v[96:99], v[0:3], 0\n"
  "ds_read_b128 v[112:115], v182 offset:49152\n"
  "v_mfma_f32_16x16x32_bf16 v[88:91], v[96:99], v[32:35], 0\n"
  "s_waitcnt lgkmcnt(3)\n"
  "v_mfma_f32_16x16x32_bf16 v[84:87], v[100:103], v[0:3], 0\n"
  "ds_read_b128 v[116:119], v182 offset:57344\n"
  "v_mfma_f32_16x16x32_bf16 v[92:95], v[100:103], v[32:35], 0\n"
  "v_mul_f32 v64, s42, v64\n"
  "v_mul_f32 v65, s42, v65\n"
  "s_lshr_b32 s40, s57, 8\n"
  "buffer_store_dwordx4 v[128:131], %[o0], %[ws], s40 offen sc1\n"
  "s_waitcnt lgkmcnt(3)\n"
  "v_mfma_f32_16x16x32_bf16 v[80:83], v[104:107], v[4:7], v[80:83]\n"
  "ds_read_b128 v[96:99], v183 offset:49152\n"
  "v_cvt_pk_bf16_f32 v136, v64, v65\n"
  "v_mul_f32 v66, s42, v66\n"
  "v_mfma_f32_16x16x32_bf16 v[88:91], v[104:107], v[36:39], v[88:91]\n"
  "v_mul_f32 v67, s42, v67\n"
  "v_cvt_pk_bf16_f32 v137, v66, v67\n"
  "s_waitcnt lgkmcnt(3)\n"
  "v_mfma_f32_16x16x32_bf16 v[84:87], v[108:111], v[4:7], v[84:87]\n"
  "ds_read_b128 v[100:103], v183 offset:57344\n"
  "v_mul_f32 v68, s42, v68\n"
  "v_mul_f32 v69, s42, v69\n"
  "v_mfma_f32_16x16x32_bf16 v[92:95], v[108:111], v[36:39], v[92:95]\n"
  "v_cvt_pk_bf16_f32 v138, v68, v69\n"
  "v_mul_f32 v70, s42, v70\n"
  "s_lshr_b32 s40, s57, 8\n"
  "s_add_u32 s40, s40, 14336\n"
  "buffer_store_dwordx4 v[132:135], %[o0], %[ws], s40 offen sc1\n"
  "s_waitcnt lgkmcnt(3)\n"
  "v_mfma_f32_16x16x32_bf16 v[80:83], v[112:115], v[8:11], v[80:83]\n"
  "ds_read_b128 v[104:107], v180 offset:49408\n"
  "v_mul_f32 v71, s42, v71\n"
  "v_cvt_pk_bf16_f32 v139, v70, v71\n"
  "v_mfma_f32_16x16x32_bf16 v[88:91], v[112:115], v[40:43], v[88:91]\n"
  "v_mul_f32 v72, s42, v72\n"
  "v_mul_f32 v73, s42, v73\n"
  "s_waitcnt lgkmcnt(3)\n"
  "v_mfma_f32_16x16x32_bf16 v[84:87], v[116:119], v[8:11], v[84:87]\n"
  "ds_read_b128 v[108:111], v180 offset:57600\n"
  "v_cvt_pk_bf16_f32 v140, v72, v73\n"
  "v_mul_f32 v74, s42, v74\n"
  "v_mfma_f32_16x16x32_bf16 v[92:95], v[116:119], v[40:43], v[92:95]\n"
  "v_mul_f32 v75, s42, v75\n"
  "v_cvt_pk_bf16_f32 v141, v74, v75\n"
  "s_waitcnt lgkmcnt(3)\n"
  "v_mfma_f32_16x16x32_bf16 v[80:83], v[96:99], v[12:15], v[80:83]\n"
  "ds_read_b128 v[112:115], v181 offset:49408\n"
  "v_mul_f32 v76, s42, v76\n"
  "v_mul_f32 v77, s42, v77\n"
  "v_mfma_f32_16x16x32_bf16 v[88:91], v[96:99], v[44:47], v[88:91]\n"
  "v_cvt_pk_bf16_f32 v142, v76, v77\n"
  "v_mul_f32 v78, s42, v78\n"
  "s_waitcnt lgkmcnt(3)\n"
  "v_mfma_f32_16x16x32_bf16 v[84:87], v[100:103], v[12:15], v[84:87]\n"
  "ds_read_b128 v[116:119], v181 offset:57600\n"
  "v_mul_f32 v79, s42, v79\n"
  "v_cvt_pk_bf16_f32 v143, v78, v79\n"
  "v_mfma_f32_16x16x32_bf16 v[92:95], v[100:103], v[44:47], v[92:95]\n"
  "s_waitcnt lgkmcnt(3)\n"
  "v_mfma_f32_16x16x32_bf16 v[80:83], v[104:107], v[16:19], v[80:83]\n"
  "ds_read_b128 v[96:99], v182 offset:49408\n"
  "v_mfma_f32_16x16x32_bf16 v[88:91], v[104:107], v[48:51], v[88:91]\n"
  "s_waitcnt lgkmcnt(3)\n"
  "v_mfma_f32_16x16x32_bf16 v[84:87], v[108:111], v[16:19], v[84:87]\n"
  "ds_read_b128 v[100:103], v182 offset:57600\n"
  "v_mfma_f32_16x16x32_bf16 v[92:95], v[108:111], v[48:51], v[92:95]\n"
  "s_lshr_b32 s40, s58, 8\n"
  "buffer_store_dwordx4 v[136:139], %[o0], %[ws], s40 offen sc1\n"
  "s_waitcnt lgkmcnt(3)\n"
  "v_mfma_f32_16x16x32_bf16 v[80:83], v[112:115], v[20:23], v[80:83]\n"
  "ds_read_b128 v[104:107], v183 offset:49408\n"
  "v_mfma_f32_16x16x32_bf16 v[88:91], v[112:115], v[52:55], v[88:91]\n"
  "s_waitcnt lgkmcnt(3)\n"
  "v_mfma_f32_16x16x32_bf16 v[84:87], v[116:119], v[20:23], v[84:87]\n"
  "ds_read_b128 v[108:111], v183 offset:57600\n"
  "v_mfma_f32_16x16x32_bf16 v[92:95], v[116:119], v[52:55], v[92:95]\n"
  "s_lshr_b32 s40, s58, 8\n"
  "s_add_u32 s40, s40, 14336\n"
  "buffer_store_dwordx4 v[140:143], %[o0], %[ws], s40 offen sc1\n"
  "s_waitcnt vmcnt(16)\n"
  "s_barrier\n"
  "s_waitcnt lgkmcnt(3)\n"
  "v_mfma_f32_16x16x32_bf16 v[80:83], v[96:99], v[24:27], v[80:83]\n"
  "ds_read_b128 v[112:115], v184 offset:0\n"
  "v_mfma_f32_16x16x32_bf16 v[88:91], v[96:99], v[56:59], v[88:91]\n"
  "s_waitcnt lgkmcnt(3)\n"
  "v_mfma_f32_16x16x32_bf16 v[84:87], v[100:103], v[24:27], v[84:87]\n"
  "ds_read_b128 v[116:119], v184 offset:8192\n"
  "v_mfma_f32_16x16x32_bf16 v[92:95], v[100:103], v[56:59], v[92:95]\n"
  "s_waitcnt lgkmcnt(3)\n"
  "v_mfma_f32_16x16x32_bf16 v[80:83], v[104:107], v[28:31], v[80:83]\n"
  "ds_read_b128 v[96:99], v185 offset:0\n"
  "v_mfma_f32_16x16x32_bf16 v[88:91], v[104:107], v[60:63], v[88:91]\n"
  "s_waitcnt lgkmcnt(3)\n"
  "v_mfma_f32_16x16x32_bf16 v[84:87], v[108:111], v[28:31], v[84:87]\n"
  "ds_read_b128 v[100:103], v185 offset:8192\n"
  "v_mfma_f32_16x16x32_bf16 v[92:95], v[108:111], v[60:63], v[92:95]\n"
  "s_waitcnt lgkmcnt(3)\n"
  "v_mfma_f32_16x16x32_bf16 v[64:67], v[112:115], v[0:3], 0\n"
  "ds_read_b128 v[104:107], v186 offset:0\n"
  "v_mfma_f32_16x16x32_bf16 v[72:75], v[112:115], v[32:35], 0\n"
  "s_waitcnt lgkmcnt(3)\n"
  "v_mfma_f32_16x16x32_bf16 v[68:71], v[116:119], v[0:3], 0\n"
  "ds_read_b128 v[108:111], v186 offset:8192\n"
  "v_mfma_f32_16x16x32_bf16 v[76:79], v[116:119], v[32:35], 0\n"
  "v_mul_f32 v80, s42, v80\n"
  "v_mul_f32 v81, s42, v81\n"
  "s_waitcnt lgkmcnt(3)\n"
  "v_mfma_f32_16x16x32_bf16 v[64:67], v[96:99], v[4:7], v[64:67]\n"
  "ds_read_b128 v[112:115], v187 offset:0\n"
  "v_cvt_pk_bf16_f32 v144, v80, v81\n"
  "v_mul_f32 v82, s42, v82\n"
  "v_mfma_f32_16x16x32_bf16 v[72:75], v[96:99], v[36:39], v[72:75]\n"
  "v_mul_f32 v83, s42, v83\n"
  "v_cvt_pk_bf16_f32 v145, v82, v83\n"
  "s_waitcnt lgkmcnt(3)\n"
  "v_mfma_f32_16x16x32_bf16 v[68:71], v[100:103], v[4:7], v[68:71]\n"
  "ds_read_b128 v[116:119], v187 offset:8192\n"
  "v_mul_f32 v84, s42, v84\n"
  "v_mul_f32 v85, s42, v85\n"
  "v_mfma_f32_16x16x32_bf16 v[76:79], v[100:103], v[36:39], v[76:79]\n"
  "v_cvt_pk_bf16_f32 v146, v84, v85\n"
  "v_mul_f32 v86, s42, v86\n"
  "s_waitcnt lgkmcnt(3)\n"
  "v_mfma_f32_16x16x32_bf16 v[64:67], v[104:107], v[8:11], v[64:67]\n"
  "ds_read_b128 v[96:99], v184 offset:256\n"
  "v_mul_f32 v87, s42, v87\n"
  "v_cvt_pk_bf16_f32 v147, v86, v87\n"
  "v_mfma_f32_16x16x32_bf16 v[72:75], v[104:107], v[40:43], v[72:75]\n"
  "v_mul_f32 v88, s42, v88\n"
  "v_mul_f32 v89, s42, v89\n"
  "s_waitcnt lgkmcnt(3)\n"
  "v_mfma_f32_16x16x32_bf16 v[68:71], v[108:111], v[8:11], v[68:71]\n"
  "ds_read_b128 v[100:103], v184 offset:8448\n"
  "v_cvt_pk_bf16_f32 v148, v88, v89\n"
  "v_mul_f32 v90, s42, v90\n"
  "v_mfma_f32_16x16x32_bf16 v[76:79], v[108:111], v[40:43], v[76:79]\n"
  "v_mul_f32 v91, s42, v91\n"
  "v_cvt_pk_bf16_f32 v149, v90, v91\n"
  "s_waitcnt lgkmcnt(3)\n"
  "v_mfma_f32_16x16x32_bf16 v[64:67], v[112:115], v[12:15], v[64:67]\n"
  "ds_read_b128 v[104:107], v185 offset:256\n"
  "v_mul_f32 v92, s42, v92\n"
  "v_mul_f32 v93, s42, v93\n"
  "v_mfma_f32_16x16x32_bf16 v[72:75], v[112:115], v[44:47], v[72:75]\n"
  "v_cvt_pk_bf16_f32 v150, v92, v93\n"
  "v_mul_f32 v94, s42, v94\n"
  "s_waitcnt lgkmcnt(3)\n"
  "v_mfma_f32_16x16x32_bf16 v[68:71], v[116:119], v[12:15], v[68:71]\n"
  "ds_read_b128 v[108:111], v185 offset:8448\n"
  "v_mul_f32 v95, s42, v95\n"
  "v_cvt_pk_bf16_f32 v151, v94, v95\n"
  "v_mfma_f32_16x16x32_bf16 v[76:79], v[116:119], v[44:47], v[76:79]\n"
  "s_waitcnt lgkmcnt(3)\n"
  "v_mfma_f32_16x16x32_bf16 v[64:67], v[96:99], v[16:19], v[64:67]\n"
  "ds_read_b128 v[112:115], v186 offset:256\n"
  "v_mfma_f32_16x16x32_bf16 v[72:75], v[96:99], v[48:51], v[72:75]\n"
  "s_waitcnt lgkmcnt(3)\n"
  "v_mfma_f32_16x16x32_bf16 v[68:71], v[100:103], v[16:19], v[68:71]\n"
  "ds_read_b128 v[116:119], v186 offset:8448\n"
  "v_mfma_f32_16x16x32_bf16 v[76:79], v[100:103], v[48:51], v[76:79]\n"
  "s_lshr_b32 s40, s59, 8\n"
  "buffer_store_dwordx4 v[144:147], %[o0], %[ws], s40 offen sc1\n"
  "s_waitcnt lgkmcnt(3)\n"
  "v_mfma_f32_16x16x32_bf16 v[64:67], v[104:107], v[20:23], v[64:67]\n"
  "ds_read_b128 v[96:99], v187 offset:256\n"
  "v_mfma_f32_16x16x32_bf16 v[72:75], v[104:107], v[52:55], v[72:75]\n"
  "s_waitcnt lgkmcnt(3)\n"
  "v_mfma_f32_16x16x32_bf16 v[68:71], v[108:111], v[20:23], v[68:71]\n"
  "ds_read_b128 v[100:103], v187 offset:8448\n"
  "v_mfma_f32_16x16x32_bf16 v[76:79], v[108:111], v[52:55], v[76:79]\n"
  "s_lshr_b32 s40, s59, 8\n"
  "s_add_u32 s40, s40, 14336\n"
  "buffer_store_dwordx4 v[148:151], %[o0], %[ws], s40 offen sc1\n"
  "s_waitcnt vmcnt(14)\n"
  "s_barrier\n"
  "s_waitcnt lgkmcnt(3)\n"
  "v_mfma_f32_16x16x32_bf16 v[64:67], v[112:115], v[24:27], v[64:67]\n"
  "ds_read_b128 v[104:107], v176 offset:0\n"
  "v_mfma_f32_16x16x32_bf16 v[72:75], v[112:115], v[56:59], v[72:75]\n"
  "s_waitcnt lgkmcnt(3)\n"
  "v_mfma_f32_16x16x32_bf16 v[68:71], v[116:119], v[24:27], v[68:71]\n"
  "ds_read_b128 v[108:111], v176 offset:8192\n"
  "v_mfma_f32_16x16x32_bf16 v[76:79], v[116:119], v[56:59], v[76:79]\n"
  "s_waitcnt lgkmcnt(3)\n"
  "v_mfma_f32_16x16x32_bf16 v[64:67], v[96:99], v[28:31], v[64:67]\n"
  "ds_read_b128 v[112:115], v177 offset:0\n"
  "v_mfma_f32_16x16x32_bf16 v[72:75], v[96:99], v[60:63], v[72:75]\n"
  "s_waitcnt lgkmcnt(3)\n"
  "v_mfma_f32_16x16x32_bf16 v[68:71], v[100:103], v[28:31], v[68:71]\n"
  "ds_read_b128 v[116:119], v177 offset:8192\n"
  "v_mfma_f32_16x16x32_bf16 v[76:79], v[100:103], v[60:63], v[76:79]\n"
  "s_waitcnt lgkmcnt(3)\n"
  "v_mfma_f32_16x16x32_bf16 v[80:83], v[104:107], v[0:3], 0\n"
  "ds_read_b128 v[96:99], v178 offset:0\n"
  "v_mfma_f32_16x16x32_bf16 v[88:91], v[104:107], v[32:35], 0\n"
  "s_waitcnt lgkmcnt(3)\n"
  "v_mfma_f32_16x16x32_bf16 v[84:87], v[108:111], v[0:3], 0\n"
  "ds_read_b128 v[100:103], v178 offset:8192\n"
  "v_mfma_f32_16x16x32_bf16 v[92:95], v[108:111], v[32:35], 0\n"
  "v_mul_f32 v64, s42, v64\n"
  "v_mul_f32 v65, s42, v65\n"
  "s_waitcnt lgkmcnt(3)\n"
  "v_mfma_f32_16x16x32_bf16 v[80:83], v[112:115], v[4:7], v[80:83]\n"
  "ds_read_b128 v[104:107], v179 offset:0\n"
  "v_cvt_pk_bf16_f32 v152, v64, v65\n"
  "v_mul_f32 v66, s42, v66\n"
  "v_mfma_f32_16x16x32_bf16 v[88:91], v[112:115], v[36:39], v[88:91]\n"
  "v_mul_f32 v67, s42, v67\n"
  "v_cvt_pk_bf16_f32 v153, v66, v67\n"
  "s_waitcnt lgkmcnt(3)\n"
  "v_mfma_f32_16x16x32_bf16 v[84:87], v[116:119], v[4:7], v[84:87]\n"
  "ds_read_b128 v[108:111], v179 offset:8192\n"
  "v_mul_f32 v68, s42, v68\n"
  "v_mul_f32 v69, s42, v69\n"
  "v_mfma_f32_16x16x32_bf16 v[92:95], v[116:119], v[36:39], v[92:95]\n"
  "v_cvt_pk_bf16_f32 v154, v68, v69\n"
  "v_mul_f32 v70, s42, v70\n"
  "s_waitcnt lgkmcnt(3)\n"
  "v_mfma_f32_16x16x32_bf16 v[80:83], v[96:99], v[8:11], v[80:83]\n"
  "ds_read_b128 v[112:115], v176 offset:256\n"
  "v_mul_f32 v71, s42, v71\n"
  "v_cvt_pk_bf16_f32 v155, v70, v71\n"
  "v_mfma_f32_16x16x32_bf16 v[88:91], v[96:99], v[40:43], v[88:91]\n"
  "v_mul_f32 v72, s42, v72\n"
  "v_mul_f32 v73, s42, v73\n"
  "s_waitcnt lgkmcnt(3)\n"
  "v_mfma_f32_16x16x32_bf16 v[84:87], v[100:103], v[8:11], v[84:87]\n"
  "ds_read_b128 v[116:119], v176 offset:8448\n"
  "v_cvt_pk_bf16_f32 v156, v72, v73\n"
  "v_mul_f32 v74, s42, v74\n"
  "v_mfma_f32_16x16x32_bf16 v[92:95], v[100:103], v[40:43], v[92:95]\n"
  "v_mul_f32 v75, s42, v75\n"
  "v_cvt_pk_bf16_f32 v157, v74, v75\n"
  "s_waitcnt lgkmcnt(3)\n"
  "v_mfma_f32_16x16x32_bf16 v[80:83], v[104:107], v[12:15], v[80:83]\n"
  "ds_read_b128 v[96:99], v177 offset:256\n"
  "v_mul_f32 v76, s42, v76\n"
  "v_mul_f32 v77, s42, v77\n"
  "v_mfma_f32_16x16x32_bf16 v[88:91], v[104:107], v[44:47], v[88:91]\n"
  "v_cvt_pk_bf16_f32 v158, v76, v77\n"
  "v_mul_f32 v78, s42, v78\n"
  "s_waitcnt lgkmcnt(3)\n"
  "v_mfma_f32_16x16x32_bf16 v[84:87], v[108:111], v[12:15], v[84:87]\n"
  "ds_read_b128 v[100:103], v177 offset:8448\n"
  "v_mul_f32 v79, s42, v79\n"
  "v_cvt_pk_bf16_f32 v159, v78, v79\n"
  "v_mfma_f32_16x16x32_bf16 v[92:95], v[108:111], v[44:47], v[92:95]\n"
  "s_waitcnt lgkmcnt(3)\n"
  "v_mfma_f32_16x16x32_bf16 v[80:83], v[112:115], v[16:19], v[80:83]\n"
  "ds_read_b128 v[104:107], v178 offset:256\n"
  "v_mfma_f32_16x16x32_bf16 v[88:91], v[112:115], v[48:51], v[88:91]\n"
  "s_waitcnt lgkmcnt(3)\n"
  "v_mfma_f32_16x16x32_bf16 v[84:87], v[116:119], v[16:19], v[84:87]\n"
  "ds_read_b128 v[108:111], v178 offset:8448\n"
  "v_mfma_f32_16x16x32_bf16 v[92:95], v[116:119], v[48:51], v[92:95]\n"
  "s_lshr_b32 s40, s60, 8\n"
  "buffer_store_dwordx4 v[152:155], %[o0], %[ws], s40 offen sc1\n"
  "s_waitcnt lgkmcnt(3)\n"
  "v_mfma_f32_16x16x32_bf16 v[80:83], v[96:99], v[20:23], v[80:83]\n"
  "ds_read_b128 v[112:115], v179 offset:256\n"
  "v_mfma_f32_16x16x32_bf16 v[88:91], v[96:99], v[52:55], v[88:91]\n"
  "s_waitcnt lgkmcnt(3)\n"
  "v_mfma_f32_16x16x32_bf16 v[84:87], v[100:103], v[20:23], v[84:87]\n"
  "ds_read_b128 v[116:119], v179 offset:8448\n"
  "v_mfma_f32_16x16x32_bf16 v[92:95], v[100:103], v[52:55], v[92:95]\n"
  "s_lshr_b32 s40, s60, 8\n"
  "s_add_u32 s40, s40, 14336\n"
  "buffer_store_dwordx4 v[156:159], %[o0], %[ws], s40 offen sc1\n"
  "s_waitcnt vmcnt(12)\n"
  "s_barrier\n"
  "s_waitcnt lgkmcnt(3)\n"
  "v_mfma_f32_16x16x32_bf16 v[80:83], v[104:107], v[24:27], v[80:83]\n"
  "ds_read_b128 v[96:99], v176 offset:16384\n"
  "v_mfma_f32_16x16x32_bf16 v[88:91], v[104:107], v[56:59], v[88:91]\n"
  "s_waitcnt lgkmcnt(3)\n"
  "v_mfma_f32_16x16x32_bf16 v[84:87], v[108:111], v[24:27], v[84:87]\n"
  "ds_read_b128 v[100:103], v176 offset:24576\n"
  "v_mfma_f32_16x16x32_bf16 v[92:95], v[108:111], v[56:59], v[92:95]\n"
  "s_waitcnt lgkmcnt(3)\n"
  "v_mfma_f32_16x16x32_bf16 v[80:83], v[112:115], v[28:31], v[80:83]\n"
  "ds_read_b128 v[104:107], v177 offset:16384\n"
  "v_mfma_f32_16x16x32_bf16 v[88:91], v[112:115], v[60:63], v[88:91]\n"
  "s_waitcnt lgkmcnt(3)\n"
  "v_mfma_f32_16x16x32_bf16 v[84:87], v[116:119], v[28:31], v[84:87]\n"
  "ds_read_b128 v[108:111], v177 offset:24576\n"
  "v_mfma_f32_16x16x32_bf16 v[92:95], v[116:119], v[60:63], v[92:95]\n"
  "s_waitcnt lgkmcnt(3)\n"
  "v_mfma_f32_16x16x32_bf16 v[64:67], v[96:99], v[0:3], 0\n"
  "ds_read_b128 v[112:115], v178 offset:16384\n"
  "v_mfma_f32_16x16x32_bf16 v[72:75], v[96:99], v[32:35], 0\n"
  "s_waitcnt lgkmcnt(3)\n"
  "v_mfma_f32_16x16x32_bf16 v[68:71], v[100:103], v[0:3], 0\n"
  "ds_read_b128 v[116:119], v178 offset:24576\n"
  "v_mfma_f32_16x16x32_bf16 v[76:79], v[100:103], v[32:35], 0\n"
  "v_mul_f32 v80, s42, v80\n"
  "v_mul_f32 v81, s42, v81\n"
  "s_waitcnt lgkmcnt(3)\n"
  "v_mfma_f32_16x16x32_bf16 v[64:67], v[104:107], v[4:7], v[64:67]\n"
  "ds_read_b128 v[96:99], v179 offset:16384\n"
  "v_cvt_pk_bf16_f32 v160, v80, v81\n"
  "v_mul_f32 v82, s42, v82\n"
  "v_mfma_f32_16x16x32_bf16 v[72:75], v[104:107], v[36:39], v[72:75]\n"
  "v_mul_f32 v83, s42, v83\n"
  "v_cvt_pk_bf16_f32 v161, v82, v83\n"
  "s_waitcnt lgkmcnt(3)\n"
  "v_mfma_f32_16x16x32_bf16 v[68:71], v[108:111], v[4:7], v[68:71]\n"
  "ds_read_b128 v[100:103], v179 offset:24576\n"
  "v_mul_f32 v84, s42, v84\n"
  "v_mul_f32 v85, s42, v85\n"
  "v_mfma_f32_16x16x32_bf16 v[76:79], v[108:111], v[36:39], v[76:79]\n"
  "v_cvt_pk_bf16_f32 v162, v84, v85\n"
  "v_mul_f32 v86, s42, v86\n"
  "s_waitcnt lgkmcnt(3)\n"
  "v_mfma_f32_16x16x32_bf16 v[64:67], v[112:115], v[8:11], v[64:67]\n"
  "ds_read_b128 v[104:107], v176 offset:16640\n"
  "v_mul_f32 v87, s42, v87\n"
  "v_cvt_pk_bf16_f32 v163, v86, v87\n"
  "v_mfma_f32_16x16x32_bf16 v[72:75], v[112:115], v[40:43], v[72:75]\n"
  "v_mul_f32 v88, s42, v88\n"
  "v_mul_f32 v89, s42, v89\n"
  "s_waitcnt lgkmcnt(3)\n"
  "v_mfma_f32_16x16x32_bf16 v[68:71], v[116:119], v[8:11], v[68:71]\n"
  "ds_read_b128 v[108:111], v176 offset:24832\n"
  "v_cvt_pk_bf16_f32 v164, v88, v89\n"
  "v_mul_f32 v90, s42, v90\n"
  "v_mfma_f32_16x16x32_bf16 v[76:79], v[116:119], v[40:43], v[76:79]\n"
  "v_mul_f32 v91, s42, v91\n"
  "v_cvt_pk_bf16_f32 v165, v90, v91\n"
  "s_waitcnt lgkmcnt(3)\n"
  "v_mfma_f32_16x16x32_bf16 v[64:67], v[96:99], v[12:15], v[64:67]\n"
  "ds_read_b128 v[112:115], v177 offset:16640\n"
  "v_mul_f32 v92, s42, v92\n"
  "v_mul_f32 v93, s42, v93\n"
  "v_mfma_f32_16x16x32_bf16 v[72:75], v[96:99], v[44:47], v[72:75]\n"
  "v_cvt_pk_bf16_f32 v166, v92, v93\n"
  "v_mul_f32 v94, s42, v94\n"
  "s_waitcnt lgkmcnt(3)\n"
  "v_mfma_f32_16x16x32_bf16 v[68:71], v[100:103], v[12:15], v[68:71]\n"
  "ds_read_b128 v[116:119], v177 offset:24832\n"
  "v_mul_f32 v95, s42, v95\n"
  "v_cvt_pk_bf16_f32 v167, v94, v95\n"
  "v_mfma_f32_16x16x32_bf16 v[76:79], v[100:103], v[44:47], v[76:79]\n"
  "s_waitcnt lgkmcnt(3)\n"
  "v_mfma_f32_16x16x32_bf16 v[64:67], v[104:107], v[16:19], v[64:67]\n"
  "ds_read_b128 v[96:99], v178 offset:16640\n"
  "v_mfma_f32_16x16x32_bf16 v[72:75], v[104:107], v[48:51], v[72:75]\n"
  "s_waitcnt lgkmcnt(3)\n"
  "v_mfma_f32_16x16x32_bf16 v[68:71], v[108:111], v[16:19], v[68:71]\n"
  "ds_read_b128 v[100:103], v178 offset:24832\n"
  "v_mfma_f32_16x16x32_bf16 v[76:79], v[108:111], v[48:51], v[76:79]\n"
  "s_lshr_b32 s40, s61, 8\n"
  "buffer_store_dwordx4 v[160:163], %[o0], %[ws], s40 offen sc1\n"
  "s_waitcnt lgkmcnt(3)\n"
  "v_mfma_f32_16x16x32_bf16 v[64:67], v[112:115], v[20:23], v[64:67]\n"
  "ds_read_b128 v[104:107], v179 offset:16640\n"
  "v_mfma_f32_16x16x32_bf16 v[72:75], v[112:115], v[52:55], v[72:75]\n"
  "s_waitcnt lgkmcnt(3)\n"
  "v_mfma_f32_16x16x32_bf16 v[68:71], v[116:119], v[20:23], v[68:71]\n"
  "ds_read_b128 v[108:111], v179 offset:24832\n"
  "v_mfma_f32_16x16x32_bf16 v[76:79], v[116:119], v[52:55], v[76:79]\n"
  "s_lshr_b32 s40, s61, 8\n"
  "s_add_u32 s40, s40, 14336\n"
  "buffer_store_dwordx4 v[164:167], %[o0], %[ws], s40 offen sc1\n"
  "s_waitcnt lgkmcnt(3)\n"
  "v_mfma_f32_16x16x32_bf16 v[64:67], v[96:99], v[24:27], v[64:67]\n"
  "v_mfma_f32_16x16x32_bf16 v[72:75], v[96:99], v[56:59], v[72:75]\n"
  "s_waitcnt lgkmcnt(2)\n"
  "v_mfma_f32_16x16x32_bf16 v[68:71], v[100:103], v[24:27], v[68:71]\n"
  "v_mfma_f32_16x16x32_bf16 v[76:79], v[100:103], v[56:59], v[76:79]\n"
  "s_waitcnt lgkmcnt(1)\n"
  "v_mfma_f32_16x16x32_bf16 v[64:67], v[104:107], v[28:31], v[64:67]\n"
  "v_mfma_f32_16x16x32_bf16 v[72:75], v[104:107], v[60:63], v[72:75]\n"
  "s_waitcnt lgkmcnt(0)\n"
  "v_mfma_f32_16x16x32_bf16 v[68:71], v[108:111], v[28:31], v[68:71]\n"
  "v_mfma_f32_16x16x32_bf16 v[76:79], v[108:111], v[60:63], v[76:79]\n"
  "s_nop 7\n"
  "v_mul_f32 v64, s42, v64\n"
  "v_mul_f32 v65, s42, v65\n"
  "v_cvt_pk_bf16_f32 v168, v64, v65\n"
  "v_mul_f32 v66, s42, v66\n"
  "v_mul_f32 v67, s42, v67\n"
  "v_cvt_pk_bf16_f32 v169, v66, v67\n"
  "v_mul_f32 v68, s42, v68\n"
  "v_mul_f32 v69, s42, v69\n"
  "v_cvt_pk_bf16_f32 v170, v68, v69\n"
  "v_mul_f32 v70, s42, v70\n"
  "v_mul_f32 v71, s42, v71\n"
  "v_cvt_pk_bf16_f32 v171, v70, v71\n"
  "v_mul_f32 v72, s42, v72\n"
  "v_mul_f32 v73, s42, v73\n"
  "v_cvt_pk_bf16_f32 v172, v72, v73\n"
  "v_mul_f32 v74, s42, v74\n"
  "v_mul_f32 v75, s42, v75\n"
  "v_cvt_pk_bf16_f32 v173, v74, v75\n"
  "v_mul_f32 v76, s42, v76\n"
  "v_mul_f32 v77, s42, v77\n"
  "v_cvt_pk_bf16_f32 v174, v76, v77\n"
  "v_mul_f32 v78, s42, v78\n"
  "v_mul_f32 v79, s42, v79\n"
  "v_cvt_pk_bf16_f32 v175, v78, v79\n"
  "s_lshr_b32 s40, s62, 8\n"
  "buffer_store_dwordx4 v[168:171], %[o0], %[ws], s40 offen sc1\n"
  "s_lshr_b32 s40, s62, 8\n"
  "s_add_u32 s40, s40, 14336\n"
  "buffer_store_dwordx4 v[172:175], %[o0], %[ws], s40 offen sc1\n"
  "s_branch Le_%=\n"
  "L3_%=:\n"
  "ds_read_b128 v[96:99], v180 offset:0\n"
  "ds_read_b128 v[100:103], v180 offset:8192\n"
  "ds_read_b128 v[104:107], v181 offset:0\n"
  "ds_read_b128 v[108:111], v181 offset:8192\n"
  "s_waitcnt lgkmcnt(3)\n"
  "v_mfma_f32_16x16x32_bf16 v[64:67], v[96:99], v[0:3], 0\n"
  "ds_read_b128 v[112:115], v182 offset:0\n"
  "s_waitcnt lgkmcnt(3)\n"
  "v_mfma_f32_16x16x32_bf16 v[68:71], v[100:103], v[0:3], 0\n"
  "ds_read_b128 v[116:119], v182 offset:8192\n"
  "s_add_u32 m0, %[wb], 98304\n"
  "s_add_u32 s40, s58, 0\n"
  "buffer_load_dwordx4 %[sa0], %[ra], s40 offen lds\n"
  "s_waitcnt lgkmcnt(3)\n"
  "v_mfma_f32_16x16x32_bf16 v[64:67], v[104:107], v[4:7], v[64:67]\n"
  "ds_read_b128 v[96:99], v183 offset:0\n"
  "s_waitcnt lgkmcnt(3)\n"
  "v_mfma_f32_16x16x32_bf16 v[68:71], v[108:111], v[4:7], v[68:71]\n"
  "ds_read_b128 v[100:103], v183 offset:8192\n"
  "s_add_u32 m0, %[wb], 102400\n"
  "s_add_u32 s40, s58, 131072\n"
  "buffer_load_dwordx4 %[sa1], %[ra], s40 offen lds\n"
  "s_waitcnt lgkmcnt(3)\n"
  "v_mfma_f32_16x16x32_bf16 v[64:67], v[112:115], v[8:11], v[64:67]\n"
  "ds_read_b128 v[104:107], v180 offset:256\n"
  "s_waitcnt lgkmcnt(3)\n"
  "v_mfma_f32_16x16x32_bf16 v[68:71], v[116:119], v[8:11], v[68:71]\n"
  "ds_read_b128 v[108:111], v180 offset:8448\n"
  "s_add_u32 m0, %[wb], 106496\n"
  "s_add_u32 s40, s58, 32768\n"
  "buffer_load_dwordx4 %[sa0], %[ra], s40 offen lds\n"
  "s_waitcnt lgkmcnt(3)\n"
  "v_mfma_f32_16x16x32_bf16 v[64:67], v[96:99], v[12:15], v[64:67]\n"
  "ds_read_b128 v[112:115], v181 offset:256\n"
  "s_waitcnt lgkmcnt(3)\n"
  "v_mfma_f32_16x16x32_bf16 v[68:71], v[100:103], v[12:15], v[68:71]\n"
  "ds_read_b128 v[116:119], v181 offset:8448\n"
  "s_add_u32 m0, %[wb], 110592\n"
  "s_add_u32 s40, s58, 163840\n"
  "buffer_load_dwordx4 %[sa1], %[ra], s40 offen lds\n"
  "s_waitcnt lgkmcnt(3)\n"
  "v_mfma_f32_16x16x32_bf16 v[64:67], v[104:107], v[16:19], v[64:67]\n"
  "ds_read_b128 v[96:99], v182 offset:256\n"
  "s_waitcnt lgkmcnt(3)\n"
  "v_mfma_f32_16x16x32_bf16 v[68:71], v[108:111], v[16:19], v[68:71]\n"
  "ds_read_b128 v[100:103], v182 offset:8448\n"
  "s_add_u32 m0, %[wb], 114688\n"
  "s_add_u32 s40, s59, 0\n"
  "buffer_load_dwordx4 %[sa0], %[ra], s40 offen lds\n"
  "s_waitcnt lgkmcnt(3)\n"
  "v_mfma_f32_16x16x32_bf16 v[64:67], v[112:115], v[20:23], v[64:67]\n"
  "ds_read_b128 v[104:107], v183 offset:256\n"
  "s_waitcnt lgkmcnt(3)\n"
  "v_mfma_f32_16x16x32_bf16 v[68:71], v[116:119], v[20:23], v[68:71]\n"
  "ds_read_b128 v[108:111], v183 offset:8448\n"
  "s_add_u32 m0, %[wb], 118784\n"
  "s_add_u32 s40, s59, 131072\n"
  "buffer_load_dwordx4 %[sa1], %[ra], s40 offen lds\n"
  "s_waitcnt vmcnt(6)\n"
  "s_barrier\n"
  "s_waitcnt lgkmcnt(3)\n"
  "v_mfma_f32_16x16x32_bf16 v[64:67], v[96:99], v[24:27], v[64:67]\n"
  "ds_read_b128 v[112:115], v180 offset:16384\n"
  "s_waitcnt lgkmcnt(3)\n"
  "v_mfma_f32_16x16x32_bf16 v[68:71], v[100:103], v[24:27], v[68:71]\n"
  "ds_read_b128 v[116:119], v180 offset:24576\n"
  "s_add_u32 m0, %[wb], 122880\n"
  "s_add_u32 s40, s59, 32768\n"
  "buffer_load_dwordx4 %[sa0], %[ra], s40 offen lds\n"
  "s_waitcnt lgkmcnt(3)\n"
  "v_mfma_f32_16x16x32_bf16 v[64:67], v[104:107], v[28:31], v[64:67]\n"
  "ds_read_b128 v[96:99], v181 offset:16384\n"
  "s_waitcnt lgkmcnt(3)\n"
  "v_mfma_f32_16x16x32_bf16 v[68:71], v[108:111], v[28:31], v[68:71]\n"
  "ds_read_b128 v[100:103], v181 offset:24576\n"
  "s_add_u32 m0, %[wb], 126976\n"
  "s_add_u32 s40, s59, 163840\n"
  "buffer_load_dwordx4 %[sa1], %[ra], s40 offen lds\n"
  "s_waitcnt lgkmcnt(3)\n"
  "v_mfma_f32_16x16x32_bf16 v[80:83], v[112:115], v[0:3], 0\n"
  "ds_read_b128 v[104:107], v182 offset:16384\n"
  "s_waitcnt lgkmcnt(3)\n"
  "v_mfma_f32_16x16x32_bf16 v[84:87], v[116:119], v[0:3], 0\n"
  "ds_read_b128 v[108:111], v182 offset:24576\n"
  "s_add_u32 m0, %[wb], 131072\n"
  "s_add_u32 s40, s60, 0\n"
  "buffer_load_dwordx4 %[sa0], %[ra], s40 offen lds\n"
  "s_waitcnt lgkmcnt(3)\n"
  "v_mfma_f32_16x16x32_bf16 v[80:83], v[96:99], v[4:7], v[80:83]\n"
  "ds_read_b128 v[112:115], v183 offset:16384\n"
  "s_waitcnt lgkmcnt(3)\n"
  "v_mfma_f32_16x16x32_bf16 v[84:87], v[100:103], v[4:7], v[84:87]\n"
  "ds_read_b128 v[116:119], v183 offset:24576\n"
  "v_mul_f32 v64, s42, v64\n"
  "v_mul_f32 v65, s42, v65\n"
  "s_add_u32 m0, %[wb], 135168\n"
  "s_add_u32 s40, s60, 131072\n"
  "buffer_load_dwordx4 %[sa1], %[ra], s40 offen lds\n"
  "s_waitcnt lgkmcnt(3)\n"
  "v_mfma_f32_16x16x32_bf16 v[80:83], v[104:107], v[8:11], v[80:83]\n"
  "ds_read_b128 v[96:99], v180 offset:16640\n"
  "v_cvt_pk_bf16_f32 v120, v64, v65\n"
  "v_mul_f32 v66, s42, v66\n"
  "s_waitcnt lgkmcnt(3)\n"
  "v_mfma_f32_16x16x32_bf16 v[84:87], v[108:111], v[8:11], v[84:87]\n"
  "ds_read_b128 v[100:103], v180 offset:24832\n"
  "v_mul_f32 v67, s42, v67\n"
  "v_cvt_pk_bf16_f32 v121, v66, v67\n"
  "s_add_u32 m0, %[wb], 139264\n"
  "s_add_u32 s40, s60, 32768\n"
  "buffer_load_dwordx4 %[sa0], %[ra], s40 offen lds\n"
  "s_waitcnt lgkmcnt(3)\n"
  "v_mfma_f32_16x16x32_bf16 v[80:83], v[112:115], v[12:15], v[80:83]\n"
  "ds_read_b128 v[104:107], v181 offset:16640\n"
  "v_mul_f32 v68, s42, v68\n"
  "v_mul_f32 v69, s42, v69\n"
  "s_waitcnt lgkmcnt(3)\n"
  "v_mfma_f32_16x16x32_bf16 v[84:87], v[116:119], v[12:15], v[84:87]\n"
  "ds_read_b128 v[108:111], v181 offset:24832\n"
  "v_cvt_pk_bf16_f32 v122, v68, v69\n"
  "v_mul_f32 v70, s42, v70\n"
  "s_add_u32 m0, %[wb], 143360\n"
  "s_add_u32 s40, s60, 163840\n"
  "buffer_load_dwordx4 %[sa1], %[ra], s40 offen lds\n"
  "s_waitcnt lgkmcnt(3)\n"
  "v_mfma_f32_16x16x32_bf16 v[80:83], v[96:99], v[16:19], v[80:83]\n"
  "ds_read_b128 v[112:115], v182 offset:16640\n"
  "v_mul_f32 v71, s42, v71\n"
  "v_cvt_pk_bf16_f32 v123, v70, v71\n"
  "s_waitcnt lgkmcnt(3)\n"
  "v_mfma_f32_16x16x32_bf16 v[84:87], v[100:103], v[16:19], v[84:87]\n"
  "ds_read_b128 v[116:119], v182 offset:24832\n"
  "s_add_u32 m0, %[wb], 0\n"
  "s_add_u32 s40, s61, 0\n"
  "buffer_load_dwordx4 %[sa0], %[ra], s40 offen lds\n"
  "s_waitcnt lgkmcnt(3)\n"
  "v_mfma_f32_16x16x32_bf16 v[80:83], v[104:107], v[20:23], v[80:83]\n"
  "ds_read_b128 v[96:99], v183 offset:16640\n"
  "s_waitcnt lgkmcnt(3)\n"
  "v_mfma_f32_16x16x32_bf16 v[84:87], v[108:111], v[20:23], v[84:87]\n"
  "ds_read_b128 v[100:103], v183 offset:24832\n"
  "s_add_u32 m0, %[wb], 4096\n"
  "s_add_u32 s40, s61, 131072\n"
  "buffer_load_dwordx4 %[sa1], %[ra], s40 offen lds\n"
  "s_waitcnt vmcnt(10)\n"
  "s_barrier\n"
  "s_waitcnt lgkmcnt(3)\n"
  "v_mfma_f32_16x16x32_bf16 v[80:83], v[112:115], v[24:27], v[80:83]\n"
  "ds_read_b128 v[104:107], v180 offset:32768\n"
  "s_waitcnt lgkmcnt(3)\n"
  "v_mfma_f32_16x16x32_bf16 v[84:87], v[116:119], v[24:27], v[84:87]\n"
  "ds_read_b128 v[108:111], v180 offset:40960\n"
  "s_add_u32 m0, %[wb], 8192\n"
  "s_add_u32 s40, s61, 32768\n"
  "buffer_load_dwordx4 %[sa0], %[ra], s40 offen lds\n"
  "s_waitcnt lgkmcnt(3)\n"
  "v_mfma_f32_16x16x32_bf16 v[80:83], v[96:99], v[28:31], v[80:83]\n"
  "ds_read_b128 v[112:115], v181 offset:32768\n"
  "s_waitcnt lgkmcnt(3)\n"
  "v_mfma_f32_16x16x32_bf16 v[84:87], v[100:103], v[28:31], v[84:87]\n"
  "ds_read_b128 v[116:119], v181 offset:40960\n"
  "s_add_u32 m0, %[wb], 12288\n"
  "s_add_u32 s40, s61, 163840\n"
  "buffer_load_dwordx4 %[sa1], %[ra], s40 offen lds\n"
  "s_waitcnt lgkmcnt(3)\n"
  "v_mfma_f32_16x16x32_bf16 v[64:67], v[104:107], v[0:3], 0\n"
  "ds_read_b128 v[96:99], v182 offset:32768\n"
  "s_waitcnt lgkmcnt(3)\n"
  "v_mfma_f32_16x16x32_bf16 v[68:71], v[108:111], v[0:3], 0\n"
  "ds_read_b128 v[100:103], v182 offset:40960\n"
  "s_add_u32 m0, %[wb], 16384\n"
  "s_add_u32 s40, s62, 0\n"
  "buffer_load_dwordx4 %[sa0], %[ra], s40 offen lds\n"
  "s_waitcnt lgkmcnt(3)\n"
  "v_mfma_f32_16x16x32_bf16 v[64:67], v[112:115], v[4:7], v[64:67]\n"
  "ds_read_b128 v[104:107], v183 offset:32768\n"
  "s_waitcnt lgkmcnt(3)\n"
  "v_mfma_f32_16x16x32_bf16 v[68:71], v[116:119], v[4:7], v[68:71]\n"
  "ds_read_b128 v[108:111], v183 offset:40960\n"
  "v_mul_f32 v80, s42, v80\n"
  "v_mul_f32 v81, s42, v81\n"
  "s_add_u32 m0, %[wb], 20480\n"
  "s_add_u32 s40, s62, 131072\n"
  "buffer_load_dwordx4 %[sa1], %[ra], s40 offen lds\n"
  "s_waitcnt lgkmcnt(3)\n"
  "v_mfma_f32_16x16x32_bf16 v[64:67], v[96:99], v[8:11], v[64:67]\n"
  "ds_read_b128 v[112:115], v180 offset:33024\n"
  "v_cvt_pk_bf16_f32 v128, v80, v81\n"
  "v_mul_f32 v82, s42, v82\n"
  "s_waitcnt lgkmcnt(3)\n"
  "v_mfma_f32_16x16x32_bf16 v[68:71], v[100:103], v[8:11], v[68:71]\n"
  "ds_read_b128 v[116:119], v180 offset:41216\n"
  "v_mul_f32 v83, s42, v83\n"
  "v_cvt_pk_bf16_f32 v129, v82, v83\n"
  "s_add_u32 m0, %[wb], 24576\n"
  "s_add_u32 s40, s62, 32768\n"
  "buffer_load_dwordx4 %[sa0], %[ra], s40 offen lds\n"
  "s_waitcnt lgkmcnt(3)\n"
  "v_mfma_f32_16x16x32_bf16 v[64:67], v[104:107], v[12:15], v[64:67]\n"
  "ds_read_b128 v[96:99], v181 offset:33024\n"
  "v_mul_f32 v84, s42, v84\n"
  "v_mul_f32 v85, s42, v85\n"
  "s_waitcnt lgkmcnt(3)\n"
  "v_mfma_f32_16x16x32_bf16 v[68:71], v[108:111], v[12:15], v[68:71]\n"
  "ds_read_b128 v[100:103], v181 offset:41216\n"
  "v_cvt_pk_bf16_f32 v130, v84, v85\n"
  "v_mul_f32 v86, s42, v86\n"
  "s_add_u32 m0, %[wb], 28672\n"
  "s_add_u32 s40, s62, 163840\n"
  "buffer_load_dwordx4 %[sa1], %[ra], s40 offen lds\n"
  "s_waitcnt lgkmcnt(3)\n"
  "v_mfma_f32_16x16x32_bf16 v[64:67], v[112:115], v[16:19], v[64:67]\n"
  "ds_read_b128 v[104:107], v182 offset:33024\n"
  "v_mul_f32 v87, s42, v87\n"
  "v_cvt_pk_bf16_f32 v131, v86, v87\n"
  "s_waitcnt lgkmcnt(3)\n"
  "v_mfma_f32_16x16x32_bf16 v[68:71], v[116:119], v[16:19], v[68:71]\n"
  "ds_read_b128 v[108:111], v182 offset:41216\n"
  "buffer_store_dwordx4 v[192:195], %[x0], %[ws], 0 offen sc1\n"
  "s_waitcnt lgkmcnt(3)\n"
  "v_mfma_f32_16x16x32_bf16 v[64:67], v[96:99], v[20:23], v[64:67]\n"
  "ds_read_b128 v[112:115], v183 offset:33024\n"
  "s_waitcnt lgkmcnt(3)\n"
  "v_mfma_f32_16x16x32_bf16 v[68:71], v[100:103], v[20:23], v[68:71]\n"
  "ds_read_b128 v[116:119], v183 offset:41216\n"
  "buffer_store_dwordx4 v[196:199], %[x1], %[ws], 0 offen sc1\n"
  "s_waitcnt vmcnt(14)\n"
  "s_barrier\n"
  "s_waitcnt lgkmcnt(3)\n"
  "v_mfma_f32_16x16x32_bf16 v[64:67], v[104:107], v[24:27], v[64:67]\n"
  "ds_read_b128 v[96:99], v180 offset:49152\n"
  "s_waitcnt lgkmcnt(3)\n"
  "v_mfma_f32_16x16x32_bf16 v[68:71], v[108:111], v[24:27], v[68:71]\n"
  "ds_read_b128 v[100:103], v180 offset:57344\n"
  "s_lshr_b32 s40, s56, 8\n"
  "buffer_store_dwordx4 v[120:123], %[o0], %[ws], s40 offen sc1\n"
  "s_waitcnt lgkmcnt(3)\n"
  "v_mfma_f32_16x16x32_bf16 v[64:67], v[112:115], v[28:31], v[64:67]\n"
  "ds_read_b128 v[104:107], v181 offset:49152\n"
  "s_waitcnt lgkmcnt(3)\n"
  "v_mfma_f32_16x16x32_bf16 v[68:71], v[116:119], v[28:31], v[68:71]\n"
  "ds_read_b128 v[108:111], v181 offset:57344\n"
  "s_lshr_b32 s40, s57, 8\n"
  "buffer_store_dwordx4 v[128:131], %[o0], %[ws], s40 offen sc1\n"
  "s_waitcnt lgkmcnt(3)\n"
  "v_mfma_f32_16x16x32_bf16 v[80:83], v[96:99], v[0:3], 0\n"
  "ds_read_b128 v[112:115], v182 offset:49152\n"
  "s_waitcnt lgkmcnt(3)\n"
  "v_mfma_f32_16x16x32_bf16 v[84:87], v[100:103], v[0:3], 0\n"
  "ds_read_b128 v[116:119], v182 offset:57344\n"
  "s_waitcnt lgkmcnt(3)\n"
  "v_mfma_f32_16x16x32_bf16 v[80:83], v[104:107], v[4:7], v[80:83]\n"
  "ds_read_b128 v[96:99], v183 offset:49152\n"
  "s_waitcnt lgkmcnt(3)\n"
  "v_mfma_f32_16x16x32_bf16 v[84:87], v[108:111], v[4:7], v[84:87]\n"
  "ds_read_b128 v[100:103], v183 offset:57344\n"
  "v_mul_f32 v64, s42, v64\n"
  "v_mul_f32 v65, s42, v65\n"
  "s_waitcnt lgkmcnt(3)\n"
  "v_mfma_f32_16x16x32_bf16 v[80:83], v[112:115], v[8:11], v[80:83]\n"
  "ds_read_b128 v[104:107], v180 offset:49408\n"
  "v_cvt_pk_bf16_f32 v136, v64, v65\n"
  "v_mul_f32 v66, s42, v66\n"
  "s_waitcnt lgkmcnt(3)\n"
  "v_mfma_f32_16x16x32_bf16 v[84:87], v[116:119], v[8:11], v[84:87]\n"
  "ds_read_b128 v[108:111], v180 offset:57600\n"
  "v_mul_f32 v67, s42, v67\n"
  "v_cvt_pk_bf16_f32 v137, v66, v67\n"
  "s_waitcnt lgkmcnt(3)\n"
  "v_mfma_f32_16x16x32_bf16 v[80:83], v[96:99], v[12:15], v[80:83]\n"
  "ds_read_b128 v[112:115], v181 offset:49408\n"
  "v_mul_f32 v68, s42, v68\n"
  "v_mul_f32 v69, s42, v69\n"
  "s_waitcnt lgkmcnt(3)\n"
  "v_mfma_f32_16x16x32_bf16 v[84:87], v[100:103], v[12:15], v[84:87]\n"
  "ds_read_b128 v[116:119], v181 offset:57600\n"
  "v_cvt_pk_bf16_f32 v138, v68, v69\n"
  "v_mul_f32 v70, s42, v70\n"
  "s_waitcnt lgkmcnt(3)\n"
  "v_mfma_f32_16x16x32_bf16 v[80:83], v[104:107], v[16:19], v[80:83]\n"
  "ds_read_b128 v[96:99], v182 offset:49408\n"
  "v_mul_f32 v71, s42, v71\n"
  "v_cvt_pk_bf16_f32 v139, v70, v71\n"
  "s_waitcnt lgkmcnt(3)\n"
  "v_mfma_f32_16x16x32_bf16 v[84:87], v[108:111], v[16:19], v[84:87]\n"
  "ds_read_b128 v[100:103], v182 offset:57600\n"
  "s_waitcnt lgkmcnt(3)\n"
  "v_mfma_f32_16x16x32_bf16 v[80:83], v[112:115], v[20:23], v[80:83]\n"
  "ds_read_b128 v[104:107], v183 offset:49408\n"
  "s_waitcnt lgkmcnt(3)\n"
  "v_mfma_f32_16x16x32_bf16 v[84:87], v[116:119], v[20:23], v[84:87]\n"
  "ds_read_b128 v[108:111], v183 offset:57600\n"
  "s_lshr_b32 s40, s58, 8\n"
  "buffer_store_dwordx4 v[136:139], %[o0], %[ws], s40 offen sc1\n"
  "s_waitcnt vmcnt(13)\n"
  "s_barrier\n"
  "s_waitcnt lgkmcnt(3)\n"
  "v_mfma_f32_16x16x32_bf16 v[80:83], v[96:99], v[24:27], v[80:83]\n"
  "ds_read_b128 v[112:115], v184 offset:0\n"
  "s_waitcnt lgkmcnt(3)\n"
  "v_mfma_f32_16x16x32_bf16 v[84:87], v[100:103], v[24:27], v[84:87]\n"
  "ds_read_b128 v[116:119], v184 offset:8192\n"
  "s_waitcnt lgkmcnt(3)\n"
  "v_mfma_f32_16x16x32_bf16 v[80:83], v[104:107], v[28:31], v[80:83]\n"
  "ds_read_b128 v[96:99], v185 offset:0\n"
  "s_waitcnt lgkmcnt(3)\n"
  "v_mfma_f32_16x16x32_bf16 v[84:87], v[108:111], v[28:31], v[84:87]\n"
  "ds_read_b128 v[100:103], v185 offset:8192\n"
  "s_waitcnt lgkmcnt(3)\n"
  "v_mfma_f32_16x16x32_bf16 v[64:67], v[112:115], v[0:3], 0\n"
  "ds_read_b128 v[104:107], v186 offset:0\n"
  "s_waitcnt lgkmcnt(3)\n"
  "v_mfma_f32_16x16x32_bf16 v[68:71], v[116:119], v[0:3], 0\n"
  "ds_read_b128 v[108:111], v186 offset:8192\n"
  "s_waitcnt lgkmcnt(3)\n"
  "v_mfma_f32_16x16x32_bf16 v[64:67], v[96:99], v[4:7], v[64:67]\n"
  "ds_read_b128 v[112:115], v187 offset:0\n"
  "s_waitcnt lgkmcnt(3)\n"
  "v_mfma_f32_16x16x32_bf16 v[68:71], v[100:103], v[4:7], v[68:71]\n"
  "ds_read_b128 v[116:119], v187 offset:8192\n"
  "v_mul_f32 v80, s42, v80\n"
  "v_mul_f32 v81, s42, v81\n"
  "s_waitcnt lgkmcnt(3)\n"
  "v_mfma_f32_16x16x32_bf16 v[64:67], v[104:107], v[8:11], v[64:67]\n"
  "ds_read_b128 v[96:99], v184 offset:256\n"
  "v_cvt_pk_bf16_f32 v144, v80, v81\n"
  "v_mul_f32 v82, s42, v82\n"
  "s_waitcnt lgkmcnt(3)\n"
  "v_mfma_f32_16x16x32_bf16 v[68:71], v[108:111], v[8:11], v[68:71]\n"
  "ds_read_b128 v[100:103], v184 offset:8448\n"
  "v_mul_f32 v83, s42, v83\n"
  "v_cvt_pk_bf16_f32 v145, v82, v83\n"
  "s_waitcnt lgkmcnt(3)\n"
  "v_mfma_f32_16x16x32_bf16 v[64:67], v[112:115], v[12:15], v[64:67]\n"
  "ds_read_b128 v[104:107], v185 offset:256\n"
  "v_mul_f32 v84, s42, v84\n"
  "v_mul_f32 v85, s42, v85\n"
  "s_waitcnt lgkmcnt(3)\n"
  "v_mfma_f32_16x16x32_bf16 v[68:71], v[116:119], v[12:15], v[68:71]\n"
  "ds_read_b128 v[108:111], v185 offset:8448\n"
  "v_cvt_pk_bf16_f32 v146, v84, v85\n"
  "v_mul_f32 v86, s42, v86\n"
  "s_waitcnt lgkmcnt(3)\n"
  "v_mfma_f32_16x16x32_bf16 v[64:67], v[96:99], v[16:19], v[64:67]\n"
  "ds_read_b128 v[112:115], v186 offset:256\n"
  "v_mul_f32 v87, s42, v87\n"
  "v_cvt_pk_bf16_f32 v147, v86, v87\n"
  "s_waitcnt lgkmcnt(3)\n"
  "v_mfma_f32_16x16x32_bf16 v[68:71], v[100:103], v[16:19], v[68:71]\n"
  "ds_read_b128 v[116:119], v186 offset:8448\n"
  "s_waitcnt lgkmcnt(3)\n"
  "v_mfma_f32_16x16x32_bf16 v[64:67], v[104:107], v[20:23], v[64:67]\n"
  "ds_read_b128 v[96:99], v187 offset:256\n"
  "s_waitcnt lgkmcnt(3)\n"
  "v_mfma_f32_16x16x32_bf16 v[68:71], v[108:111], v[20:23], v[68:71]\n"
  "ds_read_b128 v[100:103], v187 offset:8448\n"
  "s_lshr_b32 s40, s59, 8\n"
  "buffer_store_dwordx4 v[144:147], %[o0], %[ws], s40 offen sc1\n"
  "s_waitcnt vmcnt(10)\n"
  "s_barrier\n"
  "s_waitcnt lgkmcnt(3)\n"
  "v_mfma_f32_16x16x32_bf16 v[64:67], v[112:115], v[24:27], v[64:67]\n"
  "ds_read_b128 v[104:107], v176 offset:0\n"
  "s_waitcnt lgkmcnt(3)\n"
  "v_mfma_f32_16x16x32_bf16 v[68:71], v[116:119], v[24:27], v[68:71]\n"
  "ds_read_b128 v[108:111], v176 offset:8192\n"
  "s_waitcnt lgkmcnt(3)\n"
  "v_mfma_f32_16x16x32_bf16 v[64:67], v[96:99], v[28:31], v[64:67]\n"
  "ds_read_b128 v[112:115], v177 offset:0\n"
  "s_waitcnt lgkmcnt(3)\n"
  "v_mfma_f32_16x16x32_bf16 v[68:71], v[100:103], v[28:31], v[68:71]\n"
  "ds_read_b128 v[116:119], v177 offset:8192\n"
  "s_waitcnt lgkmcnt(3)\n"
  "v_mfma_f32_16x16x32_bf16 v[80:83], v[104:107], v[0:3], 0\n"
  "ds_read_b128 v[96:99], v178 offset:0\n"
  "s_waitcnt lgkmcnt(3)\n"
  "v_mfma_f32_16x16x32_bf16 v[84:87], v[108:111], v[0:3], 0\n"
  "ds_read_b128 v[100:103], v178 offset:8192\n"
  "s_waitcnt lgkmcnt(3)\n"
  "v_mfma_f32_16x16x32_bf16 v[80:83], v[112:115], v[4:7], v[80:83]\n"
  "ds_read_b128 v[104:107], v179 offset:0\n"
  "s_waitcnt lgkmcnt(3)\n"
  "v_mfma_f32_16x16x32_bf16 v[84:87], v[116:119], v[4:7], v[84:87]\n"
  "ds_read_b128 v[108:111], v179 offset:8192\n"
  "v_mul_f32 v64, s42, v64\n"
  "v_mul_f32 v65, s42, v65\n"
  "s_waitcnt lgkmcnt(3)\n"
  "v_mfma_f32_16x16x32_bf16 v[80:83], v[96:99], v[8:11], v[80:83]\n"
  "ds_read_b128 v[112:115], v176 offset:256\n"
  "v_cvt_pk_bf16_f32 v152, v64, v65\n"
  "v_mul_f32 v66, s42, v66\n"
  "s_waitcnt lgkmcnt(3)\n"
  "v_mfma_f32_16x16x32_bf16 v[84:87], v[100:103], v[8:11], v[84:87]\n"
  "ds_read_b128 v[116:119], v176 offset:8448\n"
  "v_mul_f32 v67, s42, v67\n"
  "v_cvt_pk_bf16_f32 v153, v66, v67\n"
  "s_waitcnt lgkmcnt(3)\n"
  "v_mfma_f32_16x16x32_bf16 v[80:83], v[104:107], v[12:15], v[80:83]\n"
  "ds_read_b128 v[96:99], v177 offset:256\n"
  "v_mul_f32 v68, s42, v68\n"
  "v_mul_f32 v69, s42, v69\n"
  "s_waitcnt lgkmcnt(3)\n"
  "v_mfma_f32_16x16x32_bf16 v[84:87], v[108:111], v[12:15], v[84:87]\n"
  "ds_read_b128 v[100:103], v177 offset:8448\n"
  "v_cvt_pk_bf16_f32 v154, v68, v69\n"
  "v_mul_f32 v70, s42, v70\n"
  "s_waitcnt lgkmcnt(3)\n"
  "v_mfma_f32_16x16x32_bf16 v[80:83], v[112:115], v[16:19], v[80:83]\n"
  "ds_read_b128 v[104:107], v178 offset:256\n"
  "v_mul_f32 v71, s42, v71\n"
  "v_cvt_pk_bf16_f32 v155, v70, v71\n"
  "s_waitcnt lgkmcnt(3)\n"
  "v_mfma_f32_16x16x32_bf16 v[84:87], v[116:119], v[16:19], v[84:87]\n"
  "ds_read_b128 v[108:111], v178 offset:8448\n"
  "s_waitcnt lgkmcnt(3)\n"
  "v_mfma_f32_16x16x32_bf16 v[80:83], v[96:99], v[20:23], v[80:83]\n"
  "ds_read_b128 v[112:115], v179 offset:256\n"
  "s_waitcnt lgkmcnt(3)\n"
  "v_mfma_f32_16x16x32_bf16 v[84:87], v[100:103], v[20:23], v[84:87]\n"
  "ds_read_b128 v[116:119], v179 offset:8448\n"
  "s_lshr_b32 s40, s60, 8\n"
  "buffer_store_dwordx4 v[152:155], %[o0], %[ws], s40 offen sc1\n"
  "s_waitcnt vmcnt(7)\n"
  "s_barrier\n"
  "s_waitcnt lgkmcnt(3)\n"
  "v_mfma_f32_16x16x32_bf16 v[80:83], v[104:107], v[24:27], v[80:83]\n"
  "ds_read_b128 v[96:99], v176 offset:16384\n"
  "s_waitcnt lgkmcnt(3)\n"
  "v_mfma_f32_16x16x32_bf16 v[84:87], v[108:111], v[24:27], v[84:87]\n"
  "ds_read_b128 v[100:103], v176 offset:24576\n"
  "s_waitcnt lgkmcnt(3)\n"
  "v_mfma_f32_16x16x32_bf16 v[80:83], v[112:115], v[28:31], v[80:83]\n"
  "ds_read_b128 v[104:107], v177 offset:16384\n"
  "s_waitcnt lgkmcnt(3)\n"
  "v_mfma_f32_16x16x32_bf16 v[84:87], v[116:119], v[28:31], v[84:87]\n"
  "ds_read_b128 v[108:111], v177 offset:24576\n"
  "s_waitcnt lgkmcnt(3)\n"
  "v_mfma_f32_16x16x32_bf16 v[64:67], v[96:99], v[0:3], 0\n"
  "ds_read_b128 v[112:115], v178 offset:16384\n"
  "s_waitcnt lgkmcnt(3)\n"
  "v_mfma_f32_16x16x32_bf16 v[68:71], v[100:103], v[0:3], 0\n"
  "ds_read_b128 v[116:119], v178 offset:24576\n"
  "s_waitcnt lgkmcnt(3)\n"
  "v_mfma_f32_16x16x32_bf16 v[64:67], v[104:107], v[4:7], v[64:67]\n"
  "ds_read_b128 v[96:99], v179 offset:16384\n"
  "s_waitcnt lgkmcnt(3)\n"
  "v_mfma_f32_16x16x32_bf16 v[68:71], v[108:111], v[4:7], v[68:71]\n"
  "ds_read_b128 v[100:103], v179 offset:24576\n"
  "v_mul_f32 v80, s42, v80\n"
  "v_mul_f32 v81, s42, v81\n"
  "s_waitcnt lgkmcnt(3)\n"
  "v_mfma_f32_16x16x32_bf16 v[64:67], v[112:115], v[8:11], v[64:67]\n"
  "ds_read_b128 v[104:107], v176 offset:16640\n"
  "v_cvt_pk_bf16_f32 v160, v80, v81\n"
  "v_mul_f32 v82, s42, v82\n"
  "s_waitcnt lgkmcnt(3)\n"
  "v_mfma_f32_16x16x32_bf16 v[68:71], v[116:119], v[8:11], v[68:71]\n"
  "ds_read_b128 v[108:111], v176 offset:24832\n"
  "v_mul_f32 v83, s42, v83\n"
  "v_cvt_pk_bf16_f32 v161, v82, v83\n"
  "s_waitcnt lgkmcnt(3)\n"
  "v_mfma_f32_16x16x32_bf16 v[64:67], v[96:99], v[12:15], v[64:67]\n"
  "ds_read_b128 v[112:115], v177 offset:16640\n"
  "v_mul_f32 v84, s42, v84\n"
  "v_mul_f32 v85, s42, v85\n"
  "s_waitcnt lgkmcnt(3)\n"
  "v_mfma_f32_16x16x32_bf16 v[68:71], v[100:103], v[12:15], v[68:71]\n"
  "ds_read_b128 v[116:119], v177 offset:24832\n"
  "v_cvt_pk_bf16_f32 v162, v84, v85\n"
  "v_mul_f32 v86, s42, v86\n"
  "s_waitcnt lgkmcnt(3)\n"
  "v_mfma_f32_16x16x32_bf16 v[64:67], v[104:107], v[16:19], v[64:67]\n"
  "ds_read_b128 v[96:99], v178 offset:16640\n"
  "v_mul_f32 v87, s42, v87\n"
  "v_cvt_pk_bf16_f32 v163, v86, v87\n"
  "s_waitcnt lgkmcnt(3)\n"
  "v_mfma_f32_16x16x32_bf16 v[68:71], v[108:111], v[16:19], v[68:71]\n"
  "ds_read_b128 v[100:103], v178 offset:24832\n"
  "s_waitcnt lgkmcnt(3)\n"
  "v_mfma_f32_16x16x32_bf16 v[64:67], v[112:115], v[20:23], v[64:67]\n"
  "ds_read_b128 v[104:107], v179 offset:16640\n"
  "s_waitcnt lgkmcnt(3)\n"
  "v_mfma_f32_16x16x32_bf16 v[68:71], v[116:119], v[20:23], v[68:71]\n"
  "ds_read_b128 v[108:111], v179 offset:24832\n"
  "s_lshr_b32 s40, s61, 8\n"
  "buffer_store_dwordx4 v[160:163], %[o0], %[ws], s40 offen sc1\n"
  "s_waitcnt lgkmcnt(3)\n"
  "v_mfma_f32_16x16x32_bf16 v[64:67], v[96:99], v[24:27], v[64:67]\n"
  "s_waitcnt lgkmcnt(2)\n"
  "v_mfma_f32_16x16x32_bf16 v[68:71], v[100:103], v[24:27], v[68:71]\n"
  "s_waitcnt lgkmcnt(1)\n"
  "v_mfma_f32_16x16x32_bf16 v[64:67], v[104:107], v[28:31], v[64:67]\n"
  "s_waitcnt lgkmcnt(0)\n"
  "v_mfma_f32_16x16x32_bf16 v[68:71], v[108:111], v[28:31], v[68:71]\n"
  "s_nop 7\n"
  "v_mul_f32 v64, s42, v64\n"
  "v_mul_f32 v65, s42, v65\n"
  "v_cvt_pk_bf16_f32 v168, v64, v65\n"
  "v_mul_f32 v66, s42, v66\n"
  "v_mul_f32 v67, s42, v67\n"
  "v_cvt_pk_bf16_f32 v169, v66, v67\n"
  "v_mul_f32 v68, s42, v68\n"
  "v_mul_f32 v69, s42, v69\n"
  "v_cvt_pk_bf16_f32 v170, v68, v69\n"
  "v_mul_f32 v70, s42, v70\n"
  "v_mul_f32 v71, s42, v71\n"
  "v_cvt_pk_bf16_f32 v171, v70, v71\n"
  "s_lshr_b32 s40, s62, 8\n"
  "buffer_store_dwordx4 v[168:171], %[o0], %[ws], s40 offen sc1\n"
  "Le_%=:\n"
  :: [ws] "s"(ws), [ra] "s"(ra), [wb] "s"(wb), [rk] "s"(2 * nb), [sa0] "v"(sa), [sa1] "v"(sa ^ 128),
     [o0] "v"(W_WF + ((mh * 28 + 7 * nb + 2 * w) * 14 + 7 * kb) * 1024 + lane * 16), [x0] "v"(xf), [x1] "v"(tid < 192 ? xf + 8192 : OOB),
     [f0] "s"(mk(a.wg, D * F * 2)), [f1] "s"(mk(a.wu, D * F * 2)), [f2] "s"(mk(a.wd, D * F * 2)),
     [pf0] "v"(blk * 6272 + tid * 16), [pf1] "v"(tid < 136 ? blk * 6272 + 4096 + tid * 16 : OOB),
     [ll] "v"(PRE_LN + tid * 16), [la] "v"(c * 512 + ((g ^ c) << 4)),
     [lb] "v"(mat ? (8 * g + (c >> 2)) * 256 + ((((c >> 2) + 4 * (g & 1)) ^ (2 * w)) << 5) + (c & 3) * 8 : w * 16384)
  : "v0", "v1", "v2", "v3", "v4", "v5", "v6", "v7", "v8", "v9", "v10", "v11", "v12", "v13", "v14", "v15", "v16", "v17", "v18", "v19", "v20", "v21", "v22", "v23", "v24", "v25", "v26", "v27", "v28", "v29", "v30", "v31", "v32", "v33", "v34", "v35", "v36", "v37", "v38", "v39", "v40", "v41", "v42", "v43", "v44", "v45", "v46", "v47", "v48", "v49", "v50", "v51", "v52", "v53", "v54", "v55", "v56", "v57", "v58", "v59", "v60", "v61", "v62", "v63", "v64", "v65", "v66", "v67", "v68", "v69", "v70", "v71", "v72", "v73", "v74", "v75", "v76", "v77", "v78", "v79", "v80", "v81", "v82", "v83", "v84", "v85", "v86", "v87", "v88", "v89", "v90", "v91", "v92", "v93", "v94", "v95", "v96", "v97", "v98", "v99", "v100", "v101", "v102", "v103", "v104", "v105", "v106", "v107", "v108", "v109", "v110", "v111", "v112", "v113", "v114", "v115", "v116", "v117", "v118", "v119", "v120", "v121", "v122", "v123", "v124", "v125", "v126", "v127", "v128", "v129", "v130", "v131", "v132", "v133", "v134", "v135", "v136", "v137", "v138", "v139", "v140", "v141", "v142", "v143", "v144", "v145", "v146", "v147", "v148", "v149", "v150", "v151", "v152", "v153", "v154", "v155", "v156", "v157", "v158", "v159", "v160", "v161", "v162", "v163", "v164", "v165", "v166", "v167", "v168", "v169", "v170", "v171", "v172", "v173", "v174", "v175", "v176", "v177", "v178", "v179", "v180", "v181", "v182", "v183", "v184", "v185", "v186", "v187", "v188", "v189", "v190", "v191", "v192", "v193", "v194", "v195", "v196", "v197", "v198", "v199", "a248", "a249", "a250", "a251", "s40", "s41", "s42", "s43", "s44", "s45", "s46", "s47", "s48", "s49", "s50", "s51", "s52", "s53", "s54", "s55", "s56", "s57", "s58", "s59", "s60", "s61", "s62", "scc", "memory");
}

__device__ __forceinline__ void pre_block1(const Args& a, rsrc_t ws) {
  constexpr int mat = 1;
  const int tid = threadIdx.x, lane = tid & 63, w = __builtin_amdgcn_readfirstlane(tid >> 6), g = lane >> 4, c = lane & 15;
  const int blk = blockIdx.x, h = (blk & 7) * 2 + ((blk >> 6) & 1), mh = 16 * mat + h, kb = (blk >> 5) & 1, nb = (blk >> 3) & 3;
  const rsrc_t ra = mk(mat ? a.wv : a.wk, D * H * E * 2), rbw = mk(mat ? a.wo : a.wq, D * H * E * 2), rx = mk(a.x, S * D * 2);
  const int r8 = 2 * w + (lane >> 5), pp = lane & 31;
  const unsigned sw = (pp ^ r8) << 4, wb = w * 1024;
  const unsigned sa = ((224 * kb + 8 * (w >> 1) + (r8 & 3)) * H + h) * (E * 2) + sw;
  const unsigned sb = mat ? (h * E + 4 * w + g) * (D * 2) + nb * 224 + ((c ^ (2 * g + 8 * (w >> 1))) << 4) : ((112 * nb + r8) * H + h) * (E * 2) + sw;
  const unsigned xo = (blk * 8 + r8) * (D * 2) + pp * 16;
  v4u v0, v1;
  asm volatile("buffer_load_dwordx4 %[v0], %[x0], %[rx], 0 offen\n buffer_load_dwordx4 %[v1], %[x1], %[rx], 0 offen\n"
  "s_lshl_b32 s56, %[rk], 18\n"
  "s_add_u32 s57, s56, 262144\n"
  "s_cmp_eq_u32 s57, 1835008\n"
  "s_cselect_b32 s57, 0, s57\n"
  "s_lshl_b32 s41, %[rn], 15\n"
  "s_add_u32 s41, s41, %[wb]\n"
  "s_mul_i32 s42, %[rn], 114688\n"
  "s_add_u32 m0, s41, 0\n"
  "s_add_u32 s40, s42, 0\n"
  "buffer_load_dwordx4 %[sb0], %[rb], s40 offen lds\n"
  "s_add_u32 m0, s41, 4096\n"
  "s_add_u32 s40, s42, 14336\n"
  "buffer_load_dwordx4 %[sb0], %[rb], s40 offen lds\n"
  "s_add_u32 m0, s41, 8192\n"
  "s_add_u32 s40, s42, 28672\n"
  "buffer_load_dwordx4 %[sb0], %[rb], s40 offen lds\n"
  "s_add_u32 m0, s41, 12288\n"
  "s_add_u32 s40, s42, 43008\n"
  "buffer_load_dwordx4 %[sb0], %[rb], s40 offen lds\n"
  "s_add_u32 m0, s41, 16384\n"
  "s_add_u32 s40, s42, 57344\n"
  "buffer_load_dwordx4 %[sb0], %[rb], s40 offen lds\n"
  "s_add_u32 m0, s41, 20480\n"
  "s_add_u32 s40, s42, 71680\n"
  "buffer_load_dwordx4 %[sb0], %[rb], s40 offen lds\n"
  "s_add_u32 m0, s41, 24576\n"
  "s_add_u32 s40, s42, 86016\n"
  "buffer_load_dwordx4 %[sb0], %[rb], s40 offen lds\n"
  "s_add_u32 m0, s41, 28672\n"
  "s_add_u32 s40, s42, 100352\n"
  "buffer_load_dwordx4 %[sb0], %[rb], s40 offen lds\n"
  "s_mul_i32 s40, %[rn], 65536\n"
  "s_sub_u32 s41, s41, s40\n"
  "s_mul_i32 s40, %[rn], 229376\n"
  "s_sub_u32 s42, s42, s40\n"
  "s_add_u32 m0, %[wb], 65536\n"
  "s_add_u32 s40, s56, 0\n"
  "buffer_load_dwordx4 %[sa0], %[ra], s40 offen lds\n"
  "s_add_u32 m0, %[wb], 69632\n"
  "s_add_u32 s40, s56, 131072\n"
  "buffer_load_dwordx4 %[sa1], %[ra], s40 offen lds\n"
  "s_add_u32 m0, %[wb], 73728\n"
  "s_add_u32 s40, s56, 32768\n"
  "buffer_load_dwordx4 %[sa0], %[ra], s40 offen lds\n"
  "s_add_u32 m0, %[wb], 77824\n"
  "s_add_u32 s40, s56, 163840\n"
  "buffer_load_dwordx4 %[sa1], %[ra], s40 offen lds\n"
  "s_add_u32 m0, s41, 32768\n"
  "s_add_u32 s40, s42, 114688\n"
  "buffer_load_dwordx4 %[sb0], %[rb], s40 offen lds\n"
  "s_add_u32 m0, s41, 36864\n"
  "s_add_u32 s40, s42, 129024\n"
  "buffer_load_dwordx4 %[sb0], %[rb], s40 offen lds\n"
  "s_add_u32 m0, s41, 40960\n"
  "s_add_u32 s40, s42, 143360\n"
  "buffer_load_dwordx4 %[sb0], %[rb], s40 offen lds\n"
  "s_add_u32 m0, s41, 45056\n"
  "s_add_u32 s40, s42, 157696\n"
  "buffer_load_dwordx4 %[sb0], %[rb], s40 offen lds\n"
  "s_add_u32 m0, s41, 49152\n"
  "s_add_u32 s40, s42, 172032\n"
  "buffer_load_dwordx4 %[sb0], %[rb], s40 offen lds\n"
  "s_add_u32 m0, s41, 53248\n"
  "s_add_u32 s40, s42, 186368\n"
  "buffer_load_dwordx4 %[sb0], %[rb], s40 offen lds\n"
  "s_add_u32 m0, s41, 57344\n"
  "s_add_u32 s40, s42, 200704\n"
  "buffer_load_dwordx4 %[sb0], %[rb], s40 offen lds\n"
  "s_add_u32 m0, s41, 61440\n"
  "s_add_u32 s40, s42, 215040\n"
  "buffer_load_dwordx4 %[sb0], %[rb], s40 offen lds\n"
  "s_add_u32 m0, %[wb], 81920\n"
  "s_add_u32 s40, s57, 0\n"
  "buffer_load_dwordx4 %[sa0], %[ra], s40 offen lds\n"
  "s_add_u32 m0, %[wb], 86016\n"
  "s_add_u32 s40, s57, 131072\n"
  "buffer_load_dwordx4 %[sa1], %[ra], s40 offen lds\n"
  "s_add_u32 m0, %[wb], 90112\n"
  "s_add_u32 s40, s57, 32768\n"
  "buffer_load_dwordx4 %[sa0], %[ra], s40 offen lds\n"
  "s_add_u32 m0, %[wb], 94208\n"
  "s_add_u32 s40, s57, 163840\n"
  "buffer_load_dwordx4 %[sa1], %[ra], s40 offen lds\n"
  "s_waitcnt vmcnt(24)\n"
  : [v0] "=&v"(v0), [v1] "=&v"(v1)
  : [x0] "v"(xo), [x1] "v"(pp < 24 ? xo + 512 : OOB), [rx] "s"(rx), [ra] "s"(ra), [rb] "s"(rbw), [wb] "s"(wb),
    [rk] "s"(2 * nb), [rn] "s"(kb), [sa0] "v"(sa), [sa1] "v"(sa ^ 128), [sb0] "v"(sb), [sb1] "v"(sb ^ 128) : "s40", "s41", "s42", "s56", "s57", "s58", "scc", "memory");
  float f[16], s = 0.f;
#pragma unroll
  for (int e = 0; e < 4; ++e) { f[2 * e] = lo16(v0[e]); f[2 * e + 1] = hi16(v0[e]); f[8 + 2 * e] = lo16(v1[e]); f[9 + 2 * e] = hi16(v1[e]); }
#pragma unroll
  for (int e = 0; e < 16; ++e) s += f[e];
#pragma unroll
  for (int o = 1; o < 32; o <<= 1) s += __shfl_xor(s, o);
  const float mean = s / D;
  float q0 = 0.f, q1 = 0.f;
#pragma unroll
  for (int e = 0; e < 8; ++e) { q0 += (f[e] - mean) * (f[e] - mean); q1 += (f[8 + e] - mean) * (f[8 + e] - mean); }
  float q = pp < 24 ? q0 + q1 : q0;
#pragma unroll
  for (int o = 1; o < 32; o <<= 1) q += __shfl_xor(q, o);
  const float m = rb(mean), rden = 1.f / rb(sqrtf(rb(rb(q / D) + 1e-5f)));
#pragma unroll
  for (int e = 0; e < 4; ++e) {
    const unsigned d0 = pk2(f[2 * e] - m, f[2 * e + 1] - m), d1 = pk2(f[8 + 2 * e] - m, f[9 + 2 * e] - m);
    v0[e] = pk2(lo16(d0) * rden, hi16(d0) * rden); v1[e] = pk2(lo16(d1) * rden, hi16(d1) * rden);
  }
  *(LAS v4u*)(PRE_LN + (pp * 8 + r8) * 16) = v0;
  *(LAS v4u*)(PRE_LN + ((pp + 32) * 8 + r8) * 16) = v1;
  const unsigned xf = W_XF + (blk >> 1) * 14336 + (tid >> 3) * 256 + (blk & 1) * 128 + (tid & 7) * 16;
  asm volatile(
  "s_mov_b32 s42, 0x3db8aa3b\n"
  "s_lshl_b32 s56, %[rk], 18\n"
  "s_add_u32 s57, s56, 262144\n"
  "s_cmp_eq_u32 s57, 1835008\n"
  "s_cselect_b32 s57, 0, s57\n"
  "s_add_u32 s58, s57, 262144\n"
  "s_cmp_eq_u32 s58, 1835008\n"
  "s_cselect_b32 s58, 0, s58\n"
  "s_add_u32 s59, s58, 262144\n"
  "s_cmp_eq_u32 s59, 1835008\n"
  "s_cselect_b32 s59, 0, s59\n"
  "s_add_u32 s60, s59, 262144\n"
  "s_cmp_eq_u32 s60, 1835008\n"
  "s_cselect_b32 s60, 0, s60\n"
  "s_add_u32 s61, s60, 262144\n"
  "s_cmp_eq_u32 s61, 1835008\n"
  "s_cselect_b32 s61, 0, s61\n"
  "s_add_u32 s62, s61, 262144\n"
  "s_cmp_eq_u32 s62, 1835008\n"
  "s_cselect_b32 s62, 0, s62\n"
  "v_xor_b32 v176, 0, %[la]\n"
  "v_add_u32 v180, 0x10000, v176\n"
  "v_add_u32 v184, 0x20000, v176\n"
  "v_xor_b32 v188, 0, %[lb]\n"
  "v_xor_b32 v177, 64, %[la]\n"
  "v_add_u32 v181, 0x10000, v177\n"
  "v_add_u32 v185, 0x20000, v177\n"
  "v_xor_b32 v189, 32, %[lb]\n"
  "v_xor_b32 v178, 128, %[la]\n"
  "v_add_u32 v182, 0x10000, v178\n"
  "v_add_u32 v186, 0x20000, v178\n"
  "v_xor_b32 v190, 64, %[lb]\n"
  "v_xor_b32 v179, 192, %[la]\n"
  "v_add_u32 v183, 0x10000, v179\n"
  "v_add_u32 v187, 0x20000, v179\n"
  "v_xor_b32 v191, 96, %[lb]\n"
  "s_waitcnt vmcnt(4) lgkmcnt(0)\n"
  "s_barrier\n"
  "ds_read_b128 v[192:195], %[ll]\n"
  "ds_read_b128 v[196:199], %[ll] offset:4096\n"
  "ds_read_b64_tr_b16 v[0:1], v188 offset:0\n"
  "ds_read_b64_tr_b16 v[2:3], v188 offset:1024\n"
  "ds_read_b64_tr_b16 v[32:33], v189 offset:0\n"
  "ds_read_b64_tr_b16 v[34:35], v189 offset:1024\n"
  "ds_read_b64_tr_b16 v[4:5], v188 offset:8192\n"
  "ds_read_b64_tr_b16 v[6:7], v188 offset:9216\n"
  "ds_read_b64_tr_b16 v[36:37], v189 offset:8192\n"
  "ds_read_b64_tr_b16 v[38:39], v189 offset:9216\n"
  "s_waitcnt lgkmcnt(4)\n"
  "ds_read_b64_tr_b16 v[8:9], v188 offset:16384\n"
  "ds_read_b64_tr_b16 v[10:11], v188 offset:17408\n"
  "ds_read_b64_tr_b16 v[40:41], v189 offset:16384\n"
  "ds_read_b64_tr_b16 v[42:43], v189 offset:17408\n"
  "ds_read_b64_tr_b16 v[12:13], v188 offset:24576\n"
  "ds_read_b64_tr_b16 v[14:15], v188 offset:25600\n"
  "ds_read_b64_tr_b16 v[44:45], v189 offset:24576\n"
  "ds_read_b64_tr_b16 v[46:47], v189 offset:25600\n"
  "s_waitcnt lgkmcnt(4)\n"
  "ds_read_b64_tr_b16 v[16:17], v188 offset:32768\n"
  "ds_read_b64_tr_b16 v[18:19], v188 offset:33792\n"
  "ds_read_b64_tr_b16 v[48:49], v189 offset:32768\n"
  "ds_read_b64_tr_b16 v[50:51], v189 offset:33792\n"
  "ds_read_b64_tr_b16 v[20:21], v188 offset:40960\n"
  "ds_read_b64_tr_b16 v[22:23], v188 offset:41984\n"
  "ds_read_b64_tr_b16 v[52:53], v189 offset:40960\n"
  "ds_read_b64_tr_b16 v[54:55], v189 offset:41984\n"
  "s_waitcnt lgkmcnt(4)\n"
  "ds_read_b64_tr_b16 v[24:25], v188 offset:49152\n"
  "ds_read_b64_tr_b16 v[26:27], v188 offset:50176\n"
  "ds_read_b64_tr_b16 v[56:57], v189 offset:49152\n"
  "ds_read_b64_tr_b16 v[58:59], v189 offset:50176\n"
  "ds_read_b64_tr_b16 v[28:29], v188 offset:57344\n"
  "ds_read_b64_tr_b16 v[30:31], v188 offset:58368\n"
  "ds_read_b64_tr_b16 v[60:61], v189 offset:57344\n"
  "ds_read_b64_tr_b16 v[62:63], v189 offset:58368\n"
  "s_waitcnt lgkmcnt(4)\n"
  "s_cmp_eq_u32 %[wb], 3072\n"
  "s_cbranch_scc1 L3_%=\n"
  "ds_read_b128 v[96:99], v180 offset:0\n"
  "ds_read_b128 v[100:103], v180 offset:8192\n"
  "ds_read_b128 v[104:107], v181 offset:0\n"
  "ds_read_b128 v[108:111], v181 offset:8192\n"
  "s_waitcnt lgkmcnt(3)\n"
  "v_mfma_f32_16x16x32_bf16 v[64:67], v[96:99], v[0:3], 0\n"
  "ds_read_b128 v[112:115], v182 offset:0\n"
  "v_mfma_f32_16x16x32_bf16 v[72:75], v[96:99], v[32:35], 0\n"
  "s_waitcnt lgkmcnt(3)\n"
  "v_mfma_f32_16x16x32_bf16 v[68:71], v[100:103], v[0:3], 0\n"
  "ds_read_b128 v[116:119], v182 offset:8192\n"
  "v_mfma_f32_16x16x32_bf16 v[76:79], v[100:103], v[32:35], 0\n"
  "s_add_u32 m0, %[wb], 98304\n"
  "s_add_u32 s40, s58, 0\n"
  "buffer_load_dwordx4 %[sa0], %[ra], s40 offen lds\n"
  "s_waitcnt lgkmcnt(3)\n"
  "v_mfma_f32_16x16x32_bf16 v[64:67], v[104:107], v[4:7], v[64:67]\n"
  "ds_read_b128 v[96:99], v183 offset:0\n"
  "v_mfma_f32_16x16x32_bf16 v[72:75], v[104:107], v[36:39], v[72:75]\n"
  "s_waitcnt lgkmcnt(3)\n"
  "v_mfma_f32_16x16x32_bf16 v[68:71], v[108:111], v[4:7], v[68:71]\n"
  "ds_read_b128 v[100:103], v183 offset:8192\n"
  "v_mfma_f32_16x16x32_bf16 v[76:79], v[108:111], v[36:39], v[76:79]\n"
  "s_add_u32 m0, %[wb], 102400\n"
  "s_add_u32 s40, s58, 131072\n"
  "buffer_load_dwordx4 %[sa1], %[ra], s40 offen lds\n"
  "s_waitcnt lgkmcnt(3)\n"
  "v_mfma_f32_16x16x32_bf16 v[64:67], v[112:115], v[8:11], v[64:67]\n"
  "ds_read_b128 v[104:107], v180 offset:256\n"
  "v_mfma_f32_16x16x32_bf16 v[72:75], v[112:115], v[40:43], v[72:75]\n"
  "s_waitcnt lgkmcnt(3)\n"
  "v_mfma_f32_16x16x32_bf16 v[68:71], v[116:119], v[8:11], v[68:71]\n"
  "ds_read_b128 v[108:111], v180 offset:8448\n"
  "v_mfma_f32_16x16x32_bf16 v[76:79], v[116:119], v[40:43], v[76:79]\n"
  "s_add_u32 m0, %[wb], 106496\n"
  "s_add_u32 s40, s58, 32768\n"
  "buffer_load_dwordx4 %[sa0], %[ra], s40 offen lds\n"
  "s_waitcnt lgkmcnt(3)\n"
  "v_mfma_f32_16x16x32_bf16 v[64:67], v[96:99], v[12:15], v[64:67]\n"
  "ds_read_b128 v[112:115], v181 offset:256\n"
  "v_mfma_f32_16x16x32_bf16 v[72:75], v[96:99], v[44:47], v[72:75]\n"
  "s_waitcnt lgkmcnt(3)\n"
  "v_mfma_f32_16x16x32_bf16 v[68:71], v[100:103], v[12:15], v[68:71]\n"
  "ds_read_b128 v[116:119], v181 offset:8448\n"
  "v_mfma_f32_16x16x32_bf16 v[76:79], v[100:103], v[44:47], v[76:79]\n"
  "s_add_u32 m0, %[wb], 110592\n"
  "s_add_u32 s40, s58, 163840\n"
  "buffer_load_dwordx4 %[sa1], %[ra], s40 offen lds\n"
  "s_waitcnt lgkmcnt(3)\n"
  "v_mfma_f32_16x16x32_bf16 v[64:67], v[104:107], v[16:19], v[64:67]\n"
  "ds_read_b128 v[96:99], v182 offset:256\n"
  "v_mfma_f32_16x16x32_bf16 v[72:75], v[104:107], v[48:51], v[72:75]\n"
  "s_waitcnt lgkmcnt(3)\n"
  "v_mfma_f32_16x16x32_bf16 v[68:71], v[108:111], v[16:19], v[68:71]\n"
  "ds_read_b128 v[100:103], v182 offset:8448\n"
  "v_mfma_f32_16x16x32_bf16 v[76:79], v[108:111], v[48:51], v[76:79]\n"
  "s_add_u32 m0, %[wb], 114688\n"
  "s_add_u32 s40, s59, 0\n"
  "buffer_load_dwordx4 %[sa0], %[ra], s40 offen lds\n"
  "s_waitcnt lgkmcnt(3)\n"
  "v_mfma_f32_16x16x32_bf16 v[64:67], v[112:115], v[20:23], v[64:67]\n"
  "ds_read_b128 v[104:107], v183 offset:256\n"
  "v_mfma_f32_16x16x32_bf16 v[72:75], v[112:115], v[52:55], v[72:75]\n"
  "s_waitcnt lgkmcnt(3)\n"
  "v_mfma_f32_16x16x32_bf16 v[68:71], v[116:119], v[20:23], v[68:71]\n"
  "ds_read_b128 v[108:111], v183 offset:8448\n"
  "v_mfma_f32_16x16x32_bf16 v[76:79], v[116:119], v[52:55], v[76:79]\n"
  "s_add_u32 m0, %[wb], 118784\n"
  "s_add_u32 s40, s59, 131072\n"
  "buffer_load_dwordx4 %[sa1], %[ra], s40 offen lds\n"
  "s_waitcnt vmcnt(6)\n"
  "s_barrier\n"
  "s_waitcnt lgkmcnt(3)\n"
  "v_mfma_f32_16x16x32_bf16 v[64:67], v[96:99], v[24:27], v[64:67]\n"
  "ds_read_b128 v[112:115], v180 offset:16384\n"
  "v_mfma_f32_16x16x32_bf16 v[72:75], v[96:99], v[56:59], v[72:75]\n"
  "s_waitcnt lgkmcnt(3)\n"
  "v_mfma_f32_16x16x32_bf16 v[68:71], v[100:103], v[24:27], v[68:71]\n"
  "ds_read_b128 v[116:119], v180 offset:24576\n"
  "v_mfma_f32_16x16x32_bf16 v[76:79], v[100:103], v[56:59], v[76:79]\n"
  "s_add_u32 m0, %[wb], 122880\n"
  "s_add_u32 s40, s59, 32768\n"
  "buffer_load_dwordx4 %[sa0], %[ra], s40 offen lds\n"
  "s_waitcnt lgkmcnt(3)\n"
  "v_mfma_f32_16x16x32_bf16 v[64:67], v[104:107], v[28:31], v[64:67]\n"
  "ds_read_b128 v[96:99], v181 offset:16384\n"
  "v_mfma_f32_16x16x32_bf16 v[72:75], v[104:107], v[60:63], v[72:75]\n"
  "s_waitcnt lgkmcnt(3)\n"
  "v_mfma_f32_16x16x32_bf16 v[68:71], v[108:111], v[28:31], v[68:71]\n"
  "ds_read_b128 v[100:103], v181 offset:24576\n"
  "v_mfma_f32_16x16x32_bf16 v[76:79], v[108:111], v[60:63], v[76:79]\n"
  "s_add_u32 m0, %[wb], 126976\n"
  "s_add_u32 s40, s59, 163840\n"
  "buffer_load_dwordx4 %[sa1], %[ra], s40 offen lds\n"
  "s_waitcnt lgkmcnt(3)\n"
  "v_mfma_f32_16x16x32_bf16 v[80:83], v[112:115], v[0:3], 0\n"
  "ds_read_b128 v[104:107], v182 offset:16384\n"
  "v_mfma_f32_16x16x32_bf16 v[88:91], v[112:115], v[32:35], 0\n"
  "s_waitcnt lgkmcnt(3)\n"
  "v_mfma_f32_16x16x32_bf16 v[84:87], v[116:119], v[0:3], 0\n"
  "ds_read_b128 v[108:111], v182 offset:24576\n"
  "v_mfma_f32_16x16x32_bf16 v[92:95], v[116:119], v[32:35], 0\n"
  "v_cvt_pk_bf16_f32 v120, v64, v65\n"
  "v_cvt_pk_bf16_f32 v121, v66, v67\n"
  "s_add_u32 m0, %[wb], 131072\n"
  "s_add_u32 s40, s60, 0\n"
  "buffer_load_dwordx4 %[sa0], %[ra], s40 offen lds\n"
  "s_waitcnt lgkmcnt(3)\n"
  "v_mfma_f32_16x16x32_bf16 v[80:83], v[96:99], v[4:7], v[80:83]\n"
  "ds_read_b128 v[112:115], v183 offset:16384\n"
  "v_cvt_pk_bf16_f32 v122, v68, v69\n"
  "v_cvt_pk_bf16_f32 v123, v70, v71\n"
  "v_mfma_f32_16x16x32_bf16 v[88:91], v[96:99], v[36:39], v[88:91]\n"
  "v_cvt_pk_bf16_f32 v124, v72, v73\n"
  "v_cvt_pk_bf16_f32 v125, v74, v75\n"
  "s_waitcnt lgkmcnt(3)\n"
  "v_mfma_f32_16x16x32_bf16 v[84:87], v[100:103], v[4:7], v[84:87]\n"
  "ds_read_b128 v[116:119], v183 offset:24576\n"
  "v_cvt_pk_bf16_f32 v126, v76, v77\n"
  "v_cvt_pk_bf16_f32 v127, v78, v79\n"
  "v_mfma_f32_16x16x32_bf16 v[92:95], v[100:103], v[36:39], v[92:95]\n"
  "s_add_u32 m0, %[wb], 135168\n"
  "s_add_u32 s40, s60, 131072\n"
  "buffer_load_dwordx4 %[sa1], %[ra], s40 offen lds\n"
  "s_waitcnt lgkmcnt(3)\n"
  "v_mfma_f32_16x16x32_bf16 v[80:83], v[104:107], v[8:11], v[80:83]\n"
  "ds_read_b128 v[96:99], v180 offset:16640\n"
  "v_mfma_f32_16x16x32_bf16 v[88:91], v[104:107], v[40:43], v[88:91]\n"
  "s_waitcnt lgkmcnt(3)\n"
  "v_mfma_f32_16x16x32_bf16 v[84:87], v[108:111], v[8:11], v[84:87]\n"
  "ds_read_b128 v[100:103], v180 offset:24832\n"
  "v_mfma_f32_16x16x32_bf16 v[92:95], v[108:111], v[40:43], v[92:95]\n"
  "s_add_u32 m0, %[wb], 139264\n"
  "s_add_u32 s40, s60, 32768\n"
  "buffer_load_dwordx4 %[sa0], %[ra], s40 offen lds\n"
  "s_waitcnt lgkmcnt(3)\n"
  "v_mfma_f32_16x16x32_bf16 v[80:83], v[112:115], v[12:15], v[80:83]\n"
  "ds_read_b128 v[104:107], v181 offset:16640\n"
  "v_mfma_f32_16x16x32_bf16 v[88:91], v[112:115], v[44:47], v[88:91]\n"
  "s_waitcnt lgkmcnt(3)\n"
  "v_mfma_f32_16x16x32_bf16 v[84:87], v[116:119], v[12:15], v[84:87]\n"
  "ds_read_b128 v[108:111], v181 offset:24832\n"
  "v_mfma_f32_16x16x32_bf16 v[92:95], v[116:119], v[44:47], v[92:95]\n"
  "s_add_u32 m0, %[wb], 143360\n"
  "s_add_u32 s40, s60, 163840\n"
  "buffer_load_dwordx4 %[sa1], %[ra], s40 offen lds\n"
  "s_waitcnt lgkmcnt(3)\n"
  "v_mfma_f32_16x16x32_bf16 v[80:83], v[96:99], v[16:19], v[80:83]\n"
  "ds_read_b128 v[112:115], v182 offset:16640\n"
  "v_mfma_f32_16x16x32_bf16 v[88:91], v[96:99], v[48:51], v[88:91]\n"
  "s_waitcnt lgkmcnt(3)\n"
  "v_mfma_f32_16x16x32_bf16 v[84:87], v[100:103], v[16:19], v[84:87]\n"
  "ds_read_b128 v[116:119], v182 offset:24832\n"
  "v_mfma_f32_16x16x32_bf16 v[92:95], v[100:103], v[48:51], v[92:95]\n"
  "s_add_u32 m0, %[wb], 0\n"
  "s_add_u32 s40, s61, 0\n"
  "buffer_load_dwordx4 %[sa0], %[ra], s40 offen lds\n"
  "s_waitcnt lgkmcnt(3)\n"
  "v_mfma_f32_16x16x32_bf16 v[80:83], v[104:107], v[20:23], v[80:83]\n"
  "ds_read_b128 v[96:99], v183 offset:16640\n"
  "v_mfma_f32_16x16x32_bf16 v[88:91], v[104:107], v[52:55], v[88:91]\n"
  "s_waitcnt lgkmcnt(3)\n"
  "v_mfma_f32_16x16x32_bf16 v[84:87], v[108:111], v[20:23], v[84:87]\n"
  "ds_read_b128 v[100:103], v183 offset:24832\n"
  "v_mfma_f32_16x16x32_bf16 v[92:95], v[108:111], v[52:55], v[92:95]\n"
  "s_add_u32 m0, %[wb], 4096\n"
  "s_add_u32 s40, s61, 131072\n"
  "buffer_load_dwordx4 %[sa1], %[ra], s40 offen lds\n"
  "s_waitcnt vmcnt(10)\n"
  "s_barrier\n"
  "s_waitcnt lgkmcnt(3)\n"
  "v_mfma_f32_16x16x32_bf16 v[80:83], v[112:115], v[24:27], v[80:83]\n"
  "ds_read_b128 v[104:107], v180 offset:32768\n"
  "v_mfma_f32_16x16x32_bf16 v[88:91], v[112:115], v[56:59], v[88:91]\n"
  "s_waitcnt lgkmcnt(3)\n"
  "v_mfma_f32_16x16x32_bf16 v[84:87], v[116:119], v[24:27], v[84:87]\n"
  "ds_read_b128 v[108:111], v180 offset:40960\n"
  "v_mfma_f32_16x16x32_bf16 v[92:95], v[116:119], v[56:59], v[92:95]\n"
  "s_add_u32 m0, %[wb], 8192\n"
  "s_add_u32 s40, s61, 32768\n"
  "buffer_load_dwordx4 %[sa0], %[ra], s40 offen lds\n"
  "s_waitcnt lgkmcnt(3)\n"
  "v_mfma_f32_16x16x32_bf16 v[80:83], v[96:99], v[28:31], v[80:83]\n"
  "ds_read_b128 v[112:115], v181 offset:32768\n"
  "v_mfma_f32_16x16x32_bf16 v[88:91], v[96:99], v[60:63], v[88:91]\n"
  "s_waitcnt lgkmcnt(3)\n"
  "v_mfma_f32_16x16x32_bf16 v[84:87], v[100:103], v[28:31], v[84:87]\n"
  "ds_read_b128 v[116:119], v181 offset:40960\n"
  "v_mfma_f32_16x16x32_bf16 v[92:95], v[100:103], v[60:63], v[92:95]\n"
  "s_add_u32 m0, %[wb], 12288\n"
  "s_add_u32 s40, s61, 163840\n"
  "buffer_load_dwordx4 %[sa1], %[ra], s40 offen lds\n"
  "s_waitcnt lgkmcnt(3)\n"
  "v_mfma_f32_16x16x32_bf16 v[64:67], v[104:107], v[0:3], 0\n"
  "ds_read_b128 v[96:99], v182 offset:32768\n"
  "v_mfma_f32_16x16x32_bf16 v[72:75], v[104:107], v[32:35], 0\n"
  "s_waitcnt lgkmcnt(3)\n"
  "v_mfma_f32_16x16x32_bf16 v[68:71], v[108:111], v[0:3], 0\n"
  "ds_read_b128 v[100:103], v182 offset:40960\n"
  "v_mfma_f32_16x16x32_bf16 v[76:79], v[108:111], v[32:35], 0\n"
  "v_cvt_pk_bf16_f32 v128, v80, v81\n"
  "v_cvt_pk_bf16_f32 v129, v82, v83\n"
  "s_add_u32 m0, %[wb], 16384\n"
  "s_add_u32 s40, s62, 0\n"
  "buffer_load_dwordx4 %[sa0], %[ra], s40 offen lds\n"
  "s_waitcnt lgkmcnt(3)\n"
  "v_mfma_f32_16x16x32_bf16 v[64:67], v[112:115], v[4:7], v[64:67]\n"
  "ds_read_b128 v[104:107], v183 offset:32768\n"
  "v_cvt_pk_bf16_f32 v130, v84, v85\n"
  "v_cvt_pk_bf16_f32 v131, v86, v87\n"
  "v_mfma_f32_16x16x32_bf16 v[72:75], v[112:115], v[36:39], v[72:75]\n"
  "v_cvt_pk_bf16_f32 v132, v88, v89\n"
  "v_cvt_pk_bf16_f32 v133, v90, v91\n"
  "s_waitcnt lgkmcnt(3)\n"
  "v_mfma_f32_16x16x32_bf16 v[68:71], v[116:119], v[4:7], v[68:71]\n"
  "ds_read_b128 v[108:111], v183 offset:40960\n"
  "v_cvt_pk_bf16_f32 v134, v92, v93\n"
  "v_cvt_pk_bf16_f32 v135, v94, v95\n"
  "v_mfma_f32_16x16x32_bf16 v[76:79], v[116:119], v[36:39], v[76:79]\n"
  "s_add_u32 m0, %[wb], 20480\n"
  "s_add_u32 s40, s62, 131072\n"
  "buffer_load_dwordx4 %[sa1], %[ra], s40 offen lds\n"
  "s_waitcnt lgkmcnt(3)\n"
  "v_mfma_f32_16x16x32_bf16 v[64:67], v[96:99], v[8:11], v[64:67]\n"
  "ds_read_b128 v[112:115], v180 offset:33024\n"
  "v_mfma_f32_16x16x32_bf16 v[72:75], v[96:99], v[40:43], v[72:75]\n"
  "s_waitcnt lgkmcnt(3)\n"
  "v_mfma_f32_16x16x32_bf16 v[68:71], v[100:103], v[8:11], v[68:71]\n"
  "ds_read_b128 v[116:119], v180 offset:41216\n"
  "v_mfma_f32_16x16x32_bf16 v[76:79], v[100:103], v[40:43], v[76:79]\n"
  "s_add_u32 m0, %[wb], 24576\n"
  "s_add_u32 s40, s62, 32768\n"
  "buffer_load_dwordx4 %[sa0], %[ra], s40 offen lds\n"
  "s_waitcnt lgkmcnt(3)\n"
  "v_mfma_f32_16x16x32_bf16 v[64:67], v[104:107], v[12:15], v[64:67]\n"
  "ds_read_b128 v[96:99], v181 offset:33024\n"
  "v_mfma_f32_16x16x32_bf16 v[72:75], v[104:107], v[44:47], v[72:75]\n"
  "s_waitcnt lgkmcnt(3)\n"
  "v_mfma_f32_16x16x32_bf16 v[68:71], v[108:111], v[12:15], v[68:71]\n"
  "ds_read_b128 v[100:103], v181 offset:41216\n"
  "v_mfma_f32_16x16x32_bf16 v[76:79], v[108:111], v[44:47], v[76:79]\n"
  "s_add_u32 m0, %[wb], 28672\n"
  "s_add_u32 s40, s62, 163840\n"
  "buffer_load_dwordx4 %[sa1], %[ra], s40 offen lds\n"
  "s_waitcnt lgkmcnt(3)\n"
  "v_mfma_f32_16x16x32_bf16 v[64:67], v[112:115], v[16:19], v[64:67]\n"
  "ds_read_b128 v[104:107], v182 offset:33024\n"
  "v_mfma_f32_16x16x32_bf16 v[72:75], v[112:115], v[48:51], v[72:75]\n"
  "s_waitcnt lgkmcnt(3)\n"
  "v_mfma_f32_16x16x32_bf16 v[68:71], v[116:119], v[16:19], v[68:71]\n"
  "ds_read_b128 v[108:111], v182 offset:41216\n"
  "v_mfma_f32_16x16x32_bf16 v[76:79], v[116:119], v[48:51], v[76:79]\n"
  "buffer_store_dwordx4 v[192:195], %[x0], %[ws], 0 offen sc1\n"
  "s_waitcnt lgkmcnt(3)\n"
  "v_mfma_f32_16x16x32_bf16 v[64:67], v[96:99], v[20:23], v[64:67]\n"
  "ds_read_b128 v[112:115], v183 offset:33024\n"
  "v_mfma_f32_16x16x32_bf16 v[72:75], v[96:99], v[52:55], v[72:75]\n"
  "s_waitcnt lgkmcnt(3)\n"
  "v_mfma_f32_16x16x32_bf16 v[68:71], v[100:103], v[20:23], v[68:71]\n"
  "ds_read_b128 v[116:119], v183 offset:41216\n"
  "v_mfma_f32_16x16x32_bf16 v[76:79], v[100:103], v[52:55], v[76:79]\n"
  "buffer_store_dwordx4 v[196:199], %[x1], %[ws], 0 offen sc1\n"
  "s_waitcnt vmcnt(14)\n"
  "s_barrier\n"
  "s_waitcnt lgkmcnt(3)\n"
  "v_mfma_f32_16x16x32_bf16 v[64:67], v[104:107], v[24:27], v[64:67]\n"
  "ds_read_b128 v[96:99], v180 offset:49152\n"
  "v_mfma_f32_16x16x32_bf16 v[72:75], v[104:107], v[56:59], v[72:75]\n"
  "s_waitcnt lgkmcnt(3)\n"
  "v_mfma_f32_16x16x32_bf16 v[68:71], v[108:111], v[24:27], v[68:71]\n"
  "ds_read_b128 v[100:103], v180 offset:57344\n"
  "v_mfma_f32_16x16x32_bf16 v[76:79], v[108:111], v[56:59], v[76:79]\n"
  "s_lshr_b32 s40, s56, 8\n"
  "buffer_store_dwordx4 v[120:123], %[o0], %[ws], s40 offen sc1\n"
  "s_waitcnt lgkmcnt(3)\n"
  "v_mfma_f32_16x16x32_bf16 v[64:67], v[112:115], v[28:31], v[64:67]\n"
  "ds_read_b128 v[104:107], v181 offset:49152\n"
  "v_mfma_f32_16x16x32_bf16 v[72:75], v[112:115], v[60:63], v[72:75]\n"
  "s_waitcnt lgkmcnt(3)\n"
  "v_mfma_f32_16x16x32_bf16 v[68:71], v[116:119], v[28:31], v[68:71]\n"
  "ds_read_b128 v[108:111], v181 offset:57344\n"
  "v_mfma_f32_16x16x32_bf16 v[76:79], v[116:119], v[60:63], v[76:79]\n"
  "s_lshr_b32 s40, s56, 8\n"
  "s_add_u32 s40, s40, 14336\n"
  "buffer_store_dwordx4 v[124:127], %[o0], %[ws], s40 offen sc1\n"
  "s_waitcnt lgkmcnt(3)\n"
  "v_mfma_f32_16x16x32_bf16 v[80:83], v[96:99], v[0:3], 0\n"
  "ds_read_b128 v[112:115], v182 offset:49152\n"
  "v_mfma_f32_16x16x32_bf16 v[88:91], v[96:99], v[32:35], 0\n"
  "s_waitcnt lgkmcnt(3)\n"
  "v_mfma_f32_16x16x32_bf16 v[84:87], v[100:103], v[0:3], 0\n"
  "ds_read_b128 v[116:119], v182 offset:57344\n"
  "v_mfma_f32_16x16x32_bf16 v[92:95], v[100:103], v[32:35], 0\n"
  "v_cvt_pk_bf16_f32 v136, v64, v65\n"
  "v_cvt_pk_bf16_f32 v137, v66, v67\n"
  "s_lshr_b32 s40, s57, 8\n"
  "buffer_store_dwordx4 v[128:131], %[o0], %[ws], s40 offen sc1\n"
  "s_waitcnt lgkmcnt(3)\n"
  "v_mfma_f32_16x16x32_bf16 v[80:83], v[104:107], v[4:7], v[80:83]\n"
  "ds_read_b128 v[96:99], v183 offset:49152\n"
  "v_cvt_pk_bf16_f32 v138, v68, v69\n"
  "v_cvt_pk_bf16_f32 v139, v70, v71\n"
  "v_mfma_f32_16x16x32_bf16 v[88:91], v[104:107], v[36:39], v[88:91]\n"
  "v_cvt_pk_bf16_f32 v140, v72, v73\n"
  "v_cvt_pk_bf16_f32 v141, v74, v75\n"
  "s_waitcnt lgkmcnt(3)\n"
  "v_mfma_f32_16x16x32_bf16 v[84:87], v[108:111], v[4:7], v[84:87]\n"
  "ds_read_b128 v[100:103], v183 offset:57344\n"
  "v_cvt_pk_bf16_f32 v142, v76, v77\n"
  "v_cvt_pk_bf16_f32 v143, v78, v79\n"
  "v_mfma_f32_16x16x32_bf16 v[92:95], v[108:111], v[36:39], v[92:95]\n"
  "s_lshr_b32 s40, s57, 8\n"
  "s_add_u32 s40, s40, 14336\n"
  "buffer_store_dwordx4 v[132:135], %[o0], %[ws], s40 offen sc1\n"
  "s_waitcnt lgkmcnt(3)\n"
  "v_mfma_f32_16x16x32_bf16 v[80:83], v[112:115], v[8:11], v[80:83]\n"
  "ds_read_b128 v[104:107], v180 offset:49408\n"
  "v_mfma_f32_16x16x32_bf16 v[88:91], v[112:115], v[40:43], v[88:91]\n"
  "s_waitcnt lgkmcnt(3)\n"
  "v_mfma_f32_16x16x32_bf16 v[84:87], v[116:119], v[8:11], v[84:87]\n"
  "ds_read_b128 v[108:111], v180 offset:57600\n"
  "v_mfma_f32_16x16x32_bf16 v[92:95], v[116:119], v[40:43], v[92:95]\n"
  "s_lshr_b32 s40, s58, 8\n"
  "buffer_store_dwordx4 v[136:139], %[o0], %[ws], s40 offen sc1\n"
  "s_waitcnt lgkmcnt(3)\n"
  "v_mfma_f32_16x16x32_bf16 v[80:83], v[96:99], v[12:15], v[80:83]\n"
  "ds_read_b128 v[112:115], v181 offset:49408\n"
  "v_mfma_f32_16x16x32_bf16 v[88:91], v[96:99], v[44:47], v[88:91]\n"
  "s_waitcnt lgkmcnt(3)\n"
  "v_mfma_f32_16x16x32_bf16 v[84:87], v[100:103], v[12:15], v[84:87]\n"
  "ds_read_b128 v[116:119], v181 offset:57600\n"
  "v_mfma_f32_16x16x32_bf16 v[92:95], v[100:103], v[44:47], v[92:95]\n"
  "s_lshr_b32 s40, s58, 8\n"
  "s_add_u32 s40, s40, 14336\n"
  "buffer_store_dwordx4 v[140:143], %[o0], %[ws], s40 offen sc1\n"
  "s_waitcnt lgkmcnt(3)\n"
  "v_mfma_f32_16x16x32_bf16 v[80:83], v[104:107], v[16:19], v[80:83]\n"
  "ds_read_b128 v[96:99], v182 offset:49408\n"
  "v_mfma_f32_16x16x32_bf16 v[88:91], v[104:107], v[48:51], v[88:91]\n"
  "s_waitcnt lgkmcnt(3)\n"
  "v_mfma_f32_16x16x32_bf16 v[84:87], v[108:111], v[16:19], v[84:87]\n"
  "ds_read_b128 v[100:103], v182 offset:57600\n"
  "v_mfma_f32_16x16x32_bf16 v[92:95], v[108:111], v[48:51], v[92:95]\n"
  "s_waitcnt lgkmcnt(3)\n"
  "v_mfma_f32_16x16x32_bf16 v[80:83], v[112:115], v[20:23], v[80:83]\n"
  "ds_read_b128 v[104:107], v183 offset:49408\n"
  "v_mfma_f32_16x16x32_bf16 v[88:91], v[112:115], v[52:55], v[88:91]\n"
  "s_waitcnt lgkmcnt(3)\n"
  "v_mfma_f32_16x16x32_bf16 v[84:87], v[116:119], v[20:23], v[84:87]\n"
  "ds_read_b128 v[108:111], v183 offset:57600\n"
  "v_mfma_f32_16x16x32_bf16 v[92:95], v[116:119], v[52:55], v[92:95]\n"
  "s_waitcnt vmcnt(16)\n"
  "s_barrier\n"
  "s_waitcnt lgkmcnt(3)\n"
  "v_mfma_f32_16x16x32_bf16 v[80:83], v[96:99], v[24:27], v[80:83]\n"
  "ds_read_b128 v[112:115], v184 offset:0\n"
  "v_mfma_f32_16x16x32_bf16 v[88:91], v[96:99], v[56:59], v[88:91]\n"
  "s_waitcnt lgkmcnt(3)\n"
  "v_mfma_f32_16x16x32_bf16 v[84:87], v[100:103], v[24:27], v[84:87]\n"
  "ds_read_b128 v[116:119], v184 offset:8192\n"
  "v_mfma_f32_16x16x32_bf16 v[92:95], v[100:103], v[56:59], v[92:95]\n"
  "s_waitcnt lgkmcnt(3)\n"
  "v_mfma_f32_16x16x32_bf16 v[80:83], v[104:107], v[28:31], v[80:83]\n"
  "ds_read_b128 v[96:99], v185 offset:0\n"
  "v_mfma_f32_16x16x32_bf16 v[88:91], v[104:107], v[60:63], v[88:91]\n"
  "s_waitcnt lgkmcnt(3)\n"
  "v_mfma_f32_16x16x32_bf16 v[84:87], v[108:111], v[28:31], v[84:87]\n"
  "ds_read_b128 v[100:103], v185 offset:8192\n"
  "v_mfma_f32_16x16x32_bf16 v[92:95], v[108:111], v[60:63], v[92:95]\n"
  "s_waitcnt lgkmcnt(3)\n"
  "v_mfma_f32_16x16x32_bf16 v[64:67], v[112:115], v[0:3], 0\n"
  "ds_read_b128 v[104:107], v186 offset:0\n"
  "v_mfma_f32_16x16x32_bf16 v[72:75], v[112:115], v[32:35], 0\n"
  "s_waitcnt lgkmcnt(3)\n"
  "v_mfma_f32_16x16x32_bf16 v[68:71], v[116:119], v[0:3], 0\n"
  "ds_read_b128 v[108:111], v186 offset:8192\n"
  "v_mfma_f32_16x16x32_bf16 v[76:79], v[116:119], v[32:35], 0\n"
  "v_cvt_pk_bf16_f32 v144, v80, v81\n"
  "v_cvt_pk_bf16_f32 v145, v82, v83\n"
  "s_waitcnt lgkmcnt(3)\n"
  "v_mfma_f32_16x16x32_bf16 v[64:67], v[96:99], v[4:7], v[64:67]\n"
  "ds_read_b128 v[112:115], v187 offset:0\n"
  "v_cvt_pk_bf16_f32 v146, v84, v85\n"
  "v_cvt_pk_bf16_f32 v147, v86, v87\n"
  "v_mfma_f32_16x16x32_bf16 v[72:75], v[96:99], v[36:39], v[72:75]\n"
  "v_cvt_pk_bf16_f32 v148, v88, v89\n"
  "v_cvt_pk_bf16_f32 v149, v90, v91\n"
  "s_waitcnt lgkmcnt(3)\n"
  "v_mfma_f32_16x16x32_bf16 v[68:71], v[100:103], v[4:7], v[68:71]\n"
  "ds_read_b128 v[116:119], v187 offset:8192\n"
  "v_cvt_pk_bf16_f32 v150, v92, v93\n"
  "v_cvt_pk_bf16_f32 v151, v94, v95\n"
  "v_mfma_f32_16x16x32_bf16 v[76:79], v[100:103], v[36:39], v[76:79]\n"
  "s_waitcnt lgkmcnt(3)\n"
  "v_mfma_f32_16x16x32_bf16 v[64:67], v[104:107], v[8:11], v[64:67]\n"
  "ds_read_b128 v[96:99], v184 offset:256\n"
  "v_mfma_f32_16x16x32_bf16 v[72:75], v[104:107], v[40:43], v[72:75]\n"
  "s_waitcnt lgkmcnt(3)\n"
  "v_mfma_f32_16x16x32_bf16 v[68:71], v[108:111], v[8:11], v[68:71]\n"
  "ds_read_b128 v[100:103], v184 offset:8448\n"
  "v_mfma_f32_16x16x32_bf16 v[76:79], v[108:111], v[40:43], v[76:79]\n"
  "s_lshr_b32 s40, s59, 8\n"
  "buffer_store_dwordx4 v[144:147], %[o0], %[ws], s40 offen sc1\n"
  "s_waitcnt lgkmcnt(3)\n"
  "v_mfma_f32_16x16x32_bf16 v[64:67], v[112:115], v[12:15], v[64:67]\n"
  "ds_read_b128 v[104:107], v185 offset:256\n"
  "v_mfma_f32_16x16x32_bf16 v[72:75], v[112:115], v[44:47], v[72:75]\n"
  "s_waitcnt lgkmcnt(3)\n"
  "v_mfma_f32_16x16x32_bf16 v[68:71], v[116:119], v[12:15], v[68:71]\n"
  "ds_read_b128 v[108:111], v185 offset:8448\n"
  "v_mfma_f32_16x16x32_bf16 v[76:79], v[116:119], v[44:47], v[76:79]\n"
  "s_lshr_b32 s40, s59, 8\n"
  "s_add_u32 s40, s40, 14336\n"
  "buffer_store_dwordx4 v[148:151], %[o0], %[ws], s40 offen sc1\n"
  "s_waitcnt lgkmcnt(3)\n"
  "v_mfma_f32_16x16x32_bf16 v[64:67], v[96:99], v[16:19], v[64:67]\n"
  "ds_read_b128 v[112:115], v186 offset:256\n"
  "v_mfma_f32_16x16x32_bf16 v[72:75], v[96:99], v[48:51], v[72:75]\n"
  "s_waitcnt lgkmcnt(3)\n"
  "v_mfma_f32_16x16x32_bf16 v[68:71], v[100:103], v[16:19], v[68:71]\n"
  "ds_read_b128 v[116:119], v186 offset:8448\n"
  "v_mfma_f32_16x16x32_bf16 v[76:79], v[100:103], v[48:51], v[76:79]\n"
  "s_waitcnt lgkmcnt(3)\n"
  "v_mfma_f32_16x16x32_bf16 v[64:67], v[104:107], v[20:23], v[64:67]\n"
  "ds_read_b128 v[96:99], v187 offset:256\n"
  "v_mfma_f32_16x16x32_bf16 v[72:75], v[104:107], v[52:55], v[72:75]\n"
  "s_waitcnt lgkmcnt(3)\n"
  "v_mfma_f32_16x16x32_bf16 v[68:71], v[108:111], v[20:23], v[68:71]\n"
  "ds_read_b128 v[100:103], v187 offset:8448\n"
  "v_mfma_f32_16x16x32_bf16 v[76:79], v[108:111], v[52:55], v[76:79]\n"
  "s_waitcnt vmcnt(14)\n"
  "s_barrier\n"
  "s_waitcnt lgkmcnt(3)\n"
  "v_mfma_f32_16x16x32_bf16 v[64:67], v[112:115], v[24:27], v[64:67]\n"
  "ds_read_b128 v[104:107], v176 offset:0\n"
  "v_mfma_f32_16x16x32_bf16 v[72:75], v[112:115], v[56:59], v[72:75]\n"
  "s_waitcnt lgkmcnt(3)\n"
  "v_mfma_f32_16x16x32_bf16 v[68:71], v[116:119], v[24:27], v[68:71]\n"
  "ds_read_b128 v[108:111], v176 offset:8192\n"
  "v_mfma_f32_16x16x32_bf16 v[76:79], v[116:119], v[56:59], v[76:79]\n"
  "s_waitcnt lgkmcnt(3)\n"
  "v_mfma_f32_16x16x32_bf16 v[64:67], v[96:99], v[28:31], v[64:67]\n"
  "ds_read_b128 v[112:115], v177 offset:0\n"
  "v_mfma_f32_16x16x32_bf16 v[72:75], v[96:99], v[60:63], v[72:75]\n"
  "s_waitcnt lgkmcnt(3)\n"
  "v_mfma_f32_16x16x32_bf16 v[68:71], v[100:103], v[28:31], v[68:71]\n"
  "ds_read_b128 v[116:119], v177 offset:8192\n"
  "v_mfma_f32_16x16x32_bf16 v[76:79], v[100:103], v[60:63], v[76:79]\n"
  "s_waitcnt lgkmcnt(3)\n"
  "v_mfma_f32_16x16x32_bf16 v[80:83], v[104:107], v[0:3], 0\n"
  "ds_read_b128 v[96:99], v178 offset:0\n"
  "v_mfma_f32_16x16x32_bf16 v[88:91], v[104:107], v[32:35], 0\n"
  "s_waitcnt lgkmcnt(3)\n"
  "v_mfma_f32_16x16x32_bf16 v[84:87], v[108:111], v[0:3], 0\n"
  "ds_read_b128 v[100:103], v178 offset:8192\n"
  "v_mfma_f32_16x16x32_bf16 v[92:95], v[108:111], v[32:35], 0\n"
  "v_cvt_pk_bf16_f32 v152, v64, v65\n"
  "v_cvt_pk_bf16_f32 v153, v66, v67\n"
  "s_waitcnt lgkmcnt(3)\n"
  "v_mfma_f32_16x16x32_bf16 v[80:83], v[112:115], v[4:7], v[80:83]\n"
  "ds_read_b128 v[104:107], v179 offset:0\n"
  "v_cvt_pk_bf16_f32 v154, v68, v69\n"
  "v_cvt_pk_bf16_f32 v155, v70, v71\n"
  "v_mfma_f32_16x16x32_bf16 v[88:91], v[112:115], v[36:39], v[88:91]\n"
  "v_cvt_pk_bf16_f32 v156, v72, v73\n"
  "v_cvt_pk_bf16_f32 v157, v74, v75\n"
  "s_waitcnt lgkmcnt(3)\n"
  "v_mfma_f32_16x16x32_bf16 v[84:87], v[116:119], v[4:7], v[84:87]\n"
  "ds_read_b128 v[108:111], v179 offset:8192\n"
  "v_cvt_pk_bf16_f32 v158, v76, v77\n"
  "v_cvt_pk_bf16_f32 v159, v78, v79\n"
  "v_mfma_f32_16x16x32_bf16 v[92:95], v[116:119], v[36:39], v[92:95]\n"
  "s_waitcnt lgkmcnt(3)\n"
  "v_mfma_f32_16x16x32_bf16 v[80:83], v[96:99], v[8:11], v[80:83]\n"
  "ds_read_b128 v[112:115], v176 offset:256\n"
  "v_mfma_f32_16x16x32_bf16 v[88:91], v[96:99], v[40:43], v[88:91]\n"
  "s_waitcnt lgkmcnt(3)\n"
  "v_mfma_f32_16x16x32_bf16 v[84:87], v[100:103], v[8:11], v[84:87]\n"
  "ds_read_b128 v[116:119], v176 offset:8448\n"
  "v_mfma_f32_16x16x32_bf16 v[92:95], v[100:103], v[40:43], v[92:95]\n"
  "s_lshr_b32 s40, s60, 8\n"
  "buffer_store_dwordx4 v[152:155], %[o0], %[ws], s40 offen sc1\n"
  "s_waitcnt lgkmcnt(3)\n"
  "v_mfma_f32_16x16x32_bf16 v[80:83], v[104:107], v[12:15], v[80:83]\n"
  "ds_read_b128 v[96:99], v177 offset:256\n"
  "v_mfma_f32_16x16x32_bf16 v[88:91], v[104:107], v[44:47], v[88:91]\n"
  "s_waitcnt lgkmcnt(3)\n"
  "v_mfma_f32_16x16x32_bf16 v[84:87], v[108:111], v[12:15], v[84:87]\n"
  "ds_read_b128 v[100:103], v177 offset:8448\n"
  "v_mfma_f32_16x16x32_bf16 v[92:95], v[108:111], v[44:47], v[92:95]\n"
  "s_lshr_b32 s40, s60, 8\n"
  "s_add_u32 s40, s40, 14336\n"
  "buffer_store_dwordx4 v[156:159], %[o0], %[ws], s40 offen sc1\n"
  "s_waitcnt lgkmcnt(3)\n"
  "v_mfma_f32_16x16x32_bf16 v[80:83], v[112:115], v[16:19], v[80:83]\n"
  "ds_read_b128 v[104:107], v178 offset:256\n"
  "v_mfma_f32_16x16x32_bf16 v[88:91], v[112:115], v[48:51], v[88:91]\n"
  "s_waitcnt lgkmcnt(3)\n"
  "v_mfma_f32_16x16x32_bf16 v[84:87], v[116:119], v[16:19], v[84:87]\n"
  "ds_read_b128 v[108:111], v178 offset:8448\n"
  "v_mfma_f32_16x16x32_bf16 v[92:95], v[116:119], v[48:51], v[92:95]\n"
  "s_waitcnt lgkmcnt(3)\n"
  "v_mfma_f32_16x16x32_bf16 v[80:83], v[96:99], v[20:23], v[80:83]\n"
  "ds_read_b128 v[112:115], v179 offset:256\n"
  "v_mfma_f32_16x16x32_bf16 v[88:91], v[96:99], v[52:55], v[88:91]\n"
  "s_waitcnt lgkmcnt(3)\n"
  "v_mfma_f32_16x16x32_bf16 v[84:87], v[100:103], v[20:23], v[84:87]\n"
  "ds_read_b128 v[116:119], v179 offset:8448\n"
  "v_mfma_f32_16x16x32_bf16 v[92:95], v[100:103], v[52:55], v[92:95]\n"
  "s_waitcnt vmcnt(12)\n"
  "s_barrier\n"
  "s_waitcnt lgkmcnt(3)\n"
  "v_mfma_f32_16x16x32_bf16 v[80:83], v[104:107], v[24:27], v[80:83]\n"
  "ds_read_b128 v[96:99], v176 offset:16384\n"
  "v_mfma_f32_16x16x32_bf16 v[88:91], v[104:107], v[56:59], v[88:91]\n"
  "s_waitcnt lgkmcnt(3)\n"
  "v_mfma_f32_16x16x32_bf16 v[84:87], v[108:111], v[24:27], v[84:87]\n"
  "ds_read_b128 v[100:103], v176 offset:24576\n"
  "v_mfma_f32_16x16x32_bf16 v[92:95], v[108:111], v[56:59], v[92:95]\n"
  "s_waitcnt lgkmcnt(3)\n"
  "v_mfma_f32_16x16x32_bf16 v[80:83], v[112:115], v[28:31], v[80:83]\n"
  "ds_read_b128 v[104:107], v177 offset:16384\n"
  "v_mfma_f32_16x16x32_bf16 v[88:91], v[112:115], v[60:63], v[88:91]\n"
  "s_waitcnt lgkmcnt(3)\n"
  "v_mfma_f32_16x16x32_bf16 v[84:87], v[116:119], v[28:31], v[84:87]\n"
  "ds_read_b128 v[108:111], v177 offset:24576\n"
  "v_mfma_f32_16x16x32_bf16 v[92:95], v[116:119], v[60:63], v[92:95]\n"
  "s_waitcnt lgkmcnt(3)\n"
  "v_mfma_f32_16x16x32_bf16 v[64:67], v[96:99], v[0:3], 0\n"
  "ds_read_b128 v[112:115], v178 offset:16384\n"
  "v_mfma_f32_16x16x32_bf16 v[72:75], v[96:99], v[32:35], 0\n"
  "s_waitcnt lgkmcnt(3)\n"
  "v_mfma_f32_16x16x32_bf16 v[68:71], v[100:103], v[0:3], 0\n"
  "ds_read_b128 v[116:119], v178 offset:24576\n"
  "v_mfma_f32_16x16x32_bf16 v[76:79], v[100:103], v[32:35], 0\n"
  "v_cvt_pk_bf16_f32 v160, v80, v81\n"
  "v_cvt_pk_bf16_f32 v161, v82, v83\n"
  "s_waitcnt lgkmcnt(3)\n"
  "v_mfma_f32_16x16x32_bf16 v[64:67], v[104:107], v[4:7], v[64:67]\n"
  "ds_read_b128 v[96:99], v179 offset:16384\n"
  "v_cvt_pk_bf16_f32 v162, v84, v85\n"
  "v_cvt_pk_bf16_f32 v163, v86, v87\n"
  "v_mfma_f32_16x16x32_bf16 v[72:75], v[104:107], v[36:39], v[72:75]\n"
  "v_cvt_pk_bf16_f32 v164, v88, v89\n"
  "v_cvt_pk_bf16_f32 v165, v90, v91\n"
  "s_waitcnt lgkmcnt(3)\n"
  "v_mfma_f32_16x16x32_bf16 v[68:71], v[108:111], v[4:7], v[68:71]\n"
  "ds_read_b128 v[100:103], v179 offset:24576\n"
  "v_cvt_pk_bf16_f32 v166, v92, v93\n"
  "v_cvt_pk_bf16_f32 v167, v94, v95\n"
  "v_mfma_f32_16x16x32_bf16 v[76:79], v[108:111], v[36:39], v[76:79]\n"
  "s_waitcnt lgkmcnt(3)\n"
  "v_mfma_f32_16x16x32_bf16 v[64:67], v[112:115], v[8:11], v[64:67]\n"
  "ds_read_b128 v[104:107], v176 offset:16640\n"
  "v_mfma_f32_16x16x32_bf16 v[72:75], v[112:115], v[40:43], v[72:75]\n"
  "s_waitcnt lgkmcnt(3)\n"
  "v_mfma_f32_16x16x32_bf16 v[68:71], v[116:119], v[8:11], v[68:71]\n"
  "ds_read_b128 v[108:111], v176 offset:24832\n"
  "v_mfma_f32_16x16x32_bf16 v[76:79], v[116:119], v[40:43], v[76:79]\n"
  "s_lshr_b32 s40, s61, 8\n"
  "buffer_store_dwordx4 v[160:163], %[o0], %[ws], s40 offen sc1\n"
  "s_waitcnt lgkmcnt(3)\n"
  "v_mfma_f32_16x16x32_bf16 v[64:67], v[96:99], v[12:15], v[64:67]\n"
  "ds_read_b128 v[112:115], v177 offset:16640\n"
  "v_mfma_f32_16x16x32_bf16 v[72:75], v[96:99], v[44:47], v[72:75]\n"
  "s_waitcnt lgkmcnt(3)\n"
  "v_mfma_f32_16x16x32_bf16 v[68:71], v[100:103], v[12:15], v[68:71]\n"
  "ds_read_b128 v[116:119], v177 offset:24832\n"
  "v_mfma_f32_16x16x32_bf16 v[76:79], v[100:103], v[44:47], v[76:79]\n"
  "s_lshr_b32 s40, s61, 8\n"
  "s_add_u32 s40, s40, 14336\n"
  "buffer_store_dwordx4 v[164:167], %[o0], %[ws], s40 offen sc1\n"
  "s_waitcnt lgkmcnt(3)\n"
  "v_mfma_f32_16x16x32_bf16 v[64:67], v[104:107], v[16:19], v[64:67]\n"
  "ds_read_b128 v[96:99], v178 offset:16640\n"
  "v_mfma_f32_16x16x32_bf16 v[72:75], v[104:107], v[48:51], v[72:75]\n"
  "s_waitcnt lgkmcnt(3)\n"
  "v_mfma_f32_16x16x32_bf16 v[68:71], v[108:111], v[16:19], v[68:71]\n"
  "ds_read_b128 v[100:103], v178 offset:24832\n"
  "v_mfma_f32_16x16x32_bf16 v[76:79], v[108:111], v[48:51], v[76:79]\n"
  "s_waitcnt lgkmcnt(3)\n"
  "v_mfma_f32_16x16x32_bf16 v[64:67], v[112:115], v[20:23], v[64:67]\n"
  "ds_read_b128 v[104:107], v179 offset:16640\n"
  "v_mfma_f32_16x16x32_bf16 v[72:75], v[112:115], v[52:55], v[72:75]\n"
  "s_waitcnt lgkmcnt(3)\n"
  "v_mfma_f32_16x16x32_bf16 v[68:71], v[116:119], v[20:23], v[68:71]\n"
  "ds_read_b128 v[108:111], v179 offset:24832\n"
  "v_mfma_f32_16x16x32_bf16 v[76:79], v[116:119], v[52:55], v[76:79]\n"
  "s_waitcnt lgkmcnt(3)\n"
  "v_mfma_f32_16x16x32_bf16 v[64:67], v[96:99], v[24:27], v[64:67]\n"
  "v_mfma_f32_16x16x32_bf16 v[72:75], v[96:99], v[56:59], v[72:75]\n"
  "s_waitcnt lgkmcnt(2)\n"
  "v_mfma_f32_16x16x32_bf16 v[68:71], v[100:103], v[24:27], v[68:71]\n"
  "v_mfma_f32_16x16x32_bf16 v[76:79], v[100:103], v[56:59], v[76:79]\n"
  "s_waitcnt lgkmcnt(1)\n"
  "v_mfma_f32_16x16x32_bf16 v[64:67], v[104:107], v[28:31], v[64:67]\n"
  "v_mfma_f32_16x16x32_bf16 v[72:75], v[104:107], v[60:63], v[72:75]\n"
  "s_waitcnt lgkmcnt(0)\n"
  "v_mfma_f32_16x16x32_bf16 v[68:71], v[108:111], v[28:31], v[68:71]\n"
  "v_mfma_f32_16x16x32_bf16 v[76:79], v[108:111], v[60:63], v[76:79]\n"
  "s_nop 7\n"
  "v_cvt_pk_bf16_f32 v168, v64, v65\n"
  "v_cvt_pk_bf16_f32 v169, v66, v67\n"
  "v_cvt_pk_bf16_f32 v170, v68, v69\n"
  "v_cvt_pk_bf16_f32 v171, v70, v71\n"
  "v_cvt_pk_bf16_f32 v172, v72, v73\n"
  "v_cvt_pk_bf16_f32 v173, v74, v75\n"
  "v_cvt_pk_bf16_f32 v174, v76, v77\n"
  "v_cvt_pk_bf16_f32 v175, v78, v79\n"
  "s_lshr_b32 s40, s62, 8\n"
  "buffer_store_dwordx4 v[168:171], %[o0], %[ws], s40 offen sc1\n"
  "s_lshr_b32 s40, s62, 8\n"
  "s_add_u32 s40, s40, 14336\n"
  "buffer_store_dwordx4 v[172:175], %[o0], %[ws], s40 offen sc1\n"
  "s_branch Le_%=\n"
  "L3_%=:\n"
  "ds_read_b128 v[96:99], v180 offset:0\n"
  "ds_read_b128 v[100:103], v180 offset:8192\n"
  "ds_read_b128 v[104:107], v181 offset:0\n"
  "ds_read_b128 v[108:111], v181 offset:8192\n"
  "s_waitcnt lgkmcnt(3)\n"
  "v_mfma_f32_16x16x32_bf16 v[64:67], v[96:99], v[0:3], 0\n"
  "ds_read_b128 v[112:115], v182 offset:0\n"
  "s_waitcnt lgkmcnt(3)\n"
  "v_mfma_f32_16x16x32_bf16 v[68:71], v[100:103], v[0:3], 0\n"
  "ds_read_b128 v[116:119], v182 offset:8192\n"
  "s_add_u32 m0, %[wb], 98304\n"
  "s_add_u32 s40, s58, 0\n"
  "buffer_load_dwordx4 %[sa0], %[ra], s40 offen lds\n"
  "s_waitcnt lgkmcnt(3)\n"
  "v_mfma_f32_16x16x32_bf16 v[64:67], v[104:107], v[4:7], v[64:67]\n"
  "ds_read_b128 v[96:99], v183 offset:0\n"
  "s_waitcnt lgkmcnt(3)\n"
  "v_mfma_f32_16x16x32_bf16 v[68:71], v[108:111], v[4:7], v[68:71]\n"
  "ds_read_b128 v[100:103], v183 offset:8192\n"
  "s_add_u32 m0, %[wb], 102400\n"
  "s_add_u32 s40, s58, 131072\n"
  "buffer_load_dwordx4 %[sa1], %[ra], s40 offen lds\n"
  "s_waitcnt lgkmcnt(3)\n"
  "v_mfma_f32_16x16x32_bf16 v[64:67], v[112:115], v[8:11], v[64:67]\n"
  "ds_read_b128 v[104:107], v180 offset:256\n"
  "s_waitcnt lgkmcnt(3)\n"
  "v_mfma_f32_16x16x32_bf16 v[68:71], v[116:119], v[8:11], v[68:71]\n"
  "ds_read_b128 v[108:111], v180 offset:8448\n"
  "s_add_u32 m0, %[wb], 106496\n"
  "s_add_u32 s40, s58, 32768\n"
  "buffer_load_dwordx4 %[sa0], %[ra], s40 offen lds\n"
  "s_waitcnt lgkmcnt(3)\n"
  "v_mfma_f32_16x16x32_bf16 v[64:67], v[96:99], v[12:15], v[64:67]\n"
  "ds_read_b128 v[112:115], v181 offset:256\n"
  "s_waitcnt lgkmcnt(3)\n"
  "v_mfma_f32_16x16x32_bf16 v[68:71], v[100:103], v[12:15], v[68:71]\n"
  "ds_read_b128 v[116:119], v181 offset:8448\n"
  "s_add_u32 m0, %[wb], 110592\n"
  "s_add_u32 s40, s58, 163840\n"
  "buffer_load_dwordx4 %[sa1], %[ra], s40 offen lds\n"
  "s_waitcnt lgkmcnt(3)\n"
  "v_mfma_f32_16x16x32_bf16 v[64:67], v[104:107], v[16:19], v[64:67]\n"
  "ds_read_b128 v[96:99], v182 offset:256\n"
  "s_waitcnt lgkmcnt(3)\n"
  "v_mfma_f32_16x16x32_bf16 v[68:71], v[108:111], v[16:19], v[68:71]\n"
  "ds_read_b128 v[100:103], v182 offset:8448\n"
  "s_add_u32 m0, %[wb], 114688\n"
  "s_add_u32 s40, s59, 0\n"
  "buffer_load_dwordx4 %[sa0], %[ra], s40 offen lds\n"
  "s_waitcnt lgkmcnt(3)\n"
  "v_mfma_f32_16x16x32_bf16 v[64:67], v[112:115], v[20:23], v[64:67]\n"
  "ds_read_b128 v[104:107], v183 offset:256\n"
  "s_waitcnt lgkmcnt(3)\n"
  "v_mfma_f32_16x16x32_bf16 v[68:71], v[116:119], v[20:23], v[68:71]\n"
  "ds_read_b128 v[108:111], v183 offset:8448\n"
  "s_add_u32 m0, %[wb], 118784\n"
  "s_add_u32 s40, s59, 131072\n"
  "buffer_load_dwordx4 %[sa1], %[ra], s40 offen lds\n"
  "s_waitcnt vmcnt(6)\n"
  "s_barrier\n"
  "s_waitcnt lgkmcnt(3)\n"
  "v_mfma_f32_16x16x32_bf16 v[64:67], v[96:99], v[24:27], v[64:67]\n"
  "ds_read_b128 v[112:115], v180 offset:16384\n"
  "s_waitcnt lgkmcnt(3)\n"
  "v_mfma_f32_16x16x32_bf16 v[68:71], v[100:103], v[24:27], v[68:71]\n"
  "ds_read_b128 v[116:119], v180 offset:24576\n"
  "s_add_u32 m0, %[wb], 122880\n"
  "s_add_u32 s40, s59, 32768\n"
  "buffer_load_dwordx4 %[sa0], %[ra], s40 offen lds\n"
  "s_waitcnt lgkmcnt(3)\n"
  "v_mfma_f32_16x16x32_bf16 v[64:67], v[104:107], v[28:31], v[64:67]\n"
  "ds_read_b128 v[96:99], v181 offset:16384\n"
  "s_waitcnt lgkmcnt(3)\n"
  "v_mfma_f32_16x16x32_bf16 v[68:71], v[108:111], v[28:31], v[68:71]\n"
  "ds_read_b128 v[100:103], v181 offset:24576\n"
  "s_add_u32 m0, %[wb], 126976\n"
  "s_add_u32 s40, s59, 163840\n"
  "buffer_load_dwordx4 %[sa1], %[ra], s40 offen lds\n"
  "s_waitcnt lgkmcnt(3)\n"
  "v_mfma_f32_16x16x32_bf16 v[80:83], v[112:115], v[0:3], 0\n"
  "ds_read_b128 v[104:107], v182 offset:16384\n"
  "s_waitcnt lgkmcnt(3)\n"
  "v_mfma_f32_16x16x32_bf16 v[84:87], v[116:119], v[0:3], 0\n"
  "ds_read_b128 v[108:111], v182 offset:24576\n"
  "s_add_u32 m0, %[wb], 131072\n"
  "s_add_u32 s40, s60, 0\n"
  "buffer_load_dwordx4 %[sa0], %[ra], s40 offen lds\n"
  "s_waitcnt lgkmcnt(3)\n"
  "v_mfma_f32_16x16x32_bf16 v[80:83], v[96:99], v[4:7], v[80:83]\n"
  "ds_read_b128 v[112:115], v183 offset:16384\n"
  "s_waitcnt lgkmcnt(3)\n"
  "v_mfma_f32_16x16x32_bf16 v[84:87], v[100:103], v[4:7], v[84:87]\n"
  "ds_read_b128 v[116:119], v183 offset:24576\n"
  "v_cvt_pk_bf16_f32 v120, v64, v65\n"
  "v_cvt_pk_bf16_f32 v121, v66, v67\n"
  "s_add_u32 m0, %[wb], 135168\n"
  "s_add_u32 s40, s60, 131072\n"
  "buffer_load_dwordx4 %[sa1], %[ra], s40 offen lds\n"
  "s_waitcnt lgkmcnt(3)\n"
  "v_mfma_f32_16x16x32_bf16 v[80:83], v[104:107], v[8:11], v[80:83]\n"
  "ds_read_b128 v[96:99], v180 offset:16640\n"
  "v_cvt_pk_bf16_f32 v122, v68, v69\n"
  "v_cvt_pk_bf16_f32 v123, v70, v71\n"
  "s_waitcnt lgkmcnt(3)\n"
  "v_mfma_f32_16x16x32_bf16 v[84:87], v[108:111], v[8:11], v[84:87]\n"
  "ds_read_b128 v[100:103], v180 offset:24832\n"
  "s_add_u32 m0, %[wb], 139264\n"
  "s_add_u32 s40, s60, 32768\n"
  "buffer_load_dwordx4 %[sa0], %[ra], s40 offen lds\n"
  "s_waitcnt lgkmcnt(3)\n"
  "v_mfma_f32_16x16x32_bf16 v[80:83], v[112:115], v[12:15], v[80:83]\n"
  "ds_read_b128 v[104:107], v181 offset:16640\n"
  "s_waitcnt lgkmcnt(3)\n"
  "v_mfma_f32_16x16x32_bf16 v[84:87], v[116:119], v[12:15], v[84:87]\n"
  "ds_read_b128 v[108:111], v181 offset:24832\n"
  "s_add_u32 m0, %[wb], 143360\n"
  "s_add_u32 s40, s60, 163840\n"
  "buffer_load_dwordx4 %[sa1], %[ra], s40 offen lds\n"
  "s_waitcnt lgkmcnt(3)\n"
  "v_mfma_f32_16x16x32_bf16 v[80:83], v[96:99], v[16:19], v[80:83]\n"
  "ds_read_b128 v[112:115], v182 offset:16640\n"
  "s_waitcnt lgkmcnt(3)\n"
  "v_mfma_f32_16x16x32_bf16 v[84:87], v[100:103], v[16:19], v[84:87]\n"
  "ds_read_b128 v[116:119], v182 offset:24832\n"
  "s_add_u32 m0, %[wb], 0\n"
  "s_add_u32 s40, s61, 0\n"
  "buffer_load_dwordx4 %[sa0], %[ra], s40 offen lds\n"
  "s_waitcnt lgkmcnt(3)\n"
  "v_mfma_f32_16x16x32_bf16 v[80:83], v[104:107], v[20:23], v[80:83]\n"
  "ds_read_b128 v[96:99], v183 offset:16640\n"
  "s_waitcnt lgkmcnt(3)\n"
  "v_mfma_f32_16x16x32_bf16 v[84:87], v[108:111], v[20:23], v[84:87]\n"
  "ds_read_b128 v[100:103], v183 offset:24832\n"
  "s_add_u32 m0, %[wb], 4096\n"
  "s_add_u32 s40, s61, 131072\n"
  "buffer_load_dwordx4 %[sa1], %[ra], s40 offen lds\n"
  "s_waitcnt vmcnt(10)\n"
  "s_barrier\n"
  "s_waitcnt lgkmcnt(3)\n"
  "v_mfma_f32_16x16x32_bf16 v[80:83], v[112:115], v[24:27], v[80:83]\n"
  "ds_read_b128 v[104:107], v180 offset:32768\n"
  "s_waitcnt lgkmcnt(3)\n"
  "v_mfma_f32_16x16x32_bf16 v[84:87], v[116:119], v[24:27], v[84:87]\n"
  "ds_read_b128 v[108:111], v180 offset:40960\n"
  "s_add_u32 m0, %[wb], 8192\n"
  "s_add_u32 s40, s61, 32768\n"
  "buffer_load_dwordx4 %[sa0], %[ra], s40 offen lds\n"
  "s_waitcnt lgkmcnt(3)\n"
  "v_mfma_f32_16x16x32_bf16 v[80:83], v[96:99], v[28:31], v[80:83]\n"
  "ds_read_b128 v[112:115], v181 offset:32768\n"
  "s_waitcnt lgkmcnt(3)\n"
  "v_mfma_f32_16x16x32_bf16 v[84:87], v[100:103], v[28:31], v[84:87]\n"
  "ds_read_b128 v[116:119], v181 offset:40960\n"
  "s_add_u32 m0, %[wb], 12288\n"
  "s_add_u32 s40, s61, 163840\n"
  "buffer_load_dwordx4 %[sa1], %[ra], s40 offen lds\n"
  "s_waitcnt lgkmcnt(3)\n"
  "v_mfma_f32_16x16x32_bf16 v[64:67], v[104:107], v[0:3], 0\n"
  "ds_read_b128 v[96:99], v182 offset:32768\n"
  "s_waitcnt lgkmcnt(3)\n"
  "v_mfma_f32_16x16x32_bf16 v[68:71], v[108:111], v[0:3], 0\n"
  "ds_read_b128 v[100:103], v182 offset:40960\n"
  "s_add_u32 m0, %[wb], 16384\n"
  "s_add_u32 s40, s62, 0\n"
  "buffer_load_dwordx4 %[sa0], %[ra], s40 offen lds\n"
  "s_waitcnt lgkmcnt(3)\n"
  "v_mfma_f32_16x16x32_bf16 v[64:67], v[112:115], v[4:7], v[64:67]\n"
  "ds_read_b128 v[104:107], v183 offset:32768\n"
  "s_waitcnt lgkmcnt(3)\n"
  "v_mfma_f32_16x16x32_bf16 v[68:71], v[116:119], v[4:7], v[68:71]\n"
  "ds_read_b128 v[108:111], v183 offset:40960\n"
  "v_cvt_pk_bf16_f32 v128, v80, v81\n"
  "v_cvt_pk_bf16_f32 v129, v82, v83\n"
  "s_add_u32 m0, %[wb], 20480\n"
  "s_add_u32 s40, s62, 131072\n"
  "buffer_load_dwordx4 %[sa1], %[ra], s40 offen lds\n"
  "s_waitcnt lgkmcnt(3)\n"
  "v_mfma_f32_16x16x32_bf16 v[64:67], v[96:99], v[8:11], v[64:67]\n"
  "ds_read_b128 v[112:115], v180 offset:33024\n"
  "v_cvt_pk_bf16_f32 v130, v84, v85\n"
  "v_cvt_pk_bf16_f32 v131, v86, v87\n"
  "s_waitcnt lgkmcnt(3)\n"
  "v_mfma_f32_16x16x32_bf16 v[68:71], v[100:103], v[8:11], v[68:71]\n"
  "ds_read_b128 v[116:119], v180 offset:41216\n"
  "s_add_u32 m0, %[wb], 24576\n"
  "s_add_u32 s40, s62, 32768\n"
  "buffer_load_dwordx4 %[sa0], %[ra], s40 offen lds\n"
  "s_waitcnt lgkmcnt(3)\n"
  "v_mfma_f32_16x16x32_bf16 v[64:67], v[104:107], v[12:15], v[64:67]\n"
  "ds_read_b128 v[96:99], v181 offset:33024\n"
  "s_waitcnt lgkmcnt(3)\n"
  "v_mfma_f32_16x16x32_bf16 v[68:71], v[108:111], v[12:15], v[68:71]\n"
  "ds_read_b128 v[100:103], v181 offset:41216\n"
  "s_add_u32 m0, %[wb], 28672\n"
  "s_add_u32 s40, s62, 163840\n"
  "buffer_load_dwordx4 %[sa1], %[ra], s40 offen lds\n"
  "s_waitcnt lgkmcnt(3)\n"
  "v_mfma_f32_16x16x32_bf16 v[64:67], v[112:115], v[16:19], v[64:67]\n"
  "ds_read_b128 v[104:107], v182 offset:33024\n"
  "s_waitcnt lgkmcnt(3)\n"
  "v_mfma_f32_16x16x32_bf16 v[68:71], v[116:119], v[16:19], v[68:71]\n"
  "ds_read_b128 v[108:111], v182 offset:41216\n"
  "buffer_store_dwordx4 v[192:195], %[x0], %[ws], 0 offen sc1\n"
  "s_waitcnt lgkmcnt(3)\n"
  "v_mfma_f32_16x16x32_bf16 v[64:67], v[96:99], v[20:23], v[64:67]\n"
  "ds_read_b128 v[112:115], v183 offset:33024\n"
  "s_waitcnt lgkmcnt(3)\n"
  "v_mfma_f32_16x16x32_bf16 v[68:71], v[100:103], v[20:23], v[68:71]\n"
  "ds_read_b128 v[116:119], v183 offset:41216\n"
  "buffer_store_dwordx4 v[196:199], %[x1], %[ws], 0 offen sc1\n"
  "s_waitcnt vmcnt(14)\n"
  "s_barrier\n"
  "s_waitcnt lgkmcnt(3)\n"
  "v_mfma_f32_16x16x32_bf16 v[64:67], v[104:107], v[24:27], v[64:67]\n"
  "ds_read_b128 v[96:99], v180 offset:49152\n"
  "s_waitcnt lgkmcnt(3)\n"
  "v_mfma_f32_16x16x32_bf16 v[68:71], v[108:111], v[24:27], v[68:71]\n"
  "ds_read_b128 v[100:103], v180 offset:57344\n"
  "s_lshr_b32 s40, s56, 8\n"
  "buffer_store_dwordx4 v[120:123], %[o0], %[ws], s40 offen sc1\n"
  "s_waitcnt lgkmcnt(3)\n"
  "v_mfma_f32_16x16x32_bf16 v[64:67], v[112:115], v[28:31], v[64:67]\n"
  "ds_read_b128 v[104:107], v181 offset:49152\n"
  "s_waitcnt lgkmcnt(3)\n"
  "v_mfma_f32_16x16x32_bf16 v[68:71], v[116:119], v[28:31], v[68:71]\n"
  "ds_read_b128 v[108:111], v181 offset:57344\n"
  "s_lshr_b32 s40, s57, 8\n"
  "buffer_store_dwordx4 v[128:131], %[o0], %[ws], s40 offen sc1\n"
  "s_waitcnt lgkmcnt(3)\n"
  "v_mfma_f32_16x16x32_bf16 v[80:83], v[96:99], v[0:3], 0\n"
  "ds_read_b128 v[112:115], v182 offset:49152\n"
  "s_waitcnt lgkmcnt(3)\n"
  "v_mfma_f32_16x16x32_bf16 v[84:87], v[100:103], v[0:3], 0\n"
  "ds_read_b128 v[116:119], v182 offset:57344\n"
  "s_waitcnt lgkmcnt(3)\n"
  "v_mfma_f32_16x16x32_bf16 v[80:83], v[104:107], v[4:7], v[80:83]\n"
  "ds_read_b128 v[96:99], v183 offset:49152\n"
  "s_waitcnt lgkmcnt(3)\n"
  "v_mfma_f32_16x16x32_bf16 v[84:87], v[108:111], v[4:7], v[84:87]\n"
  "ds_read_b128 v[100:103], v183 offset:57344\n"
  "v_cvt_pk_bf16_f32 v136, v64, v65\n"
  "v_cvt_pk_bf16_f32 v137, v66, v67\n"
  "s_waitcnt lgkmcnt(3)\n"
  "v_mfma_f32_16x16x32_bf16 v[80:83], v[112:115], v[8:11], v[80:83]\n"
  "ds_read_b128 v[104:107], v180 offset:49408\n"
  "v_cvt_pk_bf16_f32 v138, v68, v69\n"
  "v_cvt_pk_bf16_f32 v139, v70, v71\n"
  "s_waitcnt lgkmcnt(3)\n"
  "v_mfma_f32_16x16x32_bf16 v[84:87], v[116:119], v[8:11], v[84:87]\n"
  "ds_read_b128 v[108:111], v180 offset:57600\n"
  "s_waitcnt lgkmcnt(3)\n"
  "v_mfma_f32_16x16x32_bf16 v[80:83], v[96:99], v[12:15], v[80:83]\n"
  "ds_read_b128 v[112:115], v181 offset:49408\n"
  "s_waitcnt lgkmcnt(3)\n"
  "v_mfma_f32_16x16x32_bf16 v[84:87], v[100:103], v[12:15], v[84:87]\n"
  "ds_read_b128 v[116:119], v181 offset:57600\n"
  "s_lshr_b32 s40, s58, 8\n"
  "buffer_store_dwordx4 v[136:139], %[o0], %[ws], s40 offen sc1\n"
  "s_waitcnt lgkmcnt(3)\n"
  "v_mfma_f32_16x16x32_bf16 v[80:83], v[104:107], v[16:19], v[80:83]\n"
  "ds_read_b128 v[96:99], v182 offset:49408\n"
  "s_waitcnt lgkmcnt(3)\n"
  "v_mfma_f32_16x16x32_bf16 v[84:87], v[108:111], v[16:19], v[84:87]\n"
  "ds_read_b128 v[100:103], v182 offset:57600\n"
  "s_waitcnt lgkmcnt(3)\n"
  "v_mfma_f32_16x16x32_bf16 v[80:83], v[112:115], v[20:23], v[80:83]\n"
  "ds_read_b128 v[104:107], v183 offset:49408\n"
  "s_waitcnt lgkmcnt(3)\n"
  "v_mfma_f32_16x16x32_bf16 v[84:87], v[116:119], v[20:23], v[84:87]\n"
  "ds_read_b128 v[108:111], v183 offset:57600\n"
  "s_waitcnt vmcnt(13)\n"
  "s_barrier\n"
  "s_waitcnt lgkmcnt(3)\n"
  "v_mfma_f32_16x16x32_bf16 v[80:83], v[96:99], v[24:27], v[80:83]\n"
  "ds_read_b128 v[112:115], v184 offset:0\n"
  "s_waitcnt lgkmcnt(3)\n"
  "v_mfma_f32_16x16x32_bf16 v[84:87], v[100:103], v[24:27], v[84:87]\n"
  "ds_read_b128 v[116:119], v184 offset:8192\n"
  "s_waitcnt lgkmcnt(3)\n"
  "v_mfma_f32_16x16x32_bf16 v[80:83], v[104:107], v[28:31], v[80:83]\n"
  "ds_read_b128 v[96:99], v185 offset:0\n"
  "s_waitcnt lgkmcnt(3)\n"
  "v_mfma_f32_16x16x32_bf16 v[84:87], v[108:111], v[28:31], v[84:87]\n"
  "ds_read_b128 v[100:103], v185 offset:8192\n"
  "s_waitcnt lgkmcnt(3)\n"
  "v_mfma_f32_16x16x32_bf16 v[64:67], v[112:115], v[0:3], 0\n"
  "ds_read_b128 v[104:107], v186 offset:0\n"
  "s_waitcnt lgkmcnt(3)\n"
  "v_mfma_f32_16x16x32_bf16 v[68:71], v[116:119], v[0:3], 0\n"
  "ds_read_b128 v[108:111], v186 offset:8192\n"
  "s_waitcnt lgkmcnt(3)\n"
  "v_mfma_f32_16x16x32_bf16 v[64:67], v[96:99], v[4:7], v[64:67]\n"
  "ds_read_b128 v[112:115], v187 offset:0\n"
  "s_waitcnt lgkmcnt(3)\n"
  "v_mfma_f32_16x16x32_bf16 v[68:71], v[100:103], v[4:7], v[68:71]\n"
  "ds_read_b128 v[116:119], v187 offset:8192\n"
  "v_cvt_pk_bf16_f32 v144, v80, v81\n"
  "v_cvt_pk_bf16_f32 v145, v82, v83\n"
  "s_waitcnt lgkmcnt(3)\n"
  "v_mfma_f32_16x16x32_bf16 v[64:67], v[104:107], v[8:11], v[64:67]\n"
  "ds_read_b128 v[96:99], v184 offset:256\n"
  "v_cvt_pk_bf16_f32 v146, v84, v85\n"
  "v_cvt_pk_bf16_f32 v147, v86, v87\n"
  "s_waitcnt lgkmcnt(3)\n"
  "v_mfma_f32_16x16x32_bf16 v[68:71], v[108:111], v[8:11], v[68:71]\n"
  "ds_read_b128 v[100:103], v184 offset:8448\n"
  "s_waitcnt lgkmcnt(3)\n"
  "v_mfma_f32_16x16x32_bf16 v[64:67], v[112:115], v[12:15], v[64:67]\n"
  "ds_read_b128 v[104:107], v185 offset:256\n"
  "s_waitcnt lgkmcnt(3)\n"
  "v_mfma_f32_16x16x32_bf16 v[68:71], v[116:119], v[12:15], v[68:71]\n"
  "ds_read_b128 v[108:111], v185 offset:8448\n"
  "s_lshr_b32 s40, s59, 8\n"
  "buffer_store_dwordx4 v[144:147], %[o0], %[ws], s40 offen sc1\n"
  "s_waitcnt lgkmcnt(3)\n"
  "v_mfma_f32_16x16x32_bf16 v[64:67], v[96:99], v[16:19], v[64:67]\n"
  "ds_read_b128 v[112:115], v186 offset:256\n"
  "s_waitcnt lgkmcnt(3)\n"
  "v_mfma_f32_16x16x32_bf16 v[68:71], v[100:103], v[16:19], v[68:71]\n"
  "ds_read_b128 v[116:119], v186 offset:8448\n"
  "s_waitcnt lgkmcnt(3)\n"
  "v_mfma_f32_16x16x32_bf16 v[64:67], v[104:107], v[20:23], v[64:67]\n"
  "ds_read_b128 v[96:99], v187 offset:256\n"
  "s_waitcnt lgkmcnt(3)\n"
  "v_mfma_f32_16x16x32_bf16 v[68:71], v[108:111], v[20:23], v[68:71]\n"
  "ds_read_b128 v[100:103], v187 offset:8448\n"
  "s_waitcnt vmcnt(10)\n"
  "s_barrier\n"
  "s_waitcnt lgkmcnt(3)\n"
  "v_mfma_f32_16x16x32_bf16 v[64:67], v[112:115], v[24:27], v[64:67]\n"
  "ds_read_b128 v[104:107], v176 offset:0\n"
  "s_waitcnt lgkmcnt(3)\n"
  "v_mfma_f32_16x16x32_bf16 v[68:71], v[116:119], v[24:27], v[68:71]\n"
  "ds_read_b128 v[108:111], v176 offset:8192\n"
  "s_waitcnt lgkmcnt(3)\n"
  "v_mfma_f32_16x16x32_bf16 v[64:67], v[96:99], v[28:31], v[64:67]\n"
  "ds_read_b128 v[112:115], v177 offset:0\n"
  "s_waitcnt lgkmcnt(3)\n"
  "v_mfma_f32_16x16x32_bf16 v[68:71], v[100:103], v[28:31], v[68:71]\n"
  "ds_read_b128 v[116:119], v177 offset:8192\n"
  "s_waitcnt lgkmcnt(3)\n"
  "v_mfma_f32_16x16x32_bf16 v[80:83], v[104:107], v[0:3], 0\n"
  "ds_read_b128 v[96:99], v178 offset:0\n"
  "s_waitcnt lgkmcnt(3)\n"
  "v_mfma_f32_16x16x32_bf16 v[84:87], v[108:111], v[0:3], 0\n"
  "ds_read_b128 v[100:103], v178 offset:8192\n"
  "s_waitcnt lgkmcnt(3)\n"
  "v_mfma_f32_16x16x32_bf16 v[80:83], v[112:115], v[4:7], v[80:83]\n"
  "ds_read_b128 v[104:107], v179 offset:0\n"
  "s_waitcnt lgkmcnt(3)\n"
  "v_mfma_f32_16x16x32_bf16 v[84:87], v[116:119], v[4:7], v[84:87]\n"
  "ds_read_b128 v[108:111], v179 offset:8192\n"
  "v_cvt_pk_bf16_f32 v152, v64, v65\n"
  "v_cvt_pk_bf16_f32 v153, v66, v67\n"
  "s_waitcnt lgkmcnt(3)\n"
  "v_mfma_f32_16x16x32_bf16 v[80:83], v[96:99], v[8:11], v[80:83]\n"
  "ds_read_b128 v[112:115], v176 offset:256\n"
  "v_cvt_pk_bf16_f32 v154, v68, v69\n"
  "v_cvt_pk_bf16_f32 v155, v70, v71\n"
  "s_waitcnt lgkmcnt(3)\n"
  "v_mfma_f32_16x16x32_bf16 v[84:87], v[100:103], v[8:11], v[84:87]\n"
  "ds_read_b128 v[116:119], v176 offset:8448\n"
  "s_waitcnt lgkmcnt(3)\n"
  "v_mfma_f32_16x16x32_bf16 v[80:83], v[104:107], v[12:15], v[80:83]\n"
  "ds_read_b128 v[96:99], v177 offset:256\n"
  "s_waitcnt lgkmcnt(3)\n"
  "v_mfma_f32_16x16x32_bf16 v[84:87], v[108:111], v[12:15], v[84:87]\n"
  "ds_read_b128 v[100:103], v177 offset:8448\n"
  "s_lshr_b32 s40, s60, 8\n"
  "buffer_store_dwordx4 v[152:155], %[o0], %[ws], s40 offen sc1\n"
  "s_waitcnt lgkmcnt(3)\n"
  "v_mfma_f32_16x16x32_bf16 v[80:83], v[112:115], v[16:19], v[80:83]\n"
  "ds_read_b128 v[104:107], v178 offset:256\n"
  "s_waitcnt lgkmcnt(3)\n"
  "v_mfma_f32_16x16x32_bf16 v[84:87], v[116:119], v[16:19], v[84:87]\n"
  "ds_read_b128 v[108:111], v178 offset:8448\n"
  "s_waitcnt lgkmcnt(3)\n"
  "v_mfma_f32_16x16x32_bf16 v[80:83], v[96:99], v[20:23], v[80:83]\n"
  "ds_read_b128 v[112:115], v179 offset:256\n"
  "s_waitcnt lgkmcnt(3)\n"
  "v_mfma_f32_16x16x32_bf16 v[84:87], v[100:103], v[20:23], v[84:87]\n"
  "ds_read_b128 v[116:119], v179 offset:8448\n"
  "s_waitcnt vmcnt(7)\n"
  "s_barrier\n"
  "s_waitcnt lgkmcnt(3)\n"
  "v_mfma_f32_16x16x32_bf16 v[80:83], v[104:107], v[24:27], v[80:83]\n"
  "ds_read_b128 v[96:99], v176 offset:16384\n"
  "s_waitcnt lgkmcnt(3)\n"
  "v_mfma_f32_16x16x32_bf16 v[84:87], v[108:111], v[24:27], v[84:87]\n"
  "ds_read_b128 v[100:103], v176 offset:24576\n"
  "s_waitcnt lgkmcnt(3)\n"
  "v_mfma_f32_16x16x32_bf16 v[80:83], v[112:115], v[28:31], v[80:83]\n"
  "ds_read_b128 v[104:107], v177 offset:16384\n"
  "s_waitcnt lgkmcnt(3)\n"
  "v_mfma_f32_16x16x32_bf16 v[84:87], v[116:119], v[28:31], v[84:87]\n"
  "ds_read_b128 v[108:111], v177 offset:24576\n"
  "s_waitcnt lgkmcnt(3)\n"
  "v_mfma_f32_16x16x32_bf16 v[64:67], v[96:99], v[0:3], 0\n"
  "ds_read_b128 v[112:115], v178 offset:16384\n"
  "s_waitcnt lgkmcnt(3)\n"
  "v_mfma_f32_16x16x32_bf16 v[68:71], v[100:103], v[0:3], 0\n"
  "ds_read_b128 v[116:119], v178 offset:24576\n"
  "s_waitcnt lgkmcnt(3)\n"
  "v_mfma_f32_16x16x32_bf16 v[64:67], v[104:107], v[4:7], v[64:67]\n"
  "ds_read_b128 v[96:99], v179 offset:16384\n"
  "s_waitcnt lgkmcnt(3)\n"
  "v_mfma_f32_16x16x32_bf16 v[68:71], v[108:111], v[4:7], v[68:71]\n"
  "ds_read_b128 v[100:103], v179 offset:24576\n"
  "v_cvt_pk_bf16_f32 v160, v80, v81\n"
  "v_cvt_pk_bf16_f32 v161, v82, v83\n"
  "s_waitcnt lgkmcnt(3)\n"
  "v_mfma_f32_16x16x32_bf16 v[64:67], v[112:115], v[8:11], v[64:67]\n"
  "ds_read_b128 v[104:107], v176 offset:16640\n"
  "v_cvt_pk_bf16_f32 v162, v84, v85\n"
  "v_cvt_pk_bf16_f32 v163, v86, v87\n"
  "s_waitcnt lgkmcnt(3)\n"
  "v_mfma_f32_16x16x32_bf16 v[68:71], v[116:119], v[8:11], v[68:71]\n"
  "ds_read_b128 v[108:111], v176 offset:24832\n"
  "s_waitcnt lgkmcnt(3)\n"
  "v_mfma_f32_16x16x32_bf16 v[64:67], v[96:99], v[12:15], v[64:67]\n"
  "ds_read_b128 v[112:115], v177 offset:16640\n"
  "s_waitcnt lgkmcnt(3)\n"
  "v_mfma_f32_16x16x32_bf16 v[68:71], v[100:103], v[12:15], v[68:71]\n"
  "ds_read_b128 v[116:119], v177 offset:24832\n"
  "s_lshr_b32 s40, s61, 8\n"
  "buffer_store_dwordx4 v[160:163], %[o0], %[ws], s40 offen sc1\n"
  "s_waitcnt lgkmcnt(3)\n"
  "v_mfma_f32_16x16x32_bf16 v[64:67], v[104:107], v[16:19], v[64:67]\n"
  "ds_read_b128 v[96:99], v178 offset:16640\n"
  "s_waitcnt lgkmcnt(3)\n"
  "v_mfma_f32_16x16x32_bf16 v[68:71], v[108:111], v[16:19], v[68:71]\n"
  "ds_read_b128 v[100:103], v178 offset:24832\n"
  "s_waitcnt lgkmcnt(3)\n"
  "v_mfma_f32_16x16x32_bf16 v[64:67], v[112:115], v[20:23], v[64:67]\n"
  "ds_read_b128 v[104:107], v179 offset:16640\n"
  "s_waitcnt lgkmcnt(3)\n"
  "v_mfma_f32_16x16x32_bf16 v[68:71], v[116:119], v[20:23], v[68:71]\n"
  "ds_read_b128 v[108:111], v179 offset:24832\n"
  "s_waitcnt lgkmcnt(3)\n"
  "v_mfma_f32_16x16x32_bf16 v[64:67], v[96:99], v[24:27], v[64:67]\n"
  "s_waitcnt lgkmcnt(2)\n"
  "v_mfma_f32_16x16x32_bf16 v[68:71], v[100:103], v[24:27], v[68:71]\n"
  "s_waitcnt lgkmcnt(1)\n"
  "v_mfma_f32_16x16x32_bf16 v[64:67], v[104:107], v[28:31], v[64:67]\n"
  "s_waitcnt lgkmcnt(0)\n"
  "v_mfma_f32_16x16x32_bf16 v[68:71], v[108:111], v[28:31], v[68:71]\n"
  "s_nop 7\n"
  "v_cvt_pk_bf16_f32 v168, v64, v65\n"
  "v_cvt_pk_bf16_f32 v169, v66, v67\n"
  "v_cvt_pk_bf16_f32 v170, v68, v69\n"
  "v_cvt_pk_bf16_f32 v171, v70, v71\n"
  "s_lshr_b32 s40, s62, 8\n"
  "buffer_store_dwordx4 v[168:171], %[o0], %[ws], s40 offen sc1\n"
  "Le_%=:\n"
  :: [ws] "s"(ws), [ra] "s"(ra), [wb] "s"(wb), [rk] "s"(2 * nb), [sa0] "v"(sa), [sa1] "v"(sa ^ 128),
     [o0] "v"(W_WF + ((mh * 28 + 7 * nb + 2 * w) * 14 + 7 * kb) * 1024 + lane * 16), [x0] "v"(xf), [x1] "v"(tid < 192 ? xf + 8192 : OOB),
     [f0] "s"(mk(a.wg, D * F * 2)), [f1] "s"(mk(a.wu, D * F * 2)), [f2] "s"(mk(a.wd, D * F * 2)),
     [pf0] "v"(blk * 6272 + tid * 16), [pf1] "v"(tid < 136 ? blk * 6272 + 4096 + tid * 16 : OOB),
     [ll] "v"(PRE_LN + tid * 16), [la] "v"(c * 512 + ((g ^ c) << 4)),
     [lb] "v"(mat ? (8 * g + (c >> 2)) * 256 + ((((c >> 2) + 4 * (g & 1)) ^ (2 * w)) << 5) + (c & 3) * 8 : w * 16384)
  : "v0", "v1", "v2", "v3", "v4", "v5", "v6", "v7", "v8", "v9", "v10", "v11", "v12", "v13", "v14", "v15", "v16", "v17", "v18", "v19", "v20", "v21", "v22", "v23", "v24", "v25", "v26", "v27", "v28", "v29", "v30", "v31", "v32", "v33", "v34", "v35", "v36", "v37", "v38", "v39", "v40", "v41", "v42", "v43", "v44", "v45", "v46", "v47", "v48", "v49", "v50", "v51", "v52", "v53", "v54", "v55", "v56", "v57", "v58", "v59", "v60", "v61", "v62", "v63", "v64", "v65", "v66", "v67", "v68", "v69", "v70", "v71", "v72", "v73", "v74", "v75", "v76", "v77", "v78", "v79", "v80", "v81", "v82", "v83", "v84", "v85", "v86", "v87", "v88", "v89", "v90", "v91", "v92", "v93", "v94", "v95", "v96", "v97", "v98", "v99", "v100", "v101", "v102", "v103", "v104", "v105", "v106", "v107", "v108", "v109", "v110", "v111", "v112", "v113", "v114", "v115", "v116", "v117", "v118", "v119", "v120", "v121", "v122", "v123", "v124", "v125", "v126", "v127", "v128", "v129", "v130", "v131", "v132", "v133", "v134", "v135", "v136", "v137", "v138", "v139", "v140", "v141", "v142", "v143", "v144", "v145", "v146", "v147", "v148", "v149", "v150", "v151", "v152", "v153", "v154", "v155", "v156", "v157", "v158", "v159", "v160", "v161", "v162", "v163", "v164", "v165", "v166", "v167", "v168", "v169", "v170", "v171", "v172", "v173", "v174", "v175", "v176", "v177", "v178", "v179", "v180", "v181", "v182", "v183", "v184", "v185", "v186", "v187", "v188", "v189", "v190", "v191", "v192", "v193", "v194", "v195", "v196", "v197", "v198", "v199", "a248", "a249", "a250", "a251", "s40", "s41", "s42", "s43", "s44", "s45", "s46", "s47", "s48", "s49", "s50", "s51", "s52", "s53", "s54", "s55", "s56", "s57", "s58", "s59", "s60", "s61", "s62", "scc", "memory");
}

__global__ void __launch_bounds__(256, 1) k_pre(Args a) {
  extern __shared__ __attribute__((aligned(16))) unsigned char smem[];
  const int tid = threadIdx.x, blk = blockIdx.x;
  const rsrc_t ws = mk((const void*)ws_base(a.ws), W_END);
  warm_code(72704);
  if (blk & 128) pre_block1(a, ws); else pre_block0(a, ws);
  const v4u zero = {};
  st4c(ws, tid < 16 ? W_FLAG + blk * 256 + tid * 16 : OOB, zero);
}

template <int KIND>
__device__ __forceinline__ void producer(const Args& a, rsrc_t ws, int idx) {
  const int tid = threadIdx.x, lane = tid & 63, w = __builtin_amdgcn_readfirstlane(tid >> 6), g = lane >> 4, c = lane & 15;
  const unsigned perm = (c >> 3) * 14336 + (g * 16 + 8 * ((c >> 2) & 1) + (c & 3)) * 16, la = lane * 16, wb = w * 7168;
  const unsigned xs = W_XF + wb + la;
  const unsigned fl = tid == 0 ? W_FLAG + ((KIND == 0 ? FL_KQ : KIND == 1 ? FL_VO : FL_H) + idx) * 256 : OOB;
  if (KIND == 0) {
    const int p0 = 8 * idx + 2 * w, p1 = p0 + 1;
    asm volatile(
  "s_mov_b32 s50, 0xbfb8aa3b\n"
  "s_mov_b32 s41, 0\n"
  "s_mov_b32 s42, 4096\n"
  "s_mov_b32 s46, %[wb]\n"
  "s_mov_b32 m0, s46\n"
  "s_nop 0\n"
  "buffer_load_dwordx4 %[xs], %[ws], s41 offen offset:0 lds\n"
  "buffer_load_dwordx4 %[xs], %[ws], s41 offen offset:1024 lds\n"
  "buffer_load_dwordx4 %[xs], %[ws], s41 offen offset:2048 lds\n"
  "buffer_load_dwordx4 %[xs], %[ws], s41 offen offset:3072 lds\n"
  "s_add_u32 m0, s46, 4096\n"
  "s_nop 0\n"
  "buffer_load_dwordx4 %[xs], %[ws], s42 offen offset:0 lds\n"
  "buffer_load_dwordx4 %[xs], %[ws], s42 offen offset:1024 lds\n"
  "buffer_load_dwordx4 %[xs], %[ws], s42 offen offset:2048 lds\n"
  "s_add_u32 s41, s41, 28672\n"
  "s_add_u32 s42, s42, 28672\n"
  "s_add_u32 s46, s46, 28672\n"
  "s_cmp_ge_u32 s46, 86016\n"
  "s_cselect_b32 s48, 86016, 0\n"
  "s_sub_u32 s46, s46, s48\n"
  "s_mov_b32 s48, 0\n"
  "buffer_load_dwordx4 a[0:3], %[wsrc], %[ws], s48 offen\n"
  "s_mov_b32 s48, 64\n"
  "buffer_load_dwordx4 a[56:59], %[wsrc], %[ws], s48 offen\n"
  "s_mov_b32 s48, 28672\n"
  "buffer_load_dwordx4 a[112:115], %[wsrc], %[ws], s48 offen\n"
  "s_mov_b32 s48, 28736\n"
  "buffer_load_dwordx4 a[168:171], %[wsrc], %[ws], s48 offen\n"
  "s_mov_b32 s48, 1024\n"
  "buffer_load_dwordx4 a[4:7], %[wsrc], %[ws], s48 offen\n"
  "s_mov_b32 s48, 1088\n"
  "buffer_load_dwordx4 a[60:63], %[wsrc], %[ws], s48 offen\n"
  "s_mov_b32 s48, 29696\n"
  "buffer_load_dwordx4 a[116:119], %[wsrc], %[ws], s48 offen\n"
  "s_mov_b32 s48, 29760\n"
  "buffer_load_dwordx4 a[172:175], %[wsrc], %[ws], s48 offen\n"
  "s_mov_b32 s48, 2048\n"
  "buffer_load_dwordx4 a[8:11], %[wsrc], %[ws], s48 offen\n"
  "s_mov_b32 s48, 2112\n"
  "buffer_load_dwordx4 a[64:67], %[wsrc], %[ws], s48 offen\n"
  "s_mov_b32 s48, 30720\n"
  "buffer_load_dwordx4 a[120:123], %[wsrc], %[ws], s48 offen\n"
  "s_mov_b32 s48, 30784\n"
  "buffer_load_dwordx4 a[176:179], %[wsrc], %[ws], s48 offen\n"
  "s_mov_b32 s48, 3072\n"
  "buffer_load_dwordx4 a[12:15], %[wsrc], %[ws], s48 offen\n"
  "s_mov_b32 s48, 3136\n"
  "buffer_load_dwordx4 a[68:71], %[wsrc], %[ws], s48 offen\n"
  "s_mov_b32 s48, 31744\n"
  "buffer_load_dwordx4 a[124:127], %[wsrc], %[ws], s48 offen\n"
  "s_mov_b32 s48, 31808\n"
  "buffer_load_dwordx4 a[180:183], %[wsrc], %[ws], s48 offen\n"
  "s_mov_b32 s48, 4096\n"
  "buffer_load_dwordx4 a[16:19], %[wsrc], %[ws], s48 offen\n"
  "s_mov_b32 s48, 4160\n"
  "buffer_load_dwordx4 a[72:75], %[wsrc], %[ws], s48 offen\n"
  "s_mov_b32 s48, 32768\n"
  "buffer_load_dwordx4 a[128:131], %[wsrc], %[ws], s48 offen\n"
  "s_mov_b32 s48, 32832\n"
  "buffer_load_dwordx4 a[184:187], %[wsrc], %[ws], s48 offen\n"
  "s_mov_b32 s48, 5120\n"
  "buffer_load_dwordx4 a[20:23], %[wsrc], %[ws], s48 offen\n"
  "s_mov_b32 s48, 5184\n"
  "buffer_load_dwordx4 a[76:79], %[wsrc], %[ws], s48 offen\n"
  "s_mov_b32 s48, 33792\n"
  "buffer_load_dwordx4 a[132:135], %[wsrc], %[ws], s48 offen\n"
  "s_mov_b32 s48, 33856\n"
  "buffer_load_dwordx4 a[188:191], %[wsrc], %[ws], s48 offen\n"
  "s_mov_b32 s48, 6144\n"
  "buffer_load_dwordx4 a[24:27], %[wsrc], %[ws], s48 offen\n"
  "s_mov_b32 s48, 6208\n"
  "buffer_load_dwordx4 a[80:83], %[wsrc], %[ws], s48 offen\n"
  "s_mov_b32 s48, 34816\n"
  "buffer_load_dwordx4 a[136:139], %[wsrc], %[ws], s48 offen\n"
  "s_mov_b32 s48, 34880\n"
  "buffer_load_dwordx4 a[192:195], %[wsrc], %[ws], s48 offen\n"
  "s_mov_b32 s48, 7168\n"
  "buffer_load_dwordx4 a[28:31], %[wsrc], %[ws], s48 offen\n"
  "s_mov_b32 s48, 7232\n"
  "buffer_load_dwordx4 a[84:87], %[wsrc], %[ws], s48 offen\n"
  "s_mov_b32 s48, 35840\n"
  "buffer_load_dwordx4 a[140:143], %[wsrc], %[ws], s48 offen\n"
  "s_mov_b32 s48, 35904\n"
  "buffer_load_dwordx4 a[196:199], %[wsrc], %[ws], s48 offen\n"
  "s_mov_b32 s48, 8192\n"
  "buffer_load_dwordx4 a[32:35], %[wsrc], %[ws], s48 offen\n"
  "s_mov_b32 s48, 8256\n"
  "buffer_load_dwordx4 a[88:91], %[wsrc], %[ws], s48 offen\n"
  "s_mov_b32 s48, 36864\n"
  "buffer_load_dwordx4 a[144:147], %[wsrc], %[ws], s48 offen\n"
  "s_mov_b32 s48, 36928\n"
  "buffer_load_dwordx4 a[200:203], %[wsrc], %[ws], s48 offen\n"
  "s_mov_b32 s48, 9216\n"
  "buffer_load_dwordx4 a[36:39], %[wsrc], %[ws], s48 offen\n"
  "s_mov_b32 s48, 9280\n"
  "buffer_load_dwordx4 a[92:95], %[wsrc], %[ws], s48 offen\n"
  "s_mov_b32 s48, 37888\n"
  "buffer_load_dwordx4 a[148:151], %[wsrc], %[ws], s48 offen\n"
  "s_mov_b32 s48, 37952\n"
  "buffer_load_dwordx4 a[204:207], %[wsrc], %[ws], s48 offen\n"
  "s_mov_b32 s48, 10240\n"
  "buffer_load_dwordx4 a[40:43], %[wsrc], %[ws], s48 offen\n"
  "s_mov_b32 s48, 10304\n"
  "buffer_load_dwordx4 a[96:99], %[wsrc], %[ws], s48 offen\n"
  "s_mov_b32 s48, 38912\n"
  "buffer_load_dwordx4 a[152:155], %[wsrc], %[ws], s48 offen\n"
  "s_mov_b32 s48, 38976\n"
  "buffer_load_dwordx4 a[208:211], %[wsrc], %[ws], s48 offen\n"
  "s_mov_b32 s48, 11264\n"
  "buffer_load_dwordx4 a[44:47], %[wsrc], %[ws], s48 offen\n"
  "s_mov_b32 s48, 11328\n"
  "buffer_load_dwordx4 a[100:103], %[wsrc], %[ws], s48 offen\n"
  "s_mov_b32 s48, 39936\n"
  "buffer_load_dwordx4 a[156:159], %[wsrc], %[ws], s48 offen\n"
  "s_mov_b32 s48, 40000\n"
  "buffer_load_dwordx4 a[212:215], %[wsrc], %[ws], s48 offen\n"
  "s_mov_b32 s48, 12288\n"
  "buffer_load_dwordx4 a[48:51], %[wsrc], %[ws], s48 offen\n"
  "s_mov_b32 s48, 12352\n"
  "buffer_load_dwordx4 a[104:107], %[wsrc], %[ws], s48 offen\n"
  "s_mov_b32 s48, 40960\n"
  "buffer_load_dwordx4 a[160:163], %[wsrc], %[ws], s48 offen\n"
  "s_mov_b32 s48, 41024\n"
  "buffer_load_dwordx4 a[216:219], %[wsrc], %[ws], s48 offen\n"
  "s_mov_b32 s48, 13312\n"
  "buffer_load_dwordx4 a[52:55], %[wsrc], %[ws], s48 offen\n"
  "s_mov_b32 s48, 13376\n"
  "buffer_load_dwordx4 a[108:111], %[wsrc], %[ws], s48 offen\n"
  "s_mov_b32 s48, 41984\n"
  "buffer_load_dwordx4 a[164:167], %[wsrc], %[ws], s48 offen\n"
  "s_mov_b32 s48, 42048\n"
  "buffer_load_dwordx4 a[220:223], %[wsrc], %[ws], s48 offen\n"
  "s_mov_b32 s40, 0\n"
  "s_mov_b32 s45, 0x40000000\n"
  "s_mov_b32 s47, 28672\n"
  "v_mov_b32 v72, %[la]\n"
  "v_add_u32 v73, 28672, %[la]\n"
  "s_waitcnt vmcnt(54)\n"
  "s_barrier\n"
  "s_mov_b32 m0, s46\n"
  "s_nop 0\n"
  "buffer_load_dwordx4 %[xs], %[ws], s41 offen offset:0 lds\n"
  "buffer_load_dwordx4 %[xs], %[ws], s41 offen offset:1024 lds\n"
  "buffer_load_dwordx4 %[xs], %[ws], s41 offen offset:2048 lds\n"
  "buffer_load_dwordx4 %[xs], %[ws], s41 offen offset:3072 lds\n"
  "s_add_u32 m0, s46, 4096\n"
  "s_nop 0\n"
  "buffer_load_dwordx4 %[xs], %[ws], s42 offen offset:0 lds\n"
  "buffer_load_dwordx4 %[xs], %[ws], s42 offen offset:1024 lds\n"
  "buffer_load_dwordx4 %[xs], %[ws], s42 offen offset:2048 lds\n"
  "v_mov_b32 v70, 2155520\n"
  "buffer_load_dword v71, v70, %[ws], 0 offen sc1\n"
  "ds_read_b128 v[32:35], v72 offset:0\n"
  "ds_read_b128 v[36:39], v72 offset:1024\n"
  "ds_read_b128 v[40:43], v72 offset:2048\n"
  "s_waitcnt vmcnt(60) lgkmcnt(2)\n"
  ".p2align 6\n"
  "Lp_top_%=:\n"
  "v_mfma_f32_16x16x32_bf16 v[0:3], a[0:3], v[32:35], 0\n"
  "ds_read_b128 v[44:47], v72 offset:3072\n"
  "v_mfma_f32_16x16x32_bf16 v[4:7], a[56:59], v[32:35], 0\n"
  "v_mfma_f32_16x16x32_bf16 v[8:11], a[112:115], v[32:35], 0\n"
  "v_cvt_pk_bf16_f32 v48, v16, v17\n"
  "v_mfma_f32_16x16x32_bf16 v[12:15], a[168:171], v[32:35], 0\n"
  "s_waitcnt vmcnt(56) lgkmcnt(2)\n"
  "v_mfma_f32_16x16x32_bf16 v[0:3], a[4:7], v[36:39], v[0:3]\n"
  "ds_read_b128 v[32:35], v72 offset:4096\n"
  "v_cvt_pk_bf16_f32 v49, v18, v19\n"
  "v_mfma_f32_16x16x32_bf16 v[4:7], a[60:63], v[36:39], v[4:7]\n"
  "v_mfma_f32_16x16x32_bf16 v[8:11], a[116:119], v[36:39], v[8:11]\n"
  "v_cvt_pk_bf16_f32 v50, v20, v21\n"
  "v_mfma_f32_16x16x32_bf16 v[12:15], a[172:175], v[36:39], v[12:15]\n"
  "s_waitcnt vmcnt(52) lgkmcnt(2)\n"
  "v_mfma_f32_16x16x32_bf16 v[0:3], a[8:11], v[40:43], v[0:3]\n"
  "ds_read_b128 v[36:39], v72 offset:5120\n"
  "v_cvt_pk_bf16_f32 v51, v22, v23\n"
  "v_mfma_f32_16x16x32_bf16 v[4:7], a[64:67], v[40:43], v[4:7]\n"
  "v_mfma_f32_16x16x32_bf16 v[8:11], a[120:123], v[40:43], v[8:11]\n"
  "v_cvt_pk_bf16_f32 v52, v24, v25\n"
  "v_mfma_f32_16x16x32_bf16 v[12:15], a[176:179], v[40:43], v[12:15]\n"
  "s_waitcnt vmcnt(48) lgkmcnt(2)\n"
  "buffer_store_dwordx4 v[48:51], %[o0], %[ws], s45 offen sc1\n"
  "v_mfma_f32_16x16x32_bf16 v[0:3], a[12:15], v[44:47], v[0:3]\n"
  "ds_read_b128 v[40:43], v72 offset:6144\n"
  "v_cvt_pk_bf16_f32 v53, v26, v27\n"
  "v_mfma_f32_16x16x32_bf16 v[4:7], a[68:71], v[44:47], v[4:7]\n"
  "v_mfma_f32_16x16x32_bf16 v[8:11], a[124:127], v[44:47], v[8:11]\n"
  "v_cvt_pk_bf16_f32 v54, v28, v29\n"
  "v_mfma_f32_16x16x32_bf16 v[12:15], a[180:183], v[44:47], v[12:15]\n"
  "s_waitcnt vmcnt(44) lgkmcnt(2)\n"
  "v_mfma_f32_16x16x32_bf16 v[0:3], a[16:19], v[32:35], v[0:3]\n"
  "ds_read_b128 v[44:47], v72 offset:7168\n"
  "v_cvt_pk_bf16_f32 v55, v30, v31\n"
  "v_mfma_f32_16x16x32_bf16 v[4:7], a[72:75], v[32:35], v[4:7]\n"
  "v_mfma_f32_16x16x32_bf16 v[8:11], a[128:131], v[32:35], v[8:11]\n"
  "v_mfma_f32_16x16x32_bf16 v[12:15], a[184:187], v[32:35], v[12:15]\n"
  "s_waitcnt vmcnt(40) lgkmcnt(2)\n"
  "buffer_store_dwordx4 v[52:55], %[o1], %[ws], s45 offen sc1\n"
  "v_mfma_f32_16x16x32_bf16 v[0:3], a[20:23], v[36:39], v[0:3]\n"
  "ds_read_b128 v[32:35], v72 offset:8192\n"
  "v_mfma_f32_16x16x32_bf16 v[4:7], a[76:79], v[36:39], v[4:7]\n"
  "s_add_u32 s41, s41, 28672\n"
  "v_mfma_f32_16x16x32_bf16 v[8:11], a[132:135], v[36:39], v[8:11]\n"
  "s_add_u32 s42, s42, 28672\n"
  "v_mfma_f32_16x16x32_bf16 v[12:15], a[188:191], v[36:39], v[12:15]\n"
  "s_waitcnt vmcnt(36) lgkmcnt(2)\n"
  "v_mfma_f32_16x16x32_bf16 v[0:3], a[24:27], v[40:43], v[0:3]\n"
  "ds_read_b128 v[36:39], v72 offset:9216\n"
  "v_mfma_f32_16x16x32_bf16 v[4:7], a[80:83], v[40:43], v[4:7]\n"
  "s_add_u32 s46, s46, 28672\n"
  "v_mfma_f32_16x16x32_bf16 v[8:11], a[136:139], v[40:43], v[8:11]\n"
  "s_cmp_ge_u32 s46, 86016\n"
  "v_mfma_f32_16x16x32_bf16 v[12:15], a[192:195], v[40:43], v[12:15]\n"
  "s_waitcnt vmcnt(32) lgkmcnt(2)\n"
  "v_mfma_f32_16x16x32_bf16 v[0:3], a[28:31], v[44:47], v[0:3]\n"
  "ds_read_b128 v[40:43], v72 offset:10240\n"
  "v_mfma_f32_16x16x32_bf16 v[4:7], a[84:87], v[44:47], v[4:7]\n"
  "s_cselect_b32 s48, 86016, 0\n"
  "v_mfma_f32_16x16x32_bf16 v[8:11], a[140:143], v[44:47], v[8:11]\n"
  "s_sub_u32 s46, s46, s48\n"
  "v_mfma_f32_16x16x32_bf16 v[12:15], a[196:199], v[44:47], v[12:15]\n"
  "s_waitcnt vmcnt(28) lgkmcnt(2)\n"
  "v_mfma_f32_16x16x32_bf16 v[0:3], a[32:35], v[32:35], v[0:3]\n"
  "ds_read_b128 v[44:47], v72 offset:11264\n"
  "v_mfma_f32_16x16x32_bf16 v[4:7], a[88:91], v[32:35], v[4:7]\n"
  "s_add_u32 s47, s47, 28672\n"
  "v_mfma_f32_16x16x32_bf16 v[8:11], a[144:147], v[32:35], v[8:11]\n"
  "s_cmp_ge_u32 s47, 86016\n"
  "v_mfma_f32_16x16x32_bf16 v[12:15], a[200:203], v[32:35], v[12:15]\n"
  "s_waitcnt vmcnt(24) lgkmcnt(2)\n"
  "v_mfma_f32_16x16x32_bf16 v[0:3], a[36:39], v[36:39], v[0:3]\n"
  "ds_read_b128 v[32:35], v72 offset:12288\n"
  "v_mfma_f32_16x16x32_bf16 v[4:7], a[92:95], v[36:39], v[4:7]\n"
  "s_cselect_b32 s47, 0, s47\n"
  "v_mfma_f32_16x16x32_bf16 v[8:11], a[148:151], v[36:39], v[8:11]\n"
  "s_sub_i32 s48, s40, 0\n"
  "v_mfma_f32_16x16x32_bf16 v[12:15], a[204:207], v[36:39], v[12:15]\n"
  "s_waitcnt vmcnt(20) lgkmcnt(2)\n"
  "v_mfma_f32_16x16x32_bf16 v[0:3], a[40:43], v[40:43], v[0:3]\n"
  "ds_read_b128 v[36:39], v72 offset:13312\n"
  "v_mfma_f32_16x16x32_bf16 v[4:7], a[96:99], v[40:43], v[4:7]\n"
  "s_max_i32 s48, s48, 0\n"
  "v_mfma_f32_16x16x32_bf16 v[8:11], a[152:155], v[40:43], v[8:11]\n"
  "v_mfma_f32_16x16x32_bf16 v[12:15], a[208:211], v[40:43], v[12:15]\n"
  "s_waitcnt vmcnt(16) lgkmcnt(2)\n"
  "v_mfma_f32_16x16x32_bf16 v[0:3], a[44:47], v[44:47], v[0:3]\n"
  "ds_read_b128 v[40:43], v72 offset:14336\n"
  "v_mfma_f32_16x16x32_bf16 v[4:7], a[100:103], v[44:47], v[4:7]\n"
  "v_mfma_f32_16x16x32_bf16 v[8:11], a[156:159], v[44:47], v[8:11]\n"
  "v_mfma_f32_16x16x32_bf16 v[12:15], a[212:215], v[44:47], v[12:15]\n"
  "s_waitcnt vmcnt(12) lgkmcnt(2)\n"
  "v_mfma_f32_16x16x32_bf16 v[0:3], a[48:51], v[32:35], v[0:3]\n"
  "ds_read_b128 v[44:47], v72 offset:15360\n"
  "v_mfma_f32_16x16x32_bf16 v[4:7], a[104:107], v[32:35], v[4:7]\n"
  "v_mfma_f32_16x16x32_bf16 v[8:11], a[160:163], v[32:35], v[8:11]\n"
  "v_mfma_f32_16x16x32_bf16 v[12:15], a[216:219], v[32:35], v[12:15]\n"
  "s_waitcnt vmcnt(8) lgkmcnt(2)\n"
  "v_mfma_f32_16x16x32_bf16 v[0:3], a[52:55], v[36:39], v[0:3]\n"
  "ds_read_b128 v[32:35], v72 offset:16384\n"
  "v_mfma_f32_16x16x32_bf16 v[4:7], a[108:111], v[36:39], v[4:7]\n"
  "v_mfma_f32_16x16x32_bf16 v[8:11], a[164:167], v[36:39], v[8:11]\n"
  "s_cmp_eq_u32 s40, 0\n"
  "v_mov_b32 v74, s48\n"
  "v_mfma_f32_16x16x32_bf16 v[12:15], a[220:223], v[36:39], v[12:15]\n"
  "s_waitcnt lgkmcnt(2)\n"
  "s_cbranch_scc1 Lp_c_%=\n"
  "Lp_cb_%=:\n"
  "v_mfma_f32_16x16x32_bf16 v[16:19], a[0:3], v[40:43], 0\n"
  "ds_read_b128 v[36:39], v72 offset:17408\n"
  "v_mfma_f32_16x16x32_bf16 v[20:23], a[56:59], v[40:43], 0\n"
  "v_mfma_f32_16x16x32_bf16 v[24:27], a[112:115], v[40:43], 0\n"
  "v_cvt_pk_bf16_f32 v48, v0, v1\n"
  "v_mfma_f32_16x16x32_bf16 v[28:31], a[168:171], v[40:43], 0\n"
  "s_waitcnt lgkmcnt(2)\n"
  "v_mfma_f32_16x16x32_bf16 v[16:19], a[4:7], v[44:47], v[16:19]\n"
  "ds_read_b128 v[40:43], v72 offset:18432\n"
  "v_cvt_pk_bf16_f32 v49, v2, v3\n"
  "v_mfma_f32_16x16x32_bf16 v[20:23], a[60:63], v[44:47], v[20:23]\n"
  "s_waitcnt vmcnt(0)\n"
  "s_barrier\n"
  "v_mfma_f32_16x16x32_bf16 v[24:27], a[116:119], v[44:47], v[24:27]\n"
  "buffer_store_dword v74, %[fl], %[ws], 0 offen sc1\n"
  "v_cvt_pk_bf16_f32 v50, v4, v5\n"
  "v_mfma_f32_16x16x32_bf16 v[28:31], a[172:175], v[44:47], v[28:31]\n"
  "s_waitcnt lgkmcnt(2)\n"
  "v_mfma_f32_16x16x32_bf16 v[16:19], a[8:11], v[32:35], v[16:19]\n"
  "ds_read_b128 v[44:47], v72 offset:19456\n"
  "s_mov_b32 m0, s46\n"
  "v_cvt_pk_bf16_f32 v51, v6, v7\n"
  "v_mfma_f32_16x16x32_bf16 v[20:23], a[64:67], v[32:35], v[20:23]\n"
  "buffer_load_dwordx4 %[xs], %[ws], s41 offen offset:0 lds\n"
  "v_mfma_f32_16x16x32_bf16 v[24:27], a[120:123], v[32:35], v[24:27]\n"
  "v_cvt_pk_bf16_f32 v52, v8, v9\n"
  "v_mfma_f32_16x16x32_bf16 v[28:31], a[176:179], v[32:35], v[28:31]\n"
  "s_waitcnt lgkmcnt(2)\n"
  "buffer_store_dwordx4 v[48:51], %[o0], %[ws], s43 offen sc1\n"
  "v_mfma_f32_16x16x32_bf16 v[16:19], a[12:15], v[36:39], v[16:19]\n"
  "ds_read_b128 v[32:35], v72 offset:20480\n"
  "v_cvt_pk_bf16_f32 v53, v10, v11\n"
  "v_mfma_f32_16x16x32_bf16 v[20:23], a[68:71], v[36:39], v[20:23]\n"
  "buffer_load_dwordx4 %[xs], %[ws], s41 offen offset:1024 lds\n"
  "v_mfma_f32_16x16x32_bf16 v[24:27], a[124:127], v[36:39], v[24:27]\n"
  "v_cvt_pk_bf16_f32 v54, v12, v13\n"
  "v_mfma_f32_16x16x32_bf16 v[28:31], a[180:183], v[36:39], v[28:31]\n"
  "s_waitcnt lgkmcnt(2)\n"
  "v_mfma_f32_16x16x32_bf16 v[16:19], a[16:19], v[40:43], v[16:19]\n"
  "ds_read_b128 v[36:39], v72 offset:21504\n"
  "v_cvt_pk_bf16_f32 v55, v14, v15\n"
  "v_mfma_f32_16x16x32_bf16 v[20:23], a[72:75], v[40:43], v[20:23]\n"
  "buffer_load_dwordx4 %[xs], %[ws], s41 offen offset:2048 lds\n"
  "v_mfma_f32_16x16x32_bf16 v[24:27], a[128:131], v[40:43], v[24:27]\n"
  "v_mfma_f32_16x16x32_bf16 v[28:31], a[184:187], v[40:43], v[28:31]\n"
  "s_waitcnt lgkmcnt(2)\n"
  "buffer_store_dwordx4 v[52:55], %[o1], %[ws], s43 offen sc1\n"
  "v_mfma_f32_16x16x32_bf16 v[16:19], a[20:23], v[44:47], v[16:19]\n"
  "ds_read_b128 v[40:43], v72 offset:22528\n"
  "v_mfma_f32_16x16x32_bf16 v[20:23], a[76:79], v[44:47], v[20:23]\n"
  "buffer_load_dwordx4 %[xs], %[ws], s41 offen offset:3072 lds\n"
  "v_mfma_f32_16x16x32_bf16 v[24:27], a[132:135], v[44:47], v[24:27]\n"
  "v_mfma_f32_16x16x32_bf16 v[28:31], a[188:191], v[44:47], v[28:31]\n"
  "s_waitcnt lgkmcnt(2)\n"
  "v_mfma_f32_16x16x32_bf16 v[16:19], a[24:27], v[32:35], v[16:19]\n"
  "ds_read_b128 v[44:47], v72 offset:23552\n"
  "s_add_u32 m0, s46, 4096\n"
  "v_mfma_f32_16x16x32_bf16 v[20:23], a[80:83], v[32:35], v[20:23]\n"
  "buffer_load_dwordx4 %[xs], %[ws], s42 offen offset:0 lds\n"
  "v_mfma_f32_16x16x32_bf16 v[24:27], a[136:139], v[32:35], v[24:27]\n"
  "v_mfma_f32_16x16x32_bf16 v[28:31], a[192:195], v[32:35], v[28:31]\n"
  "s_waitcnt lgkmcnt(2)\n"
  "v_mfma_f32_16x16x32_bf16 v[16:19], a[28:31], v[36:39], v[16:19]\n"
  "ds_read_b128 v[32:35], v72 offset:24576\n"
  "v_mfma_f32_16x16x32_bf16 v[20:23], a[84:87], v[36:39], v[20:23]\n"
  "buffer_load_dwordx4 %[xs], %[ws], s42 offen offset:1024 lds\n"
  "v_mfma_f32_16x16x32_bf16 v[24:27], a[140:143], v[36:39], v[24:27]\n"
  "v_mfma_f32_16x16x32_bf16 v[28:31], a[196:199], v[36:39], v[28:31]\n"
  "s_waitcnt lgkmcnt(2)\n"
  "v_mfma_f32_16x16x32_bf16 v[16:19], a[32:35], v[40:43], v[16:19]\n"
  "ds_read_b128 v[36:39], v72 offset:25600\n"
  "v_mfma_f32_16x16x32_bf16 v[20:23], a[88:91], v[40:43], v[20:23]\n"
  "buffer_load_dwordx4 %[xs], %[ws], s42 offen offset:2048 lds\n"
  "v_mfma_f32_16x16x32_bf16 v[24:27], a[144:147], v[40:43], v[24:27]\n"
  "v_mfma_f32_16x16x32_bf16 v[28:31], a[200:203], v[40:43], v[28:31]\n"
  "s_waitcnt lgkmcnt(2)\n"
  "v_mfma_f32_16x16x32_bf16 v[16:19], a[36:39], v[44:47], v[16:19]\n"
  "ds_read_b128 v[40:43], v72 offset:26624\n"
  "v_mfma_f32_16x16x32_bf16 v[20:23], a[92:95], v[44:47], v[20:23]\n"
  "v_mfma_f32_16x16x32_bf16 v[24:27], a[148:151], v[44:47], v[24:27]\n"
  "s_add_u32 s49, s43, 2097152\n"
  "v_mfma_f32_16x16x32_bf16 v[28:31], a[204:207], v[44:47], v[28:31]\n"
  "s_waitcnt lgkmcnt(2)\n"
  "v_mfma_f32_16x16x32_bf16 v[16:19], a[40:43], v[32:35], v[16:19]\n"
  "ds_read_b128 v[44:47], v72 offset:27648\n"
  "v_mfma_f32_16x16x32_bf16 v[20:23], a[96:99], v[32:35], v[20:23]\n"
  "s_xor_b32 s49, s49, 0x2000\n"
  "v_mfma_f32_16x16x32_bf16 v[24:27], a[152:155], v[32:35], v[24:27]\n"
  "s_add_u32 s45, s43, 32768\n"
  "v_mfma_f32_16x16x32_bf16 v[28:31], a[208:211], v[32:35], v[28:31]\n"
  "s_waitcnt lgkmcnt(2)\n"
  "v_mfma_f32_16x16x32_bf16 v[16:19], a[44:47], v[36:39], v[16:19]\n"
  "ds_read_b128 v[32:35], v73 offset:0\n"
  "v_mfma_f32_16x16x32_bf16 v[20:23], a[100:103], v[36:39], v[20:23]\n"
  "s_add_u32 s40, s40, 1\n"
  "v_mfma_f32_16x16x32_bf16 v[24:27], a[156:159], v[36:39], v[24:27]\n"
  "s_cmp_lt_u32 s40, 64\n"
  "v_mfma_f32_16x16x32_bf16 v[28:31], a[212:215], v[36:39], v[28:31]\n"
  "s_waitcnt lgkmcnt(2)\n"
  "v_mfma_f32_16x16x32_bf16 v[16:19], a[48:51], v[40:43], v[16:19]\n"
  "ds_read_b128 v[36:39], v73 offset:1024\n"
  "v_mfma_f32_16x16x32_bf16 v[20:23], a[104:107], v[40:43], v[20:23]\n"
  "v_mfma_f32_16x16x32_bf16 v[24:27], a[160:163], v[40:43], v[24:27]\n"
  "v_mfma_f32_16x16x32_bf16 v[28:31], a[216:219], v[40:43], v[28:31]\n"
  "s_waitcnt lgkmcnt(2)\n"
  "v_mfma_f32_16x16x32_bf16 v[16:19], a[52:55], v[44:47], v[16:19]\n"
  "ds_read_b128 v[40:43], v73 offset:2048\n"
  "v_mov_b32 v72, v73\n"
  "v_mfma_f32_16x16x32_bf16 v[20:23], a[108:111], v[44:47], v[20:23]\n"
  "v_add_u32 v73, s47, %[la]\n"
  "v_mfma_f32_16x16x32_bf16 v[24:27], a[164:167], v[44:47], v[24:27]\n"
  "s_mov_b32 s43, s49\n"
  "v_mfma_f32_16x16x32_bf16 v[28:31], a[220:223], v[44:47], v[28:31]\n"
  "s_waitcnt lgkmcnt(2)\n"
  "s_cbranch_scc1 Lp_top_%=\n"
  "s_nop 7\n"
  "v_cvt_pk_bf16_f32 v48, v16, v17\n"
  "v_cvt_pk_bf16_f32 v49, v18, v19\n"
  "v_cvt_pk_bf16_f32 v50, v20, v21\n"
  "v_cvt_pk_bf16_f32 v51, v22, v23\n"
  "buffer_store_dwordx4 v[48:51], %[o0], %[ws], s45 offen sc1\n"
  "v_cvt_pk_bf16_f32 v52, v24, v25\n"
  "v_cvt_pk_bf16_f32 v53, v26, v27\n"
  "v_cvt_pk_bf16_f32 v54, v28, v29\n"
  "v_cvt_pk_bf16_f32 v55, v30, v31\n"
  "buffer_store_dwordx4 v[52:55], %[o1], %[ws], s45 offen sc1\n"
  "v_mov_b32 v74, 64\n"
  "s_waitcnt vmcnt(0) lgkmcnt(0)\n"
  "s_barrier\n"
  "buffer_store_dword v74, %[fl], %[ws], 0 offen sc1\n"
  "s_branch Lp_end_%=\n"
  "Lp_c_%=:\n"
  "s_mov_b32 s48, 100000\n"
  "s_branch Lp_cr_%=\n"
  "Lp_cw_%=:\n"
  "buffer_load_dword v71, v70, %[ws], 0 offen sc1\n"
  "Lp_cr_%=:\n"
  "s_waitcnt vmcnt(0)\n"
  "v_readfirstlane_b32 s44, v71\n"
  "s_nop 3\n"
  "s_cmp_lg_u32 s44, 0\n"
  "s_cbranch_scc1 Lp_cg_%=\n"
  "s_sub_u32 s48, s48, 1\n"
  "s_cbranch_scc0 Lp_cw_%=\n"
  "Lp_cg_%=:\n"
  "s_lshl_b32 s44, s44, 12\n"
  "s_andn2_b32 s43, 0x2000, s44\n"
  "s_branch Lp_cb_%=\n"
  "Lp_end_%=:\n"
    :: [ws] "s"(ws), [wb] "s"(wb), [xs] "v"(xs), [la] "v"(la), [fl] "v"(fl), [wsrc] "v"(W_WF + p0 * 28672 + perm),
       [o0] "v"(W_KQF + ex((p0 / 14 * 32 + p0 % 14) * 1024) + la), [o1] "v"(W_KQF + ex((p1 / 14 * 32 + p1 % 14) * 1024) + la)
    : "v0", "v1", "v2", "v3", "v4", "v5", "v6", "v7", "v8", "v9", "v10", "v11", "v12", "v13", "v14", "v15", "v16", "v17", "v18", "v19", "v20", "v21", "v22", "v23", "v24", "v25", "v26", "v27", "v28", "v29", "v30", "v31", "v32", "v33", "v34", "v35", "v36", "v37", "v38", "v39", "v40", "v41", "v42", "v43", "v44", "v45", "v46", "v47", "v48", "v49", "v50", "v51", "v52", "v53", "v54", "v55", "v56", "v57", "v58", "v59", "v60", "v61", "v62", "v63", "v64", "v65", "v66", "v67", "v68", "v69", "v70", "v71", "v72", "v73", "v74", "v75", "a0", "a1", "a2", "a3", "a4", "a5", "a6", "a7", "a8", "a9", "a10", "a11", "a12", "a13", "a14", "a15", "a16", "a17", "a18", "a19", "a20", "a21", "a22", "a23", "a24", "a25", "a26", "a27", "a28", "a29", "a30", "a31", "a32", "a33", "a34", "a35", "a36", "a37", "a38", "a39", "a40", "a41", "a42", "a43", "a44", "a45", "a46", "a47", "a48", "a49", "a50", "a51", "a52", "a53", "a54", "a55", "a56", "a57", "a58", "a59", "a60", "a61", "a62", "a63", "a64", "a65", "a66", "a67", "a68", "a69", "a70", "a71", "a72", "a73", "a74", "a75", "a76", "a77", "a78", "a79", "a80", "a81", "a82", "a83", "a84", "a85", "a86", "a87", "a88", "a89", "a90", "a91", "a92", "a93", "a94", "a95", "a96", "a97", "a98", "a99", "a100", "a101", "a102", "a103", "a104", "a105", "a106", "a107", "a108", "a109", "a110", "a111", "a112", "a113", "a114", "a115", "a116", "a117", "a118", "a119", "a120", "a121", "a122", "a123", "a124", "a125", "a126", "a127", "a128", "a129", "a130", "a131", "a132", "a133", "a134", "a135", "a136", "a137", "a138", "a139", "a140", "a141", "a142", "a143", "a144", "a145", "a146", "a147", "a148", "a149", "a150", "a151", "a152", "a153", "a154", "a155", "a156", "a157", "a158", "a159", "a160", "a161", "a162", "a163", "a164", "a165", "a166", "a167", "a168", "a169", "a170", "a171", "a172", "a173", "a174", "a175", "a176", "a177", "a178", "a179", "a180", "a181", "a182", "a183", "a184", "a185", "a186", "a187", "a188", "a189", "a190", "a191", "a192", "a193", "a194", "a195", "a196", "a197", "a198", "a199", "a200", "a201", "a202", "a203", "a204", "a205", "a206", "a207", "a208", "a209", "a210", "a211", "a212", "a213", "a214", "a215", "a216", "a217", "a218", "a219", "a220", "a221", "a222", "a223", "s40", "s41", "s42", "s43", "s44", "s45", "s46", "s47", "s48", "s49", "s50", "s51", "s52", "s53", "s54", "s55", "scc", "memory");
  } else if (KIND == 1) {
    asm volatile(
  "s_mov_b32 s50, 0xbfb8aa3b\n"
  "s_mov_b32 s41, 0\n"
  "s_mov_b32 s42, 4096\n"
  "s_mov_b32 s46, %[wb]\n"
  "s_mov_b32 m0, s46\n"
  "s_nop 0\n"
  "buffer_load_dwordx4 %[xs], %[ws], s41 offen offset:0 lds\n"
  "buffer_load_dwordx4 %[xs], %[ws], s41 offen offset:1024 lds\n"
  "buffer_load_dwordx4 %[xs], %[ws], s41 offen offset:2048 lds\n"
  "buffer_load_dwordx4 %[xs], %[ws], s41 offen offset:3072 lds\n"
  "s_add_u32 m0, s46, 4096\n"
  "s_nop 0\n"
  "buffer_load_dwordx4 %[xs], %[ws], s42 offen offset:0 lds\n"
  "buffer_load_dwordx4 %[xs], %[ws], s42 offen offset:1024 lds\n"
  "buffer_load_dwordx4 %[xs], %[ws], s42 offen offset:2048 lds\n"
  "s_add_u32 s41, s41, 28672\n"
  "s_add_u32 s42, s42, 28672\n"
  "s_add_u32 s46, s46, 28672\n"
  "s_cmp_ge_u32 s46, 86016\n"
  "s_cselect_b32 s48, 86016, 0\n"
  "s_sub_u32 s46, s46, s48\n"
  "s_mov_b32 s48, 0\n"
  "buffer_load_dwordx4 a[0:3], %[wsrc], %[ws], s48 offen\n"
  "s_mov_b32 s48, 401408\n"
  "buffer_load_dwordx4 a[56:59], %[wsrc], %[ws], s48 offen\n"
  "s_mov_b32 s48, 802816\n"
  "buffer_load_dwordx4 a[112:115], %[wsrc], %[ws], s48 offen\n"
  "s_mov_b32 s48, 1204224\n"
  "buffer_load_dwordx4 a[168:171], %[wsrc], %[ws], s48 offen\n"
  "s_mov_b32 s48, 1024\n"
  "buffer_load_dwordx4 a[4:7], %[wsrc], %[ws], s48 offen\n"
  "s_mov_b32 s48, 402432\n"
  "buffer_load_dwordx4 a[60:63], %[wsrc], %[ws], s48 offen\n"
  "s_mov_b32 s48, 803840\n"
  "buffer_load_dwordx4 a[116:119], %[wsrc], %[ws], s48 offen\n"
  "s_mov_b32 s48, 1205248\n"
  "buffer_load_dwordx4 a[172:175], %[wsrc], %[ws], s48 offen\n"
  "s_mov_b32 s48, 2048\n"
  "buffer_load_dwordx4 a[8:11], %[wsrc], %[ws], s48 offen\n"
  "s_mov_b32 s48, 403456\n"
  "buffer_load_dwordx4 a[64:67], %[wsrc], %[ws], s48 offen\n"
  "s_mov_b32 s48, 804864\n"
  "buffer_load_dwordx4 a[120:123], %[wsrc], %[ws], s48 offen\n"
  "s_mov_b32 s48, 1206272\n"
  "buffer_load_dwordx4 a[176:179], %[wsrc], %[ws], s48 offen\n"
  "s_mov_b32 s48, 3072\n"
  "buffer_load_dwordx4 a[12:15], %[wsrc], %[ws], s48 offen\n"
  "s_mov_b32 s48, 404480\n"
  "buffer_load_dwordx4 a[68:71], %[wsrc], %[ws], s48 offen\n"
  "s_mov_b32 s48, 805888\n"
  "buffer_load_dwordx4 a[124:127], %[wsrc], %[ws], s48 offen\n"
  "s_mov_b32 s48, 1207296\n"
  "buffer_load_dwordx4 a[180:183], %[wsrc], %[ws], s48 offen\n"
  "s_mov_b32 s48, 4096\n"
  "buffer_load_dwordx4 a[16:19], %[wsrc], %[ws], s48 offen\n"
  "s_mov_b32 s48, 405504\n"
  "buffer_load_dwordx4 a[72:75], %[wsrc], %[ws], s48 offen\n"
  "s_mov_b32 s48, 806912\n"
  "buffer_load_dwordx4 a[128:131], %[wsrc], %[ws], s48 offen\n"
  "s_mov_b32 s48, 1208320\n"
  "buffer_load_dwordx4 a[184:187], %[wsrc], %[ws], s48 offen\n"
  "s_mov_b32 s48, 5120\n"
  "buffer_load_dwordx4 a[20:23], %[wsrc], %[ws], s48 offen\n"
  "s_mov_b32 s48, 406528\n"
  "buffer_load_dwordx4 a[76:79], %[wsrc], %[ws], s48 offen\n"
  "s_mov_b32 s48, 807936\n"
  "buffer_load_dwordx4 a[132:135], %[wsrc], %[ws], s48 offen\n"
  "s_mov_b32 s48, 1209344\n"
  "buffer_load_dwordx4 a[188:191], %[wsrc], %[ws], s48 offen\n"
  "s_mov_b32 s48, 6144\n"
  "buffer_load_dwordx4 a[24:27], %[wsrc], %[ws], s48 offen\n"
  "s_mov_b32 s48, 407552\n"
  "buffer_load_dwordx4 a[80:83], %[wsrc], %[ws], s48 offen\n"
  "s_mov_b32 s48, 808960\n"
  "buffer_load_dwordx4 a[136:139], %[wsrc], %[ws], s48 offen\n"
  "s_mov_b32 s48, 1210368\n"
  "buffer_load_dwordx4 a[192:195], %[wsrc], %[ws], s48 offen\n"
  "s_mov_b32 s48, 7168\n"
  "buffer_load_dwordx4 a[28:31], %[wsrc], %[ws], s48 offen\n"
  "s_mov_b32 s48, 408576\n"
  "buffer_load_dwordx4 a[84:87], %[wsrc], %[ws], s48 offen\n"
  "s_mov_b32 s48, 809984\n"
  "buffer_load_dwordx4 a[140:143], %[wsrc], %[ws], s48 offen\n"
  "s_mov_b32 s48, 1211392\n"
  "buffer_load_dwordx4 a[196:199], %[wsrc], %[ws], s48 offen\n"
  "s_mov_b32 s48, 8192\n"
  "buffer_load_dwordx4 a[32:35], %[wsrc], %[ws], s48 offen\n"
  "s_mov_b32 s48, 409600\n"
  "buffer_load_dwordx4 a[88:91], %[wsrc], %[ws], s48 offen\n"
  "s_mov_b32 s48, 811008\n"
  "buffer_load_dwordx4 a[144:147], %[wsrc], %[ws], s48 offen\n"
  "s_mov_b32 s48, 1212416\n"
  "buffer_load_dwordx4 a[200:203], %[wsrc], %[ws], s48 offen\n"
  "s_mov_b32 s48, 9216\n"
  "buffer_load_dwordx4 a[36:39], %[wsrc], %[ws], s48 offen\n"
  "s_mov_b32 s48, 410624\n"
  "buffer_load_dwordx4 a[92:95], %[wsrc], %[ws], s48 offen\n"
  "s_mov_b32 s48, 812032\n"
  "buffer_load_dwordx4 a[148:151], %[wsrc], %[ws], s48 offen\n"
  "s_mov_b32 s48, 1213440\n"
  "buffer_load_dwordx4 a[204:207], %[wsrc], %[ws], s48 offen\n"
  "s_mov_b32 s48, 10240\n"
  "buffer_load_dwordx4 a[40:43], %[wsrc], %[ws], s48 offen\n"
  "s_mov_b32 s48, 411648\n"
  "buffer_load_dwordx4 a[96:99], %[wsrc], %[ws], s48 offen\n"
  "s_mov_b32 s48, 813056\n"
  "buffer_load_dwordx4 a[152:155], %[wsrc], %[ws], s48 offen\n"
  "s_mov_b32 s48, 1214464\n"
  "buffer_load_dwordx4 a[208:211], %[wsrc], %[ws], s48 offen\n"
  "s_mov_b32 s48, 11264\n"
  "buffer_load_dwordx4 a[44:47], %[wsrc], %[ws], s48 offen\n"
  "s_mov_b32 s48, 412672\n"
  "buffer_load_dwordx4 a[100:103], %[wsrc], %[ws], s48 offen\n"
  "s_mov_b32 s48, 814080\n"
  "buffer_load_dwordx4 a[156:159], %[wsrc], %[ws], s48 offen\n"
  "s_mov_b32 s48, 1215488\n"
  "buffer_load_dwordx4 a[212:215], %[wsrc], %[ws], s48 offen\n"
  "s_mov_b32 s48, 12288\n"
  "buffer_load_dwordx4 a[48:51], %[wsrc], %[ws], s48 offen\n"
  "s_mov_b32 s48, 413696\n"
  "buffer_load_dwordx4 a[104:107], %[wsrc], %[ws], s48 offen\n"
  "s_mov_b32 s48, 815104\n"
  "buffer_load_dwordx4 a[160:163], %[wsrc], %[ws], s48 offen\n"
  "s_mov_b32 s48, 1216512\n"
  "buffer_load_dwordx4 a[216:219], %[wsrc], %[ws], s48 offen\n"
  "s_mov_b32 s48, 13312\n"
  "buffer_load_dwordx4 a[52:55], %[wsrc], %[ws], s48 offen\n"
  "s_mov_b32 s48, 414720\n"
  "buffer_load_dwordx4 a[108:111], %[wsrc], %[ws], s48 offen\n"
  "s_mov_b32 s48, 816128\n"
  "buffer_load_dwordx4 a[164:167], %[wsrc], %[ws], s48 offen\n"
  "s_mov_b32 s48, 1217536\n"
  "buffer_load_dwordx4 a[220:223], %[wsrc], %[ws], s48 offen\n"
  "s_mov_b32 s40, 0\n"
  "s_mov_b32 s45, 0x40000000\n"
  "s_mov_b32 s47, 28672\n"
  "v_mov_b32 v72, %[la]\n"
  "v_add_u32 v73, 28672, %[la]\n"
  "s_waitcnt vmcnt(54)\n"
  "s_barrier\n"
  "s_mov_b32 m0, s46\n"
  "s_nop 0\n"
  "buffer_load_dwordx4 %[xs], %[ws], s41 offen offset:0 lds\n"
  "buffer_load_dwordx4 %[xs], %[ws], s41 offen offset:1024 lds\n"
  "buffer_load_dwordx4 %[xs], %[ws], s41 offen offset:2048 lds\n"
  "buffer_load_dwordx4 %[xs], %[ws], s41 offen offset:3072 lds\n"
  "s_add_u32 m0, s46, 4096\n"
  "s_nop 0\n"
  "buffer_load_dwordx4 %[xs], %[ws], s42 offen offset:0 lds\n"
  "buffer_load_dwordx4 %[xs], %[ws], s42 offen offset:1024 lds\n"
  "buffer_load_dwordx4 %[xs], %[ws], s42 offen offset:2048 lds\n"
  "v_mov_b32 v70, 2155520\n"
  "buffer_load_dword v71, v70, %[ws], 0 offen sc1\n"
  "ds_read_b128 v[32:35], v72 offset:0\n"
  "ds_read_b128 v[36:39], v72 offset:1024\n"
  "ds_read_b128 v[40:43], v72 offset:2048\n"
  "s_waitcnt vmcnt(60) lgkmcnt(2)\n"
  ".p2align 6\n"
  "Lp_top_%=:\n"
  "v_mfma_f32_16x16x32_bf16 v[0:3], v[32:35], a[0:3], 0\n"
  "ds_read_b128 v[44:47], v72 offset:3072\n"
  "v_mfma_f32_16x16x32_bf16 v[4:7], v[32:35], a[56:59], 0\n"
  "v_mfma_f32_16x16x32_bf16 v[8:11], v[32:35], a[112:115], 0\n"
  "v_cvt_pk_bf16_f32 v50, v16, v17\n"
  "v_mfma_f32_16x16x32_bf16 v[12:15], v[32:35], a[168:171], 0\n"
  "s_waitcnt vmcnt(56) lgkmcnt(2)\n"
  "v_mfma_f32_16x16x32_bf16 v[0:3], v[36:39], a[4:7], v[0:3]\n"
  "ds_read_b128 v[32:35], v72 offset:4096\n"
  "v_cvt_pk_bf16_f32 v51, v18, v19\n"
  "v_mfma_f32_16x16x32_bf16 v[4:7], v[36:39], a[60:63], v[4:7]\n"
  "v_mfma_f32_16x16x32_bf16 v[8:11], v[36:39], a[116:119], v[8:11]\n"
  "v_cvt_pk_bf16_f32 v54, v20, v21\n"
  "v_mfma_f32_16x16x32_bf16 v[12:15], v[36:39], a[172:175], v[12:15]\n"
  "s_waitcnt vmcnt(52) lgkmcnt(2)\n"
  "buffer_store_dwordx4 v[48:51], %[o0], %[ws], s45 offen offset:0 sc1\n"
  "v_mfma_f32_16x16x32_bf16 v[0:3], v[40:43], a[8:11], v[0:3]\n"
  "ds_read_b128 v[36:39], v72 offset:5120\n"
  "v_cvt_pk_bf16_f32 v55, v22, v23\n"
  "v_mfma_f32_16x16x32_bf16 v[4:7], v[40:43], a[64:67], v[4:7]\n"
  "v_mfma_f32_16x16x32_bf16 v[8:11], v[40:43], a[120:123], v[8:11]\n"
  "v_cvt_pk_bf16_f32 v58, v24, v25\n"
  "v_mfma_f32_16x16x32_bf16 v[12:15], v[40:43], a[176:179], v[12:15]\n"
  "s_waitcnt vmcnt(48) lgkmcnt(2)\n"
  "buffer_store_dwordx4 v[52:55], %[o0], %[ws], s45 offen offset:1024 sc1\n"
  "v_mfma_f32_16x16x32_bf16 v[0:3], v[44:47], a[12:15], v[0:3]\n"
  "ds_read_b128 v[40:43], v72 offset:6144\n"
  "v_cvt_pk_bf16_f32 v59, v26, v27\n"
  "v_mfma_f32_16x16x32_bf16 v[4:7], v[44:47], a[68:71], v[4:7]\n"
  "v_mfma_f32_16x16x32_bf16 v[8:11], v[44:47], a[124:127], v[8:11]\n"
  "v_cvt_pk_bf16_f32 v62, v28, v29\n"
  "v_mfma_f32_16x16x32_bf16 v[12:15], v[44:47], a[180:183], v[12:15]\n"
  "s_waitcnt vmcnt(44) lgkmcnt(2)\n"
  "buffer_store_dwordx4 v[56:59], %[o0], %[ws], s45 offen offset:2048 sc1\n"
  "v_mfma_f32_16x16x32_bf16 v[0:3], v[32:35], a[16:19], v[0:3]\n"
  "ds_read_b128 v[44:47], v72 offset:7168\n"
  "v_cvt_pk_bf16_f32 v63, v30, v31\n"
  "v_mfma_f32_16x16x32_bf16 v[4:7], v[32:35], a[72:75], v[4:7]\n"
  "v_mfma_f32_16x16x32_bf16 v[8:11], v[32:35], a[128:131], v[8:11]\n"
  "v_mfma_f32_16x16x32_bf16 v[12:15], v[32:35], a[184:187], v[12:15]\n"
  "s_waitcnt vmcnt(40) lgkmcnt(2)\n"
  "buffer_store_dwordx4 v[60:63], %[o0], %[ws], s45 offen offset:3072 sc1\n"
  "v_mfma_f32_16x16x32_bf16 v[0:3], v[36:39], a[20:23], v[0:3]\n"
  "ds_read_b128 v[32:35], v72 offset:8192\n"
  "v_mfma_f32_16x16x32_bf16 v[4:7], v[36:39], a[76:79], v[4:7]\n"
  "s_add_u32 s41, s41, 28672\n"
  "v_mfma_f32_16x16x32_bf16 v[8:11], v[36:39], a[132:135], v[8:11]\n"
  "s_add_u32 s42, s42, 28672\n"
  "v_mfma_f32_16x16x32_bf16 v[12:15], v[36:39], a[188:191], v[12:15]\n"
  "s_waitcnt vmcnt(36) lgkmcnt(2)\n"
  "v_mfma_f32_16x16x32_bf16 v[0:3], v[40:43], a[24:27], v[0:3]\n"
  "ds_read_b128 v[36:39], v72 offset:9216\n"
  "v_mfma_f32_16x16x32_bf16 v[4:7], v[40:43], a[80:83], v[4:7]\n"
  "s_add_u32 s46, s46, 28672\n"
  "v_mfma_f32_16x16x32_bf16 v[8:11], v[40:43], a[136:139], v[8:11]\n"
  "s_cmp_ge_u32 s46, 86016\n"
  "v_mfma_f32_16x16x32_bf16 v[12:15], v[40:43], a[192:195], v[12:15]\n"
  "s_waitcnt vmcnt(32) lgkmcnt(2)\n"
  "v_mfma_f32_16x16x32_bf16 v[0:3], v[44:47], a[28:31], v[0:3]\n"
  "ds_read_b128 v[40:43], v72 offset:10240\n"
  "v_mfma_f32_16x16x32_bf16 v[4:7], v[44:47], a[84:87], v[4:7]\n"
  "s_cselect_b32 s48, 86016, 0\n"
  "v_mfma_f32_16x16x32_bf16 v[8:11], v[44:47], a[140:143], v[8:11]\n"
  "s_sub_u32 s46, s46, s48\n"
  "v_mfma_f32_16x16x32_bf16 v[12:15], v[44:47], a[196:199], v[12:15]\n"
  "s_waitcnt vmcnt(28) lgkmcnt(2)\n"
  "v_mfma_f32_16x16x32_bf16 v[0:3], v[32:35], a[32:35], v[0:3]\n"
  "ds_read_b128 v[44:47], v72 offset:11264\n"
  "v_mfma_f32_16x16x32_bf16 v[4:7], v[32:35], a[88:91], v[4:7]\n"
  "s_add_u32 s47, s47, 28672\n"
  "v_mfma_f32_16x16x32_bf16 v[8:11], v[32:35], a[144:147], v[8:11]\n"
  "s_cmp_ge_u32 s47, 86016\n"
  "v_mfma_f32_16x16x32_bf16 v[12:15], v[32:35], a[200:203], v[12:15]\n"
  "s_waitcnt vmcnt(24) lgkmcnt(2)\n"
  "v_mfma_f32_16x16x32_bf16 v[0:3], v[36:39], a[36:39], v[0:3]\n"
  "ds_read_b128 v[32:35], v72 offset:12288\n"
  "v_mfma_f32_16x16x32_bf16 v[4:7], v[36:39], a[92:95], v[4:7]\n"
  "s_cselect_b32 s47, 0, s47\n"
  "v_mfma_f32_16x16x32_bf16 v[8:11], v[36:39], a[148:151], v[8:11]\n"
  "s_sub_i32 s48, s40, 0\n"
  "v_mfma_f32_16x16x32_bf16 v[12:15], v[36:39], a[204:207], v[12:15]\n"
  "s_waitcnt vmcnt(20) lgkmcnt(2)\n"
  "v_mfma_f32_16x16x32_bf16 v[0:3], v[40:43], a[40:43], v[0:3]\n"
  "ds_read_b128 v[36:39], v72 offset:13312\n"
  "v_mfma_f32_16x16x32_bf16 v[4:7], v[40:43], a[96:99], v[4:7]\n"
  "s_max_i32 s48, s48, 0\n"
  "v_mfma_f32_16x16x32_bf16 v[8:11], v[40:43], a[152:155], v[8:11]\n"
  "v_mfma_f32_16x16x32_bf16 v[12:15], v[40:43], a[208:211], v[12:15]\n"
  "s_waitcnt vmcnt(16) lgkmcnt(2)\n"
  "v_mfma_f32_16x16x32_bf16 v[0:3], v[44:47], a[44:47], v[0:3]\n"
  "ds_read_b128 v[40:43], v72 offset:14336\n"
  "v_mfma_f32_16x16x32_bf16 v[4:7], v[44:47], a[100:103], v[4:7]\n"
  "v_mfma_f32_16x16x32_bf16 v[8:11], v[44:47], a[156:159], v[8:11]\n"
  "v_mfma_f32_16x16x32_bf16 v[12:15], v[44:47], a[212:215], v[12:15]\n"
  "s_waitcnt vmcnt(12) lgkmcnt(2)\n"
  "v_mfma_f32_16x16x32_bf16 v[0:3], v[32:35], a[48:51], v[0:3]\n"
  "ds_read_b128 v[44:47], v72 offset:15360\n"
  "v_mfma_f32_16x16x32_bf16 v[4:7], v[32:35], a[104:107], v[4:7]\n"
  "v_mfma_f32_16x16x32_bf16 v[8:11], v[32:35], a[160:163], v[8:11]\n"
  "v_mfma_f32_16x16x32_bf16 v[12:15], v[32:35], a[216:219], v[12:15]\n"
  "s_waitcnt vmcnt(8) lgkmcnt(2)\n"
  "v_mfma_f32_16x16x32_bf16 v[0:3], v[36:39], a[52:55], v[0:3]\n"
  "ds_read_b128 v[32:35], v72 offset:16384\n"
  "v_mfma_f32_16x16x32_bf16 v[4:7], v[36:39], a[108:111], v[4:7]\n"
  "v_mfma_f32_16x16x32_bf16 v[8:11], v[36:39], a[164:167], v[8:11]\n"
  "s_cmp_eq_u32 s40, 0\n"
  "v_mov_b32 v74, s48\n"
  "v_mfma_f32_16x16x32_bf16 v[12:15], v[36:39], a[220:223], v[12:15]\n"
  "s_waitcnt lgkmcnt(2)\n"
  "s_cbranch_scc1 Lp_c_%=\n"
  "Lp_cb_%=:\n"
  "v_mfma_f32_16x16x32_bf16 v[16:19], v[40:43], a[0:3], 0\n"
  "ds_read_b128 v[36:39], v72 offset:17408\n"
  "v_mfma_f32_16x16x32_bf16 v[20:23], v[40:43], a[56:59], 0\n"
  "v_mfma_f32_16x16x32_bf16 v[24:27], v[40:43], a[112:115], 0\n"
  "v_cvt_pk_bf16_f32 v48, v0, v1\n"
  "v_mfma_f32_16x16x32_bf16 v[28:31], v[40:43], a[168:171], 0\n"
  "s_waitcnt lgkmcnt(2)\n"
  "v_mfma_f32_16x16x32_bf16 v[16:19], v[44:47], a[4:7], v[16:19]\n"
  "ds_read_b128 v[40:43], v72 offset:18432\n"
  "v_cvt_pk_bf16_f32 v49, v2, v3\n"
  "v_mfma_f32_16x16x32_bf16 v[20:23], v[44:47], a[60:63], v[20:23]\n"
  "s_waitcnt vmcnt(0)\n"
  "s_barrier\n"
  "v_mfma_f32_16x16x32_bf16 v[24:27], v[44:47], a[116:119], v[24:27]\n"
  "buffer_store_dword v74, %[fl], %[ws], 0 offen sc1\n"
  "v_cvt_pk_bf16_f32 v52, v4, v5\n"
  "v_mfma_f32_16x16x32_bf16 v[28:31], v[44:47], a[172:175], v[28:31]\n"
  "s_waitcnt lgkmcnt(2)\n"
  "v_mfma_f32_16x16x32_bf16 v[16:19], v[32:35], a[8:11], v[16:19]\n"
  "ds_read_b128 v[44:47], v72 offset:19456\n"
  "s_mov_b32 m0, s46\n"
  "v_cvt_pk_bf16_f32 v53, v6, v7\n"
  "v_mfma_f32_16x16x32_bf16 v[20:23], v[32:35], a[64:67], v[20:23]\n"
  "buffer_load_dwordx4 %[xs], %[ws], s41 offen offset:0 lds\n"
  "v_mfma_f32_16x16x32_bf16 v[24:27], v[32:35], a[120:123], v[24:27]\n"
  "v_cvt_pk_bf16_f32 v56, v8, v9\n"
  "v_mfma_f32_16x16x32_bf16 v[28:31], v[32:35], a[176:179], v[28:31]\n"
  "s_waitcnt lgkmcnt(2)\n"
  "v_mfma_f32_16x16x32_bf16 v[16:19], v[36:39], a[12:15], v[16:19]\n"
  "ds_read_b128 v[32:35], v72 offset:20480\n"
  "v_cvt_pk_bf16_f32 v57, v10, v11\n"
  "v_mfma_f32_16x16x32_bf16 v[20:23], v[36:39], a[68:71], v[20:23]\n"
  "buffer_load_dwordx4 %[xs], %[ws], s41 offen offset:1024 lds\n"
  "v_mfma_f32_16x16x32_bf16 v[24:27], v[36:39], a[124:127], v[24:27]\n"
  "v_cvt_pk_bf16_f32 v60, v12, v13\n"
  "v_mfma_f32_16x16x32_bf16 v[28:31], v[36:39], a[180:183], v[28:31]\n"
  "s_waitcnt lgkmcnt(2)\n"
  "v_mfma_f32_16x16x32_bf16 v[16:19], v[40:43], a[16:19], v[16:19]\n"
  "ds_read_b128 v[36:39], v72 offset:21504\n"
  "v_cvt_pk_bf16_f32 v61, v14, v15\n"
  "v_mfma_f32_16x16x32_bf16 v[20:23], v[40:43], a[72:75], v[20:23]\n"
  "buffer_load_dwordx4 %[xs], %[ws], s41 offen offset:2048 lds\n"
  "v_mfma_f32_16x16x32_bf16 v[24:27], v[40:43], a[128:131], v[24:27]\n"
  "v_mfma_f32_16x16x32_bf16 v[28:31], v[40:43], a[184:187], v[28:31]\n"
  "s_waitcnt lgkmcnt(2)\n"
  "v_mfma_f32_16x16x32_bf16 v[16:19], v[44:47], a[20:23], v[16:19]\n"
  "ds_read_b128 v[40:43], v72 offset:22528\n"
  "v_mfma_f32_16x16x32_bf16 v[20:23], v[44:47], a[76:79], v[20:23]\n"
  "buffer_load_dwordx4 %[xs], %[ws], s41 offen offset:3072 lds\n"
  "v_mfma_f32_16x16x32_bf16 v[24:27], v[44:47], a[132:135], v[24:27]\n"
  "v_mfma_f32_16x16x32_bf16 v[28:31], v[44:47], a[188:191], v[28:31]\n"
  "s_waitcnt lgkmcnt(2)\n"
  "v_mfma_f32_16x16x32_bf16 v[16:19], v[32:35], a[24:27], v[16:19]\n"
  "ds_read_b128 v[44:47], v72 offset:23552\n"
  "s_add_u32 m0, s46, 4096\n"
  "v_mfma_f32_16x16x32_bf16 v[20:23], v[32:35], a[80:83], v[20:23]\n"
  "buffer_load_dwordx4 %[xs], %[ws], s42 offen offset:0 lds\n"
  "v_mfma_f32_16x16x32_bf16 v[24:27], v[32:35], a[136:139], v[24:27]\n"
  "v_mfma_f32_16x16x32_bf16 v[28:31], v[32:35], a[192:195], v[28:31]\n"
  "s_waitcnt lgkmcnt(2)\n"
  "v_mfma_f32_16x16x32_bf16 v[16:19], v[36:39], a[28:31], v[16:19]\n"
  "ds_read_b128 v[32:35], v72 offset:24576\n"
  "v_mfma_f32_16x16x32_bf16 v[20:23], v[36:39], a[84:87], v[20:23]\n"
  "buffer_load_dwordx4 %[xs], %[ws], s42 offen offset:1024 lds\n"
  "v_mfma_f32_16x16x32_bf16 v[24:27], v[36:39], a[140:143], v[24:27]\n"
  "v_mfma_f32_16x16x32_bf16 v[28:31], v[36:39], a[196:199], v[28:31]\n"
  "s_waitcnt lgkmcnt(2)\n"
  "v_mfma_f32_16x16x32_bf16 v[16:19], v[40:43], a[32:35], v[16:19]\n"
  "ds_read_b128 v[36:39], v72 offset:25600\n"
  "v_mfma_f32_16x16x32_bf16 v[20:23], v[40:43], a[88:91], v[20:23]\n"
  "buffer_load_dwordx4 %[xs], %[ws], s42 offen offset:2048 lds\n"
  "v_mfma_f32_16x16x32_bf16 v[24:27], v[40:43], a[144:147], v[24:27]\n"
  "v_mfma_f32_16x16x32_bf16 v[28:31], v[40:43], a[200:203], v[28:31]\n"
  "s_waitcnt lgkmcnt(2)\n"
  "v_mfma_f32_16x16x32_bf16 v[16:19], v[44:47], a[36:39], v[16:19]\n"
  "ds_read_b128 v[40:43], v72 offset:26624\n"
  "v_mfma_f32_16x16x32_bf16 v[20:23], v[44:47], a[92:95], v[20:23]\n"
  "v_mfma_f32_16x16x32_bf16 v[24:27], v[44:47], a[148:151], v[24:27]\n"
  "s_add_u32 s49, s43, 2097152\n"
  "v_mfma_f32_16x16x32_bf16 v[28:31], v[44:47], a[204:207], v[28:31]\n"
  "s_waitcnt lgkmcnt(2)\n"
  "v_mfma_f32_16x16x32_bf16 v[16:19], v[32:35], a[40:43], v[16:19]\n"
  "ds_read_b128 v[44:47], v72 offset:27648\n"
  "v_mfma_f32_16x16x32_bf16 v[20:23], v[32:35], a[96:99], v[20:23]\n"
  "s_xor_b32 s49, s49, 0x2000\n"
  "v_mfma_f32_16x16x32_bf16 v[24:27], v[32:35], a[152:155], v[24:27]\n"
  "s_add_u32 s45, s43, 0\n"
  "v_mfma_f32_16x16x32_bf16 v[28:31], v[32:35], a[208:211], v[28:31]\n"
  "s_waitcnt lgkmcnt(2)\n"
  "v_mfma_f32_16x16x32_bf16 v[16:19], v[36:39], a[44:47], v[16:19]\n"
  "ds_read_b128 v[32:35], v73 offset:0\n"
  "v_mfma_f32_16x16x32_bf16 v[20:23], v[36:39], a[100:103], v[20:23]\n"
  "s_add_u32 s40, s40, 1\n"
  "v_mfma_f32_16x16x32_bf16 v[24:27], v[36:39], a[156:159], v[24:27]\n"
  "s_cmp_lt_u32 s40, 64\n"
  "v_mfma_f32_16x16x32_bf16 v[28:31], v[36:39], a[212:215], v[28:31]\n"
  "s_waitcnt lgkmcnt(2)\n"
  "v_mfma_f32_16x16x32_bf16 v[16:19], v[40:43], a[48:51], v[16:19]\n"
  "ds_read_b128 v[36:39], v73 offset:1024\n"
  "v_mfma_f32_16x16x32_bf16 v[20:23], v[40:43], a[104:107], v[20:23]\n"
  "v_mfma_f32_16x16x32_bf16 v[24:27], v[40:43], a[160:163], v[24:27]\n"
  "v_mfma_f32_16x16x32_bf16 v[28:31], v[40:43], a[216:219], v[28:31]\n"
  "s_waitcnt lgkmcnt(2)\n"
  "v_mfma_f32_16x16x32_bf16 v[16:19], v[44:47], a[52:55], v[16:19]\n"
  "ds_read_b128 v[40:43], v73 offset:2048\n"
  "v_mov_b32 v72, v73\n"
  "v_mfma_f32_16x16x32_bf16 v[20:23], v[44:47], a[108:111], v[20:23]\n"
  "v_add_u32 v73, s47, %[la]\n"
  "v_mfma_f32_16x16x32_bf16 v[24:27], v[44:47], a[164:167], v[24:27]\n"
  "s_mov_b32 s43, s49\n"
  "v_mfma_f32_16x16x32_bf16 v[28:31], v[44:47], a[220:223], v[28:31]\n"
  "s_waitcnt lgkmcnt(2)\n"
  "s_cbranch_scc1 Lp_top_%=\n"
  "s_nop 7\n"
  "v_cvt_pk_bf16_f32 v50, v16, v17\n"
  "v_cvt_pk_bf16_f32 v51, v18, v19\n"
  "buffer_store_dwordx4 v[48:51], %[o0], %[ws], s45 offen offset:0 sc1\n"
  "v_cvt_pk_bf16_f32 v54, v20, v21\n"
  "v_cvt_pk_bf16_f32 v55, v22, v23\n"
  "buffer_store_dwordx4 v[52:55], %[o0], %[ws], s45 offen offset:1024 sc1\n"
  "v_cvt_pk_bf16_f32 v58, v24, v25\n"
  "v_cvt_pk_bf16_f32 v59, v26, v27\n"
  "buffer_store_dwordx4 v[56:59], %[o0], %[ws], s45 offen offset:2048 sc1\n"
  "v_cvt_pk_bf16_f32 v62, v28, v29\n"
  "v_cvt_pk_bf16_f32 v63, v30, v31\n"
  "buffer_store_dwordx4 v[60:63], %[o0], %[ws], s45 offen offset:3072 sc1\n"
  "v_mov_b32 v74, 64\n"
  "s_waitcnt vmcnt(0) lgkmcnt(0)\n"
  "s_barrier\n"
  "buffer_store_dword v74, %[fl], %[ws], 0 offen sc1\n"
  "s_branch Lp_end_%=\n"
  "Lp_c_%=:\n"
  "s_mov_b32 s48, 100000\n"
  "s_branch Lp_cr_%=\n"
  "Lp_cw_%=:\n"
  "buffer_load_dword v71, v70, %[ws], 0 offen sc1\n"
  "Lp_cr_%=:\n"
  "s_waitcnt vmcnt(0)\n"
  "v_readfirstlane_b32 s44, v71\n"
  "s_nop 3\n"
  "s_cmp_lg_u32 s44, 0\n"
  "s_cbranch_scc1 Lp_cg_%=\n"
  "s_sub_u32 s48, s48, 1\n"
  "s_cbranch_scc0 Lp_cw_%=\n"
  "Lp_cg_%=:\n"
  "s_lshl_b32 s44, s44, 12\n"
  "s_andn2_b32 s43, 0x2000, s44\n"
  "s_branch Lp_cb_%=\n"
  "Lp_end_%=:\n"
    :: [ws] "s"(ws), [wb] "s"(wb), [xs] "v"(xs), [la] "v"(la), [fl] "v"(fl), [wsrc] "v"(W_WF + ((16 + 4 * w) * 28 + idx) * 14336 + la),
       [o0] "v"(W_VOF + ex((idx * 16 + 4 * w) * 1024) + la)
    : "v0", "v1", "v2", "v3", "v4", "v5", "v6", "v7", "v8", "v9", "v10", "v11", "v12", "v13", "v14", "v15", "v16", "v17", "v18", "v19", "v20", "v21", "v22", "v23", "v24", "v25", "v26", "v27", "v28", "v29", "v30", "v31", "v32", "v33", "v34", "v35", "v36", "v37", "v38", "v39", "v40", "v41", "v42", "v43", "v44", "v45", "v46", "v47", "v48", "v49", "v50", "v51", "v52", "v53", "v54", "v55", "v56", "v57", "v58", "v59", "v60", "v61", "v62", "v63", "v64", "v65", "v66", "v67", "v68", "v69", "v70", "v71", "v72", "v73", "v74", "v75", "a0", "a1", "a2", "a3", "a4", "a5", "a6", "a7", "a8", "a9", "a10", "a11", "a12", "a13", "a14", "a15", "a16", "a17", "a18", "a19", "a20", "a21", "a22", "a23", "a24", "a25", "a26", "a27", "a28", "a29", "a30", "a31", "a32", "a33", "a34", "a35", "a36", "a37", "a38", "a39", "a40", "a41", "a42", "a43", "a44", "a45", "a46", "a47", "a48", "a49", "a50", "a51", "a52", "a53", "a54", "a55", "a56", "a57", "a58", "a59", "a60", "a61", "a62", "a63", "a64", "a65", "a66", "a67", "a68", "a69", "a70", "a71", "a72", "a73", "a74", "a75", "a76", "a77", "a78", "a79", "a80", "a81", "a82", "a83", "a84", "a85", "a86", "a87", "a88", "a89", "a90", "a91", "a92", "a93", "a94", "a95", "a96", "a97", "a98", "a99", "a100", "a101", "a102", "a103", "a104", "a105", "a106", "a107", "a108", "a109", "a110", "a111", "a112", "a113", "a114", "a115", "a116", "a117", "a118", "a119", "a120", "a121", "a122", "a123", "a124", "a125", "a126", "a127", "a128", "a129", "a130", "a131", "a132", "a133", "a134", "a135", "a136", "a137", "a138", "a139", "a140", "a141", "a142", "a143", "a144", "a145", "a146", "a147", "a148", "a149", "a150", "a151", "a152", "a153", "a154", "a155", "a156", "a157", "a158", "a159", "a160", "a161", "a162", "a163", "a164", "a165", "a166", "a167", "a168", "a169", "a170", "a171", "a172", "a173", "a174", "a175", "a176", "a177", "a178", "a179", "a180", "a181", "a182", "a183", "a184", "a185", "a186", "a187", "a188", "a189", "a190", "a191", "a192", "a193", "a194", "a195", "a196", "a197", "a198", "a199", "a200", "a201", "a202", "a203", "a204", "a205", "a206", "a207", "a208", "a209", "a210", "a211", "a212", "a213", "a214", "a215", "a216", "a217", "a218", "a219", "a220", "a221", "a222", "a223", "s40", "s41", "s42", "s43", "s44", "s45", "s46", "s47", "s48", "s49", "s50", "s51", "s52", "s53", "s54", "s55", "scc", "memory");
  } else {
    asm volatile(
  "s_mov_b32 s50, 0xbfb8aa3b\n"
  "s_lshl_b32 s46, %[wb], 2\n"
  "s_add_u32 m0, s46, 0\n"
  "s_mov_b32 s48, 0\n"
  "buffer_load_dwordx4 %[wsrc], %[r0], s48 offen lds\n"
  "s_add_u32 m0, s46, 1024\n"
  "s_mov_b32 s48, 57344\n"
  "buffer_load_dwordx4 %[wsrc], %[r0], s48 offen lds\n"
  "s_add_u32 m0, s46, 2048\n"
  "s_mov_b32 s48, 114688\n"
  "buffer_load_dwordx4 %[wsrc], %[r0], s48 offen lds\n"
  "s_add_u32 m0, s46, 3072\n"
  "s_mov_b32 s48, 172032\n"
  "buffer_load_dwordx4 %[wsrc], %[r0], s48 offen lds\n"
  "s_add_u32 m0, s46, 4096\n"
  "s_mov_b32 s48, 229376\n"
  "buffer_load_dwordx4 %[wsrc], %[r0], s48 offen lds\n"
  "s_add_u32 m0, s46, 5120\n"
  "s_mov_b32 s48, 286720\n"
  "buffer_load_dwordx4 %[wsrc], %[r0], s48 offen lds\n"
  "s_add_u32 m0, s46, 6144\n"
  "s_mov_b32 s48, 344064\n"
  "buffer_load_dwordx4 %[wsrc], %[r0], s48 offen lds\n"
  "s_add_u32 m0, s46, 7168\n"
  "s_mov_b32 s48, 401408\n"
  "buffer_load_dwordx4 %[wsrc], %[r0], s48 offen lds\n"
  "s_add_u32 m0, s46, 8192\n"
  "s_mov_b32 s48, 458752\n"
  "buffer_load_dwordx4 %[wsrc], %[r0], s48 offen lds\n"
  "s_add_u32 m0, s46, 9216\n"
  "s_mov_b32 s48, 516096\n"
  "buffer_load_dwordx4 %[wsrc], %[r0], s48 offen lds\n"
  "s_add_u32 m0, s46, 10240\n"
  "s_mov_b32 s48, 573440\n"
  "buffer_load_dwordx4 %[wsrc], %[r0], s48 offen lds\n"
  "s_add_u32 m0, s46, 11264\n"
  "s_mov_b32 s48, 630784\n"
  "buffer_load_dwordx4 %[wsrc], %[r0], s48 offen lds\n"
  "s_add_u32 m0, s46, 12288\n"
  "s_mov_b32 s48, 688128\n"
  "buffer_load_dwordx4 %[wsrc], %[r0], s48 offen lds\n"
  "s_add_u32 m0, s46, 13312\n"
  "s_mov_b32 s48, 745472\n"
  "buffer_load_dwordx4 %[wsrc], %[r0], s48 offen lds\n"
  "s_add_u32 m0, s46, 14336\n"
  "s_mov_b32 s48, 802816\n"
  "buffer_load_dwordx4 %[wsrc], %[r0], s48 offen lds\n"
  "s_add_u32 m0, s46, 15360\n"
  "s_mov_b32 s48, 860160\n"
  "buffer_load_dwordx4 %[wsrc], %[r0], s48 offen lds\n"
  "s_add_u32 m0, s46, 16384\n"
  "s_mov_b32 s48, 917504\n"
  "buffer_load_dwordx4 %[wsrc], %[r0], s48 offen lds\n"
  "s_add_u32 m0, s46, 17408\n"
  "s_mov_b32 s48, 974848\n"
  "buffer_load_dwordx4 %[wsrc], %[r0], s48 offen lds\n"
  "s_add_u32 m0, s46, 18432\n"
  "s_mov_b32 s48, 1032192\n"
  "buffer_load_dwordx4 %[wsrc], %[r0], s48 offen lds\n"
  "s_add_u32 m0, s46, 19456\n"
  "s_mov_b32 s48, 1089536\n"
  "buffer_load_dwordx4 %[wsrc], %[r0], s48 offen lds\n"
  "s_add_u32 m0, s46, 20480\n"
  "s_mov_b32 s48, 1146880\n"
  "buffer_load_dwordx4 %[wsrc], %[r0], s48 offen lds\n"
  "s_add_u32 m0, s46, 21504\n"
  "s_mov_b32 s48, 1204224\n"
  "buffer_load_dwordx4 %[wsrc], %[r0], s48 offen lds\n"
  "s_add_u32 m0, s46, 22528\n"
  "s_mov_b32 s48, 1261568\n"
  "buffer_load_dwordx4 %[wsrc], %[r0], s48 offen lds\n"
  "s_add_u32 m0, s46, 23552\n"
  "s_mov_b32 s48, 1318912\n"
  "buffer_load_dwordx4 %[wsrc], %[r0], s48 offen lds\n"
  "s_add_u32 m0, s46, 24576\n"
  "s_mov_b32 s48, 1376256\n"
  "buffer_load_dwordx4 %[wsrc], %[r0], s48 offen lds\n"
  "s_add_u32 m0, s46, 25600\n"
  "s_mov_b32 s48, 1433600\n"
  "buffer_load_dwordx4 %[wsrc], %[r0], s48 offen lds\n"
  "s_add_u32 m0, s46, 26624\n"
  "s_mov_b32 s48, 1490944\n"
  "buffer_load_dwordx4 %[wsrc], %[r0], s48 offen lds\n"
  "s_add_u32 m0, s46, 27648\n"
  "s_mov_b32 s48, 1548288\n"
  "buffer_load_dwordx4 %[wsrc], %[r0], s48 offen lds\n"
  "s_waitcnt vmcnt(14)\n"
  "ds_read_b64_tr_b16 a[0:1], %[ta] offset:0\n"
  "ds_read_b64_tr_b16 a[2:3], %[ta] offset:256\n"
  "ds_read_b64_tr_b16 a[56:57], %[ta] offset:8\n"
  "ds_read_b64_tr_b16 a[58:59], %[ta] offset:264\n"
  "ds_read_b64_tr_b16 a[4:5], %[ta] offset:2048\n"
  "ds_read_b64_tr_b16 a[6:7], %[ta] offset:2304\n"
  "ds_read_b64_tr_b16 a[60:61], %[ta] offset:2056\n"
  "ds_read_b64_tr_b16 a[62:63], %[ta] offset:2312\n"
  "s_waitcnt lgkmcnt(4)\n"
  "ds_read_b64_tr_b16 a[8:9], %[ta] offset:4096\n"
  "ds_read_b64_tr_b16 a[10:11], %[ta] offset:4352\n"
  "ds_read_b64_tr_b16 a[64:65], %[ta] offset:4104\n"
  "ds_read_b64_tr_b16 a[66:67], %[ta] offset:4360\n"
  "ds_read_b64_tr_b16 a[12:13], %[ta] offset:6144\n"
  "ds_read_b64_tr_b16 a[14:15], %[ta] offset:6400\n"
  "ds_read_b64_tr_b16 a[68:69], %[ta] offset:6152\n"
  "ds_read_b64_tr_b16 a[70:71], %[ta] offset:6408\n"
  "s_waitcnt lgkmcnt(4)\n"
  "ds_read_b64_tr_b16 a[16:17], %[ta] offset:8192\n"
  "ds_read_b64_tr_b16 a[18:19], %[ta] offset:8448\n"
  "ds_read_b64_tr_b16 a[72:73], %[ta] offset:8200\n"
  "ds_read_b64_tr_b16 a[74:75], %[ta] offset:8456\n"
  "ds_read_b64_tr_b16 a[20:21], %[ta] offset:10240\n"
  "ds_read_b64_tr_b16 a[22:23], %[ta] offset:10496\n"
  "ds_read_b64_tr_b16 a[76:77], %[ta] offset:10248\n"
  "ds_read_b64_tr_b16 a[78:79], %[ta] offset:10504\n"
  "s_waitcnt lgkmcnt(4)\n"
  "ds_read_b64_tr_b16 a[24:25], %[ta] offset:12288\n"
  "ds_read_b64_tr_b16 a[26:27], %[ta] offset:12544\n"
  "ds_read_b64_tr_b16 a[80:81], %[ta] offset:12296\n"
  "ds_read_b64_tr_b16 a[82:83], %[ta] offset:12552\n"
  "s_waitcnt lgkmcnt(0)\n"
  "s_add_u32 m0, s46, 0\n"
  "s_mov_b32 s48, 0\n"
  "buffer_load_dwordx4 %[wsrc], %[r1], s48 offen lds\n"
  "s_add_u32 m0, s46, 1024\n"
  "s_mov_b32 s48, 57344\n"
  "buffer_load_dwordx4 %[wsrc], %[r1], s48 offen lds\n"
  "s_add_u32 m0, s46, 2048\n"
  "s_mov_b32 s48, 114688\n"
  "buffer_load_dwordx4 %[wsrc], %[r1], s48 offen lds\n"
  "s_add_u32 m0, s46, 3072\n"
  "s_mov_b32 s48, 172032\n"
  "buffer_load_dwordx4 %[wsrc], %[r1], s48 offen lds\n"
  "s_add_u32 m0, s46, 4096\n"
  "s_mov_b32 s48, 229376\n"
  "buffer_load_dwordx4 %[wsrc], %[r1], s48 offen lds\n"
  "s_add_u32 m0, s46, 5120\n"
  "s_mov_b32 s48, 286720\n"
  "buffer_load_dwordx4 %[wsrc], %[r1], s48 offen lds\n"
  "s_add_u32 m0, s46, 6144\n"
  "s_mov_b32 s48, 344064\n"
  "buffer_load_dwordx4 %[wsrc], %[r1], s48 offen lds\n"
  "s_add_u32 m0, s46, 7168\n"
  "s_mov_b32 s48, 401408\n"
  "buffer_load_dwordx4 %[wsrc], %[r1], s48 offen lds\n"
  "s_add_u32 m0, s46, 8192\n"
  "s_mov_b32 s48, 458752\n"
  "buffer_load_dwordx4 %[wsrc], %[r1], s48 offen lds\n"
  "s_add_u32 m0, s46, 9216\n"
  "s_mov_b32 s48, 516096\n"
  "buffer_load_dwordx4 %[wsrc], %[r1], s48 offen lds\n"
  "s_add_u32 m0, s46, 10240\n"
  "s_mov_b32 s48, 573440\n"
  "buffer_load_dwordx4 %[wsrc], %[r1], s48 offen lds\n"
  "s_add_u32 m0, s46, 11264\n"
  "s_mov_b32 s48, 630784\n"
  "buffer_load_dwordx4 %[wsrc], %[r1], s48 offen lds\n"
  "s_add_u32 m0, s46, 12288\n"
  "s_mov_b32 s48, 688128\n"
  "buffer_load_dwordx4 %[wsrc], %[r1], s48 offen lds\n"
  "s_add_u32 m0, s46, 13312\n"
  "s_mov_b32 s48, 745472\n"
  "buffer_load_dwordx4 %[wsrc], %[r1], s48 offen lds\n"
  "s_waitcnt vmcnt(14)\n"
  "ds_read_b64_tr_b16 a[28:29], %[ta] offset:14336\n"
  "ds_read_b64_tr_b16 a[30:31], %[ta] offset:14592\n"
  "ds_read_b64_tr_b16 a[84:85], %[ta] offset:14344\n"
  "ds_read_b64_tr_b16 a[86:87], %[ta] offset:14600\n"
  "s_waitcnt lgkmcnt(4)\n"
  "ds_read_b64_tr_b16 a[32:33], %[ta] offset:16384\n"
  "ds_read_b64_tr_b16 a[34:35], %[ta] offset:16640\n"
  "ds_read_b64_tr_b16 a[88:89], %[ta] offset:16392\n"
  "ds_read_b64_tr_b16 a[90:91], %[ta] offset:16648\n"
  "ds_read_b64_tr_b16 a[36:37], %[ta] offset:18432\n"
  "ds_read_b64_tr_b16 a[38:39], %[ta] offset:18688\n"
  "ds_read_b64_tr_b16 a[92:93], %[ta] offset:18440\n"
  "ds_read_b64_tr_b16 a[94:95], %[ta] offset:18696\n"
  "s_waitcnt lgkmcnt(4)\n"
  "ds_read_b64_tr_b16 a[40:41], %[ta] offset:20480\n"
  "ds_read_b64_tr_b16 a[42:43], %[ta] offset:20736\n"
  "ds_read_b64_tr_b16 a[96:97], %[ta] offset:20488\n"
  "ds_read_b64_tr_b16 a[98:99], %[ta] offset:20744\n"
  "ds_read_b64_tr_b16 a[44:45], %[ta] offset:22528\n"
  "ds_read_b64_tr_b16 a[46:47], %[ta] offset:22784\n"
  "ds_read_b64_tr_b16 a[100:101], %[ta] offset:22536\n"
  "ds_read_b64_tr_b16 a[102:103], %[ta] offset:22792\n"
  "s_waitcnt lgkmcnt(4)\n"
  "ds_read_b64_tr_b16 a[48:49], %[ta] offset:24576\n"
  "ds_read_b64_tr_b16 a[50:51], %[ta] offset:24832\n"
  "ds_read_b64_tr_b16 a[104:105], %[ta] offset:24584\n"
  "ds_read_b64_tr_b16 a[106:107], %[ta] offset:24840\n"
  "ds_read_b64_tr_b16 a[52:53], %[ta] offset:26624\n"
  "ds_read_b64_tr_b16 a[54:55], %[ta] offset:26880\n"
  "ds_read_b64_tr_b16 a[108:109], %[ta] offset:26632\n"
  "ds_read_b64_tr_b16 a[110:111], %[ta] offset:26888\n"
  "s_waitcnt lgkmcnt(4)\n"
  "s_waitcnt lgkmcnt(0)\n"
  "s_add_u32 m0, s46, 14336\n"
  "s_mov_b32 s48, 802816\n"
  "buffer_load_dwordx4 %[wsrc], %[r1], s48 offen lds\n"
  "s_add_u32 m0, s46, 15360\n"
  "s_mov_b32 s48, 860160\n"
  "buffer_load_dwordx4 %[wsrc], %[r1], s48 offen lds\n"
  "s_add_u32 m0, s46, 16384\n"
  "s_mov_b32 s48, 917504\n"
  "buffer_load_dwordx4 %[wsrc], %[r1], s48 offen lds\n"
  "s_add_u32 m0, s46, 17408\n"
  "s_mov_b32 s48, 974848\n"
  "buffer_load_dwordx4 %[wsrc], %[r1], s48 offen lds\n"
  "s_add_u32 m0, s46, 18432\n"
  "s_mov_b32 s48, 1032192\n"
  "buffer_load_dwordx4 %[wsrc], %[r1], s48 offen lds\n"
  "s_add_u32 m0, s46, 19456\n"
  "s_mov_b32 s48, 1089536\n"
  "buffer_load_dwordx4 %[wsrc], %[r1], s48 offen lds\n"
  "s_add_u32 m0, s46, 20480\n"
  "s_mov_b32 s48, 1146880\n"
  "buffer_load_dwordx4 %[wsrc], %[r1], s48 offen lds\n"
  "s_add_u32 m0, s46, 21504\n"
  "s_mov_b32 s48, 1204224\n"
  "buffer_load_dwordx4 %[wsrc], %[r1], s48 offen lds\n"
  "s_add_u32 m0, s46, 22528\n"
  "s_mov_b32 s48, 1261568\n"
  "buffer_load_dwordx4 %[wsrc], %[r1], s48 offen lds\n"
  "s_add_u32 m0, s46, 23552\n"
  "s_mov_b32 s48, 1318912\n"
  "buffer_load_dwordx4 %[wsrc], %[r1], s48 offen lds\n"
  "s_add_u32 m0, s46, 24576\n"
  "s_mov_b32 s48, 1376256\n"
  "buffer_load_dwordx4 %[wsrc], %[r1], s48 offen lds\n"
  "s_add_u32 m0, s46, 25600\n"
  "s_mov_b32 s48, 1433600\n"
  "buffer_load_dwordx4 %[wsrc], %[r1], s48 offen lds\n"
  "s_add_u32 m0, s46, 26624\n"
  "s_mov_b32 s48, 1490944\n"
  "buffer_load_dwordx4 %[wsrc], %[r1], s48 offen lds\n"
  "s_add_u32 m0, s46, 27648\n"
  "s_mov_b32 s48, 1548288\n"
  "buffer_load_dwordx4 %[wsrc], %[r1], s48 offen lds\n"
  "s_waitcnt vmcnt(14)\n"
  "ds_read_b64_tr_b16 a[112:113], %[ta] offset:0\n"
  "ds_read_b64_tr_b16 a[114:115], %[ta] offset:256\n"
  "ds_read_b64_tr_b16 a[168:169], %[ta] offset:8\n"
  "ds_read_b64_tr_b16 a[170:171], %[ta] offset:264\n"
  "ds_read_b64_tr_b16 a[116:117], %[ta] offset:2048\n"
  "ds_read_b64_tr_b16 a[118:119], %[ta] offset:2304\n"
  "ds_read_b64_tr_b16 a[172:173], %[ta] offset:2056\n"
  "ds_read_b64_tr_b16 a[174:175], %[ta] offset:2312\n"
  "s_waitcnt lgkmcnt(4)\n"
  "ds_read_b64_tr_b16 a[120:121], %[ta] offset:4096\n"
  "ds_read_b64_tr_b16 a[122:123], %[ta] offset:4352\n"
  "ds_read_b64_tr_b16 a[176:177], %[ta] offset:4104\n"
  "ds_read_b64_tr_b16 a[178:179], %[ta] offset:4360\n"
  "ds_read_b64_tr_b16 a[124:125], %[ta] offset:6144\n"
  "ds_read_b64_tr_b16 a[126:127], %[ta] offset:6400\n"
  "ds_read_b64_tr_b16 a[180:181], %[ta] offset:6152\n"
  "ds_read_b64_tr_b16 a[182:183], %[ta] offset:6408\n"
  "s_waitcnt lgkmcnt(4)\n"
  "ds_read_b64_tr_b16 a[128:129], %[ta] offset:8192\n"
  "ds_read_b64_tr_b16 a[130:131], %[ta] offset:8448\n"
  "ds_read_b64_tr_b16 a[184:185], %[ta] offset:8200\n"
  "ds_read_b64_tr_b16 a[186:187], %[ta] offset:8456\n"
  "ds_read_b64_tr_b16 a[132:133], %[ta] offset:10240\n"
  "ds_read_b64_tr_b16 a[134:135], %[ta] offset:10496\n"
  "ds_read_b64_tr_b16 a[188:189], %[ta] offset:10248\n"
  "ds_read_b64_tr_b16 a[190:191], %[ta] offset:10504\n"
  "s_waitcnt lgkmcnt(4)\n"
  "ds_read_b64_tr_b16 a[136:137], %[ta] offset:12288\n"
  "ds_read_b64_tr_b16 a[138:139], %[ta] offset:12544\n"
  "ds_read_b64_tr_b16 a[192:193], %[ta] offset:12296\n"
  "ds_read_b64_tr_b16 a[194:195], %[ta] offset:12552\n"
  "s_waitcnt lgkmcnt(0)\n"
  "s_waitcnt vmcnt(0)\n"
  "ds_read_b64_tr_b16 a[140:141], %[ta] offset:14336\n"
  "ds_read_b64_tr_b16 a[142:143], %[ta] offset:14592\n"
  "ds_read_b64_tr_b16 a[196:197], %[ta] offset:14344\n"
  "ds_read_b64_tr_b16 a[198:199], %[ta] offset:14600\n"
  "s_waitcnt lgkmcnt(4)\n"
  "ds_read_b64_tr_b16 a[144:145], %[ta] offset:16384\n"
  "ds_read_b64_tr_b16 a[146:147], %[ta] offset:16640\n"
  "ds_read_b64_tr_b16 a[200:201], %[ta] offset:16392\n"
  "ds_read_b64_tr_b16 a[202:203], %[ta] offset:16648\n"
  "ds_read_b64_tr_b16 a[148:149], %[ta] offset:18432\n"
  "ds_read_b64_tr_b16 a[150:151], %[ta] offset:18688\n"
  "ds_read_b64_tr_b16 a[204:205], %[ta] offset:18440\n"
  "ds_read_b64_tr_b16 a[206:207], %[ta] offset:18696\n"
  "s_waitcnt lgkmcnt(4)\n"
  "ds_read_b64_tr_b16 a[152:153], %[ta] offset:20480\n"
  "ds_read_b64_tr_b16 a[154:155], %[ta] offset:20736\n"
  "ds_read_b64_tr_b16 a[208:209], %[ta] offset:20488\n"
  "ds_read_b64_tr_b16 a[210:211], %[ta] offset:20744\n"
  "ds_read_b64_tr_b16 a[156:157], %[ta] offset:22528\n"
  "ds_read_b64_tr_b16 a[158:159], %[ta] offset:22784\n"
  "ds_read_b64_tr_b16 a[212:213], %[ta] offset:22536\n"
  "ds_read_b64_tr_b16 a[214:215], %[ta] offset:22792\n"
  "s_waitcnt lgkmcnt(4)\n"
  "ds_read_b64_tr_b16 a[160:161], %[ta] offset:24576\n"
  "ds_read_b64_tr_b16 a[162:163], %[ta] offset:24832\n"
  "ds_read_b64_tr_b16 a[216:217], %[ta] offset:24584\n"
  "ds_read_b64_tr_b16 a[218:219], %[ta] offset:24840\n"
  "ds_read_b64_tr_b16 a[164:165], %[ta] offset:26624\n"
  "ds_read_b64_tr_b16 a[166:167], %[ta] offset:26880\n"
  "ds_read_b64_tr_b16 a[220:221], %[ta] offset:26632\n"
  "ds_read_b64_tr_b16 a[222:223], %[ta] offset:26888\n"
  "s_waitcnt lgkmcnt(4)\n"
  "s_waitcnt lgkmcnt(0)\n"
  "s_barrier\n"
  "s_mov_b32 s41, 0\n"
  "s_mov_b32 s42, 4096\n"
  "s_mov_b32 s46, %[wb]\n"
  "s_mov_b32 m0, s46\n"
  "s_nop 0\n"
  "buffer_load_dwordx4 %[xs], %[ws], s41 offen offset:0 lds\n"
  "buffer_load_dwordx4 %[xs], %[ws], s41 offen offset:1024 lds\n"
  "buffer_load_dwordx4 %[xs], %[ws], s41 offen offset:2048 lds\n"
  "buffer_load_dwordx4 %[xs], %[ws], s41 offen offset:3072 lds\n"
  "s_add_u32 m0, s46, 4096\n"
  "s_nop 0\n"
  "buffer_load_dwordx4 %[xs], %[ws], s42 offen offset:0 lds\n"
  "buffer_load_dwordx4 %[xs], %[ws], s42 offen offset:1024 lds\n"
  "buffer_load_dwordx4 %[xs], %[ws], s42 offen offset:2048 lds\n"
  "s_add_u32 s41, s41, 28672\n"
  "s_add_u32 s42, s42, 28672\n"
  "s_add_u32 s46, s46, 28672\n"
  "s_cmp_ge_u32 s46, 86016\n"
  "s_cselect_b32 s48, 86016, 0\n"
  "s_sub_u32 s46, s46, s48\n"
  "s_mov_b32 s40, 0\n"
  "s_mov_b32 s45, 0x40000000\n"
  "s_mov_b32 s47, 28672\n"
  "v_mov_b32 v72, %[la]\n"
  "v_add_u32 v73, 28672, %[la]\n"
  "s_waitcnt vmcnt(0)\n"
  "s_barrier\n"
  "s_mov_b32 m0, s46\n"
  "s_nop 0\n"
  "buffer_load_dwordx4 %[xs], %[ws], s41 offen offset:0 lds\n"
  "buffer_load_dwordx4 %[xs], %[ws], s41 offen offset:1024 lds\n"
  "buffer_load_dwordx4 %[xs], %[ws], s41 offen offset:2048 lds\n"
  "buffer_load_dwordx4 %[xs], %[ws], s41 offen offset:3072 lds\n"
  "s_add_u32 m0, s46, 4096\n"
  "s_nop 0\n"
  "buffer_load_dwordx4 %[xs], %[ws], s42 offen offset:0 lds\n"
  "buffer_load_dwordx4 %[xs], %[ws], s42 offen offset:1024 lds\n"
  "buffer_load_dwordx4 %[xs], %[ws], s42 offen offset:2048 lds\n"
  "v_mov_b32 v70, 2155520\n"
  "buffer_load_dword v71, v70, %[ws], 0 offen sc1\n"
  "ds_read_b128 v[32:35], v72 offset:0\n"
  "ds_read_b128 v[36:39], v72 offset:1024\n"
  "ds_read_b128 v[40:43], v72 offset:2048\n"
  "s_waitcnt vmcnt(60) lgkmcnt(2)\n"
  ".p2align 6\n"
  "Lp_top_%=:\n"
  "v_mfma_f32_16x16x32_bf16 v[0:3], a[0:3], v[32:35], 0\n"
  "ds_read_b128 v[44:47], v72 offset:3072\n"
  "v_mov_b32 v69, 0\n"
  "v_mfma_f32_16x16x32_bf16 v[4:7], a[56:59], v[32:35], 0\n"
  "v_mov_b32 v69, 0\n"
  "v_mfma_f32_16x16x32_bf16 v[8:11], a[112:115], v[32:35], 0\n"
  "v_mul_f32 v66, s50, v16\n"
  "v_mul_f32 v67, s50, v17\n"
  "v_mfma_f32_16x16x32_bf16 v[12:15], a[168:171], v[32:35], 0\n"
  "s_waitcnt lgkmcnt(2)\n"
  "v_exp_f32 v66, v66\n"
  "v_mfma_f32_16x16x32_bf16 v[0:3], a[4:7], v[36:39], v[0:3]\n"
  "ds_read_b128 v[32:35], v72 offset:4096\n"
  "v_exp_f32 v67, v67\n"
  "v_mfma_f32_16x16x32_bf16 v[4:7], a[60:63], v[36:39], v[4:7]\n"
  "v_mov_b32 v69, 0\n"
  "v_mfma_f32_16x16x32_bf16 v[8:11], a[116:119], v[36:39], v[8:11]\n"
  "v_add_f32 v66, 1.0, v66\n"
  "v_mfma_f32_16x16x32_bf16 v[12:15], a[172:175], v[36:39], v[12:15]\n"
  "s_waitcnt lgkmcnt(2)\n"
  "v_add_f32 v67, 1.0, v67\n"
  "v_mfma_f32_16x16x32_bf16 v[0:3], a[8:11], v[40:43], v[0:3]\n"
  "ds_read_b128 v[36:39], v72 offset:5120\n"
  "v_rcp_f32 v66, v66\n"
  "v_mfma_f32_16x16x32_bf16 v[4:7], a[64:67], v[40:43], v[4:7]\n"
  "v_rcp_f32 v67, v67\n"
  "v_mfma_f32_16x16x32_bf16 v[8:11], a[120:123], v[40:43], v[8:11]\n"
  "v_mul_f32 v64, v16, v66\n"
  "v_mfma_f32_16x16x32_bf16 v[12:15], a[176:179], v[40:43], v[12:15]\n"
  "s_waitcnt lgkmcnt(2)\n"
  "v_mul_f32 v65, v17, v67\n"
  "v_mfma_f32_16x16x32_bf16 v[0:3], a[12:15], v[44:47], v[0:3]\n"
  "ds_read_b128 v[40:43], v72 offset:6144\n"
  "v_mul_f32 v64, v64, v24\n"
  "v_mfma_f32_16x16x32_bf16 v[4:7], a[68:71], v[44:47], v[4:7]\n"
  "v_mul_f32 v65, v65, v25\n"
  "v_cvt_pk_bf16_f32 v52, v64, v65\n"
  "v_mfma_f32_16x16x32_bf16 v[8:11], a[124:127], v[44:47], v[8:11]\n"
  "v_mul_f32 v66, s50, v18\n"
  "v_mfma_f32_16x16x32_bf16 v[12:15], a[180:183], v[44:47], v[12:15]\n"
  "s_waitcnt lgkmcnt(2)\n"
  "v_mul_f32 v67, s50, v19\n"
  "v_mfma_f32_16x16x32_bf16 v[0:3], a[16:19], v[32:35], v[0:3]\n"
  "ds_read_b128 v[44:47], v72 offset:7168\n"
  "v_exp_f32 v66, v66\n"
  "v_mfma_f32_16x16x32_bf16 v[4:7], a[72:75], v[32:35], v[4:7]\n"
  "v_exp_f32 v67, v67\n"
  "v_mfma_f32_16x16x32_bf16 v[8:11], a[128:131], v[32:35], v[8:11]\n"
  "v_add_f32 v66, 1.0, v66\n"
  "v_mfma_f32_16x16x32_bf16 v[12:15], a[184:187], v[32:35], v[12:15]\n"
  "s_waitcnt lgkmcnt(2)\n"
  "v_add_f32 v67, 1.0, v67\n"
  "v_mfma_f32_16x16x32_bf16 v[0:3], a[20:23], v[36:39], v[0:3]\n"
  "ds_read_b128 v[32:35], v72 offset:8192\n"
  "v_rcp_f32 v66, v66\n"
  "v_mfma_f32_16x16x32_bf16 v[4:7], a[76:79], v[36:39], v[4:7]\n"
  "s_add_u32 s41, s41, 28672\n"
  "v_rcp_f32 v67, v67\n"
  "v_mfma_f32_16x16x32_bf16 v[8:11], a[132:135], v[36:39], v[8:11]\n"
  "s_add_u32 s42, s42, 28672\n"
  "v_mov_b32 v69, 0\n"
  "v_mfma_f32_16x16x32_bf16 v[12:15], a[188:191], v[36:39], v[12:15]\n"
  "s_waitcnt lgkmcnt(2)\n"
  "v_mul_f32 v64, v18, v66\n"
  "v_mfma_f32_16x16x32_bf16 v[0:3], a[24:27], v[40:43], v[0:3]\n"
  "ds_read_b128 v[36:39], v72 offset:9216\n"
  "v_mul_f32 v65, v19, v67\n"
  "v_mul_f32 v64, v64, v26\n"
  "v_mfma_f32_16x16x32_bf16 v[4:7], a[80:83], v[40:43], v[4:7]\n"
  "s_add_u32 s46, s46, 28672\n"
  "v_mul_f32 v65, v65, v27\n"
  "v_mfma_f32_16x16x32_bf16 v[8:11], a[136:139], v[40:43], v[8:11]\n"
  "s_cmp_ge_u32 s46, 86016\n"
  "v_cvt_pk_bf16_f32 v53, v64, v65\n"
  "v_mfma_f32_16x16x32_bf16 v[12:15], a[192:195], v[40:43], v[12:15]\n"
  "s_waitcnt lgkmcnt(2)\n"
  "v_mul_f32 v66, s50, v20\n"
  "v_mul_f32 v67, s50, v21\n"
  "v_mfma_f32_16x16x32_bf16 v[0:3], a[28:31], v[44:47], v[0:3]\n"
  "ds_read_b128 v[40:43], v72 offset:10240\n"
  "v_exp_f32 v66, v66\n"
  "v_mfma_f32_16x16x32_bf16 v[4:7], a[84:87], v[44:47], v[4:7]\n"
  "s_cselect_b32 s48, 86016, 0\n"
  "v_exp_f32 v67, v67\n"
  "v_mfma_f32_16x16x32_bf16 v[8:11], a[140:143], v[44:47], v[8:11]\n"
  "s_sub_u32 s46, s46, s48\n"
  "v_mov_b32 v69, 0\n"
  "v_mfma_f32_16x16x32_bf16 v[12:15], a[196:199], v[44:47], v[12:15]\n"
  "s_waitcnt lgkmcnt(2)\n"
  "v_add_f32 v66, 1.0, v66\n"
  "v_mfma_f32_16x16x32_bf16 v[0:3], a[32:35], v[32:35], v[0:3]\n"
  "ds_read_b128 v[44:47], v72 offset:11264\n"
  "v_add_f32 v67, 1.0, v67\n"
  "v_mfma_f32_16x16x32_bf16 v[4:7], a[88:91], v[32:35], v[4:7]\n"
  "s_add_u32 s47, s47, 28672\n"
  "v_rcp_f32 v66, v66\n"
  "v_mfma_f32_16x16x32_bf16 v[8:11], a[144:147], v[32:35], v[8:11]\n"
  "s_cmp_ge_u32 s47, 86016\n"
  "v_rcp_f32 v67, v67\n"
  "v_mfma_f32_16x16x32_bf16 v[12:15], a[200:203], v[32:35], v[12:15]\n"
  "s_waitcnt lgkmcnt(2)\n"
  "v_mul_f32 v64, v20, v66\n"
  "v_mfma_f32_16x16x32_bf16 v[0:3], a[36:39], v[36:39], v[0:3]\n"
  "ds_read_b128 v[32:35], v72 offset:12288\n"
  "v_mul_f32 v65, v21, v67\n"
  "v_mfma_f32_16x16x32_bf16 v[4:7], a[92:95], v[36:39], v[4:7]\n"
  "s_cselect_b32 s47, 0, s47\n"
  "v_mul_f32 v64, v64, v28\n"
  "v_mfma_f32_16x16x32_bf16 v[8:11], a[148:151], v[36:39], v[8:11]\n"
  "s_sub_i32 s48, s40, 1\n"
  "v_mul_f32 v65, v65, v29\n"
  "v_cvt_pk_bf16_f32 v54, v64, v65\n"
  "v_mfma_f32_16x16x32_bf16 v[12:15], a[204:207], v[36:39], v[12:15]\n"
  "s_waitcnt lgkmcnt(2)\n"
  "v_mul_f32 v66, s50, v22\n"
  "v_mfma_f32_16x16x32_bf16 v[0:3], a[40:43], v[40:43], v[0:3]\n"
  "ds_read_b128 v[36:39], v72 offset:13312\n"
  "v_mul_f32 v67, s50, v23\n"
  "v_mfma_f32_16x16x32_bf16 v[4:7], a[96:99], v[40:43], v[4:7]\n"
  "s_max_i32 s48, s48, 0\n"
  "v_exp_f32 v66, v66\n"
  "v_mfma_f32_16x16x32_bf16 v[8:11], a[152:155], v[40:43], v[8:11]\n"
  "v_exp_f32 v67, v67\n"
  "v_mfma_f32_16x16x32_bf16 v[12:15], a[208:211], v[40:43], v[12:15]\n"
  "s_waitcnt lgkmcnt(2)\n"
  "v_add_f32 v66, 1.0, v66\n"
  "v_mfma_f32_16x16x32_bf16 v[0:3], a[44:47], v[44:47], v[0:3]\n"
  "ds_read_b128 v[40:43], v72 offset:14336\n"
  "v_add_f32 v67, 1.0, v67\n"
  "v_mfma_f32_16x16x32_bf16 v[4:7], a[100:103], v[44:47], v[4:7]\n"
  "v_rcp_f32 v66, v66\n"
  "v_mfma_f32_16x16x32_bf16 v[8:11], a[156:159], v[44:47], v[8:11]\n"
  "v_rcp_f32 v67, v67\n"
  "v_mfma_f32_16x16x32_bf16 v[12:15], a[212:215], v[44:47], v[12:15]\n"
  "s_waitcnt lgkmcnt(2)\n"
  "v_mov_b32 v69, 0\n"
  "v_mfma_f32_16x16x32_bf16 v[0:3], a[48:51], v[32:35], v[0:3]\n"
  "ds_read_b128 v[44:47], v72 offset:15360\n"
  "v_mul_f32 v64, v22, v66\n"
  "v_mfma_f32_16x16x32_bf16 v[4:7], a[104:107], v[32:35], v[4:7]\n"
  "v_mul_f32 v65, v23, v67\n"
  "v_mul_f32 v64, v64, v30\n"
  "v_mfma_f32_16x16x32_bf16 v[8:11], a[160:163], v[32:35], v[8:11]\n"
  "v_mul_f32 v65, v65, v31\n"
  "v_mfma_f32_16x16x32_bf16 v[12:15], a[216:219], v[32:35], v[12:15]\n"
  "s_waitcnt lgkmcnt(2)\n"
  "v_cvt_pk_bf16_f32 v55, v64, v65\n"
  "v_mfma_f32_16x16x32_bf16 v[0:3], a[52:55], v[36:39], v[0:3]\n"
  "ds_read_b128 v[32:35], v72 offset:16384\n"
  "v_mov_b32 v69, 0\n"
  "v_mfma_f32_16x16x32_bf16 v[4:7], a[108:111], v[36:39], v[4:7]\n"
  "buffer_store_dwordx4 v[52:55], %[o0], %[ws], s45 offen sc1\n"
  "v_mov_b32 v69, 0\n"
  "v_mfma_f32_16x16x32_bf16 v[8:11], a[164:167], v[36:39], v[8:11]\n"
  "s_cmp_eq_u32 s40, 0\n"
  "v_mov_b32 v74, s48\n"
  "v_mfma_f32_16x16x32_bf16 v[12:15], a[220:223], v[36:39], v[12:15]\n"
  "s_waitcnt lgkmcnt(2)\n"
  "v_mov_b32 v69, 0\n"
  "s_cbranch_scc1 Lp_c_%=\n"
  "Lp_cb_%=:\n"
  "v_mfma_f32_16x16x32_bf16 v[16:19], a[0:3], v[40:43], 0\n"
  "ds_read_b128 v[36:39], v72 offset:17408\n"
  "v_mov_b32 v69, 0\n"
  "v_mfma_f32_16x16x32_bf16 v[20:23], a[56:59], v[40:43], 0\n"
  "s_waitcnt vmcnt(1)\n"
  "s_barrier\n"
  "v_mov_b32 v69, 0\n"
  "v_mfma_f32_16x16x32_bf16 v[24:27], a[112:115], v[40:43], 0\n"
  "buffer_store_dword v74, %[fl], %[ws], 0 offen sc1\n"
  "v_mul_f32 v66, s50, v0\n"
  "v_mul_f32 v67, s50, v1\n"
  "v_mfma_f32_16x16x32_bf16 v[28:31], a[168:171], v[40:43], 0\n"
  "s_waitcnt lgkmcnt(2)\n"
  "v_exp_f32 v66, v66\n"
  "v_mfma_f32_16x16x32_bf16 v[16:19], a[4:7], v[44:47], v[16:19]\n"
  "ds_read_b128 v[40:43], v72 offset:18432\n"
  "s_mov_b32 m0, s46\n"
  "v_exp_f32 v67, v67\n"
  "v_mfma_f32_16x16x32_bf16 v[20:23], a[60:63], v[44:47], v[20:23]\n"
  "buffer_load_dwordx4 %[xs], %[ws], s41 offen offset:0 lds\n"
  "v_mov_b32 v69, 0\n"
  "v_mfma_f32_16x16x32_bf16 v[24:27], a[116:119], v[44:47], v[24:27]\n"
  "v_add_f32 v66, 1.0, v66\n"
  "v_mfma_f32_16x16x32_bf16 v[28:31], a[172:175], v[44:47], v[28:31]\n"
  "s_waitcnt lgkmcnt(2)\n"
  "v_add_f32 v67, 1.0, v67\n"
  "v_mfma_f32_16x16x32_bf16 v[16:19], a[8:11], v[32:35], v[16:19]\n"
  "ds_read_b128 v[44:47], v72 offset:19456\n"
  "v_rcp_f32 v66, v66\n"
  "v_mfma_f32_16x16x32_bf16 v[20:23], a[64:67], v[32:35], v[20:23]\n"
  "buffer_load_dwordx4 %[xs], %[ws], s41 offen offset:1024 lds\n"
  "v_rcp_f32 v67, v67\n"
  "v_mfma_f32_16x16x32_bf16 v[24:27], a[120:123], v[32:35], v[24:27]\n"
  "v_mul_f32 v64, v0, v66\n"
  "v_mfma_f32_16x16x32_bf16 v[28:31], a[176:179], v[32:35], v[28:31]\n"
  "s_waitcnt lgkmcnt(2)\n"
  "v_mul_f32 v65, v1, v67\n"
  "v_mfma_f32_16x16x32_bf16 v[16:19], a[12:15], v[36:39], v[16:19]\n"
  "ds_read_b128 v[32:35], v72 offset:20480\n"
  "v_mul_f32 v64, v64, v8\n"
  "v_mfma_f32_16x16x32_bf16 v[20:23], a[68:71], v[36:39], v[20:23]\n"
  "buffer_load_dwordx4 %[xs], %[ws], s41 offen offset:2048 lds\n"
  "v_mul_f32 v65, v65, v9\n"
  "v_cvt_pk_bf16_f32 v48, v64, v65\n"
  "v_mfma_f32_16x16x32_bf16 v[24:27], a[124:127], v[36:39], v[24:27]\n"
  "v_mul_f32 v66, s50, v2\n"
  "v_mfma_f32_16x16x32_bf16 v[28:31], a[180:183], v[36:39], v[28:31]\n"
  "s_waitcnt lgkmcnt(2)\n"
  "v_mul_f32 v67, s50, v3\n"
  "v_mfma_f32_16x16x32_bf16 v[16:19], a[16:19], v[40:43], v[16:19]\n"
  "ds_read_b128 v[36:39], v72 offset:21504\n"
  "v_exp_f32 v66, v66\n"
  "v_mfma_f32_16x16x32_bf16 v[20:23], a[72:75], v[40:43], v[20:23]\n"
  "buffer_load_dwordx4 %[xs], %[ws], s41 offen offset:3072 lds\n"
  "v_exp_f32 v67, v67\n"
  "v_mfma_f32_16x16x32_bf16 v[24:27], a[128:131], v[40:43], v[24:27]\n"
  "v_add_f32 v66, 1.0, v66\n"
  "v_mfma_f32_16x16x32_bf16 v[28:31], a[184:187], v[40:43], v[28:31]\n"
  "s_waitcnt lgkmcnt(2)\n"
  "v_add_f32 v67, 1.0, v67\n"
  "v_mfma_f32_16x16x32_bf16 v[16:19], a[20:23], v[44:47], v[16:19]\n"
  "ds_read_b128 v[40:43], v72 offset:22528\n"
  "s_add_u32 m0, s46, 4096\n"
  "v_rcp_f32 v66, v66\n"
  "v_mfma_f32_16x16x32_bf16 v[20:23], a[76:79], v[44:47], v[20:23]\n"
  "buffer_load_dwordx4 %[xs], %[ws], s42 offen offset:0 lds\n"
  "v_rcp_f32 v67, v67\n"
  "v_mfma_f32_16x16x32_bf16 v[24:27], a[132:135], v[44:47], v[24:27]\n"
  "v_mov_b32 v69, 0\n"
  "v_mfma_f32_16x16x32_bf16 v[28:31], a[188:191], v[44:47], v[28:31]\n"
  "s_waitcnt lgkmcnt(2)\n"
  "v_mul_f32 v64, v2, v66\n"
  "v_mfma_f32_16x16x32_bf16 v[16:19], a[24:27], v[32:35], v[16:19]\n"
  "ds_read_b128 v[44:47], v72 offset:23552\n"
  "v_mul_f32 v65, v3, v67\n"
  "v_mul_f32 v64, v64, v10\n"
  "v_mfma_f32_16x16x32_bf16 v[20:23], a[80:83], v[32:35], v[20:23]\n"
  "buffer_load_dwordx4 %[xs], %[ws], s42 offen offset:1024 lds\n"
  "v_mul_f32 v65, v65, v11\n"
  "v_mfma_f32_16x16x32_bf16 v[24:27], a[136:139], v[32:35], v[24:27]\n"
  "v_cvt_pk_bf16_f32 v49, v64, v65\n"
  "v_mfma_f32_16x16x32_bf16 v[28:31], a[192:195], v[32:35], v[28:31]\n"
  "s_waitcnt lgkmcnt(2)\n"
  "v_mul_f32 v66, s50, v4\n"
  "v_mul_f32 v67, s50, v5\n"
  "v_mfma_f32_16x16x32_bf16 v[16:19], a[28:31], v[36:39], v[16:19]\n"
  "ds_read_b128 v[32:35], v72 offset:24576\n"
  "v_exp_f32 v66, v66\n"
  "v_mfma_f32_16x16x32_bf16 v[20:23], a[84:87], v[36:39], v[20:23]\n"
  "buffer_load_dwordx4 %[xs], %[ws], s42 offen offset:2048 lds\n"
  "v_exp_f32 v67, v67\n"
  "v_mfma_f32_16x16x32_bf16 v[24:27], a[140:143], v[36:39], v[24:27]\n"
  "v_mov_b32 v69, 0\n"
  "v_mfma_f32_16x16x32_bf16 v[28:31], a[196:199], v[36:39], v[28:31]\n"
  "s_waitcnt lgkmcnt(2)\n"
  "v_add_f32 v66, 1.0, v66\n"
  "v_mfma_f32_16x16x32_bf16 v[16:19], a[32:35], v[40:43], v[16:19]\n"
  "ds_read_b128 v[36:39], v72 offset:25600\n"
  "v_add_f32 v67, 1.0, v67\n"
  "v_mfma_f32_16x16x32_bf16 v[20:23], a[88:91], v[40:43], v[20:23]\n"
  "v_rcp_f32 v66, v66\n"
  "v_mfma_f32_16x16x32_bf16 v[24:27], a[144:147], v[40:43], v[24:27]\n"
  "v_rcp_f32 v67, v67\n"
  "v_mfma_f32_16x16x32_bf16 v[28:31], a[200:203], v[40:43], v[28:31]\n"
  "s_waitcnt lgkmcnt(2)\n"
  "v_mul_f32 v64, v4, v66\n"
  "v_mfma_f32_16x16x32_bf16 v[16:19], a[36:39], v[44:47], v[16:19]\n"
  "ds_read_b128 v[40:43], v72 offset:26624\n"
  "v_mul_f32 v65, v5, v67\n"
  "v_mfma_f32_16x16x32_bf16 v[20:23], a[92:95], v[44:47], v[20:23]\n"
  "v_mul_f32 v64, v64, v12\n"
  "v_mfma_f32_16x16x32_bf16 v[24:27], a[148:151], v[44:47], v[24:27]\n"
  "s_add_u32 s49, s43, 1048576\n"
  "v_mul_f32 v65, v65, v13\n"
  "v_cvt_pk_bf16_f32 v50, v64, v65\n"
  "v_mfma_f32_16x16x32_bf16 v[28:31], a[204:207], v[44:47], v[28:31]\n"
  "s_waitcnt lgkmcnt(2)\n"
  "v_mul_f32 v66, s50, v6\n"
  "v_mfma_f32_16x16x32_bf16 v[16:19], a[40:43], v[32:35], v[16:19]\n"
  "ds_read_b128 v[44:47], v72 offset:27648\n"
  "v_mul_f32 v67, s50, v7\n"
  "v_mfma_f32_16x16x32_bf16 v[20:23], a[96:99], v[32:35], v[20:23]\n"
  "s_lshl_b32 s48, s40, 13\n"
  "v_exp_f32 v66, v66\n"
  "v_mfma_f32_16x16x32_bf16 v[24:27], a[152:155], v[32:35], v[24:27]\n"
  "s_and_b32 s48, s48, 0x2000\n"
  "v_exp_f32 v67, v67\n"
  "v_mfma_f32_16x16x32_bf16 v[28:31], a[208:211], v[32:35], v[28:31]\n"
  "s_waitcnt lgkmcnt(2)\n"
  "v_add_f32 v66, 1.0, v66\n"
  "v_mfma_f32_16x16x32_bf16 v[16:19], a[44:47], v[36:39], v[16:19]\n"
  "ds_read_b128 v[32:35], v73 offset:0\n"
  "v_add_f32 v67, 1.0, v67\n"
  "v_mfma_f32_16x16x32_bf16 v[20:23], a[100:103], v[36:39], v[20:23]\n"
  "s_xor_b32 s49, s49, s48\n"
  "v_rcp_f32 v66, v66\n"
  "v_mfma_f32_16x16x32_bf16 v[24:27], a[156:159], v[36:39], v[24:27]\n"
  "s_add_u32 s45, s43, 114688\n"
  "v_rcp_f32 v67, v67\n"
  "v_mfma_f32_16x16x32_bf16 v[28:31], a[212:215], v[36:39], v[28:31]\n"
  "s_waitcnt lgkmcnt(2)\n"
  "v_mov_b32 v69, 0\n"
  "v_mfma_f32_16x16x32_bf16 v[16:19], a[48:51], v[40:43], v[16:19]\n"
  "ds_read_b128 v[36:39], v73 offset:1024\n"
  "v_mul_f32 v64, v6, v66\n"
  "v_mfma_f32_16x16x32_bf16 v[20:23], a[104:107], v[40:43], v[20:23]\n"
  "s_add_u32 s40, s40, 1\n"
  "v_mul_f32 v65, v7, v67\n"
  "v_mul_f32 v64, v64, v14\n"
  "v_mfma_f32_16x16x32_bf16 v[24:27], a[160:163], v[40:43], v[24:27]\n"
  "s_cmp_lt_u32 s40, 64\n"
  "v_mul_f32 v65, v65, v15\n"
  "v_mfma_f32_16x16x32_bf16 v[28:31], a[216:219], v[40:43], v[28:31]\n"
  "s_waitcnt lgkmcnt(2)\n"
  "v_cvt_pk_bf16_f32 v51, v64, v65\n"
  "v_mfma_f32_16x16x32_bf16 v[16:19], a[52:55], v[44:47], v[16:19]\n"
  "ds_read_b128 v[40:43], v73 offset:2048\n"
  "v_mov_b32 v72, v73\n"
  "v_mfma_f32_16x16x32_bf16 v[20:23], a[108:111], v[44:47], v[20:23]\n"
  "buffer_store_dwordx4 v[48:51], %[o0], %[ws], s43 offen sc1\n"
  "v_add_u32 v73, s47, %[la]\n"
  "v_mfma_f32_16x16x32_bf16 v[24:27], a[164:167], v[44:47], v[24:27]\n"
  "s_mov_b32 s43, s49\n"
  "v_mov_b32 v69, 0\n"
  "v_mfma_f32_16x16x32_bf16 v[28:31], a[220:223], v[44:47], v[28:31]\n"
  "s_waitcnt lgkmcnt(2)\n"
  "v_mov_b32 v69, 0\n"
  "s_cbranch_scc1 Lp_top_%=\n"
  "s_nop 7\n"
  "v_mul_f32 v66, s50, v16\n"
  "v_mul_f32 v67, s50, v17\n"
  "v_exp_f32 v66, v66\n"
  "v_exp_f32 v67, v67\n"
  "v_add_f32 v66, 1.0, v66\n"
  "v_add_f32 v67, 1.0, v67\n"
  "v_rcp_f32 v66, v66\n"
  "v_rcp_f32 v67, v67\n"
  "v_mul_f32 v64, v16, v66\n"
  "v_mul_f32 v65, v17, v67\n"
  "v_mul_f32 v64, v64, v24\n"
  "v_mul_f32 v65, v65, v25\n"
  "v_cvt_pk_bf16_f32 v52, v64, v65\n"
  "v_mul_f32 v66, s50, v18\n"
  "v_mul_f32 v67, s50, v19\n"
  "v_exp_f32 v66, v66\n"
  "v_exp_f32 v67, v67\n"
  "v_add_f32 v66, 1.0, v66\n"
  "v_add_f32 v67, 1.0, v67\n"
  "v_rcp_f32 v66, v66\n"
  "v_rcp_f32 v67, v67\n"
  "v_mul_f32 v64, v18, v66\n"
  "v_mul_f32 v65, v19, v67\n"
  "v_mul_f32 v64, v64, v26\n"
  "v_mul_f32 v65, v65, v27\n"
  "v_cvt_pk_bf16_f32 v53, v64, v65\n"
  "v_mul_f32 v66, s50, v20\n"
  "v_mul_f32 v67, s50, v21\n"
  "v_exp_f32 v66, v66\n"
  "v_exp_f32 v67, v67\n"
  "v_add_f32 v66, 1.0, v66\n"
  "v_add_f32 v67, 1.0, v67\n"
  "v_rcp_f32 v66, v66\n"
  "v_rcp_f32 v67, v67\n"
  "v_mul_f32 v64, v20, v66\n"
  "v_mul_f32 v65, v21, v67\n"
  "v_mul_f32 v64, v64, v28\n"
  "v_mul_f32 v65, v65, v29\n"
  "v_cvt_pk_bf16_f32 v54, v64, v65\n"
  "v_mul_f32 v66, s50, v22\n"
  "v_mul_f32 v67, s50, v23\n"
  "v_exp_f32 v66, v66\n"
  "v_exp_f32 v67, v67\n"
  "v_add_f32 v66, 1.0, v66\n"
  "v_add_f32 v67, 1.0, v67\n"
  "v_rcp_f32 v66, v66\n"
  "v_rcp_f32 v67, v67\n"
  "v_mul_f32 v64, v22, v66\n"
  "v_mul_f32 v65, v23, v67\n"
  "v_mul_f32 v64, v64, v30\n"
  "v_mul_f32 v65, v65, v31\n"
  "v_cvt_pk_bf16_f32 v55, v64, v65\n"
  "buffer_store_dwordx4 v[52:55], %[o0], %[ws], s45 offen sc1\n"
  "v_mov_b32 v74, 64\n"
  "s_waitcnt vmcnt(0) lgkmcnt(0)\n"
  "s_barrier\n"
  "buffer_store_dword v74, %[fl], %[ws], 0 offen sc1\n"
  "s_branch Lp_end_%=\n"
  "Lp_c_%=:\n"
  "s_mov_b32 s48, 100000\n"
  "s_branch Lp_cr_%=\n"
  "Lp_cw_%=:\n"
  "buffer_load_dword v71, v70, %[ws], 0 offen sc1\n"
  "Lp_cr_%=:\n"
  "s_waitcnt vmcnt(0)\n"
  "v_readfirstlane_b32 s44, v71\n"
  "s_nop 3\n"
  "s_cmp_lg_u32 s44, 0\n"
  "s_cbranch_scc1 Lp_cg_%=\n"
  "s_sub_u32 s48, s48, 1\n"
  "s_cbranch_scc0 Lp_cw_%=\n"
  "Lp_cg_%=:\n"
  "s_lshl_b32 s44, s44, 12\n"
  "s_andn2_b32 s43, 0x2000, s44\n"
  "s_branch Lp_cb_%=\n"
  "Lp_end_%=:\n"
    :: [ws] "s"(ws), [wb] "s"(wb), [xs] "v"(xs), [la] "v"(la), [fl] "v"(fl), [r0] "s"(mk(a.wg, D * F * 2)), [r1] "s"(mk(a.wu, D * F * 2)),
       [wsrc] "v"((4 * idx + w) * 64 + (lane >> 2) * (F * 2) + (lane & 3) * 16), [ta] "v"(w * 28672 + (8 * g + (c >> 2)) * 64 + (lane & 3) * 16),
       [o0] "v"(W_HF + ex((4 * idx + w) * 1024) + la)
    : "v0", "v1", "v2", "v3", "v4", "v5", "v6", "v7", "v8", "v9", "v10", "v11", "v12", "v13", "v14", "v15", "v16", "v17", "v18", "v19", "v20", "v21", "v22", "v23", "v24", "v25", "v26", "v27", "v28", "v29", "v30", "v31", "v32", "v33", "v34", "v35", "v36", "v37", "v38", "v39", "v40", "v41", "v42", "v43", "v44", "v45", "v46", "v47", "v48", "v49", "v50", "v51", "v52", "v53", "v54", "v55", "v56", "v57", "v58", "v59", "v60", "v61", "v62", "v63", "v64", "v65", "v66", "v67", "v68", "v69", "v70", "v71", "v72", "v73", "v74", "v75", "a0", "a1", "a2", "a3", "a4", "a5", "a6", "a7", "a8", "a9", "a10", "a11", "a12", "a13", "a14", "a15", "a16", "a17", "a18", "a19", "a20", "a21", "a22", "a23", "a24", "a25", "a26", "a27", "a28", "a29", "a30", "a31", "a32", "a33", "a34", "a35", "a36", "a37", "a38", "a39", "a40", "a41", "a42", "a43", "a44", "a45", "a46", "a47", "a48", "a49", "a50", "a51", "a52", "a53", "a54", "a55", "a56", "a57", "a58", "a59", "a60", "a61", "a62", "a63", "a64", "a65", "a66", "a67", "a68", "a69", "a70", "a71", "a72", "a73", "a74", "a75", "a76", "a77", "a78", "a79", "a80", "a81", "a82", "a83", "a84", "a85", "a86", "a87", "a88", "a89", "a90", "a91", "a92", "a93", "a94", "a95", "a96", "a97", "a98", "a99", "a100", "a101", "a102", "a103", "a104", "a105", "a106", "a107", "a108", "a109", "a110", "a111", "a112", "a113", "a114", "a115", "a116", "a117", "a118", "a119", "a120", "a121", "a122", "a123", "a124", "a125", "a126", "a127", "a128", "a129", "a130", "a131", "a132", "a133", "a134", "a135", "a136", "a137", "a138", "a139", "a140", "a141", "a142", "a143", "a144", "a145", "a146", "a147", "a148", "a149", "a150", "a151", "a152", "a153", "a154", "a155", "a156", "a157", "a158", "a159", "a160", "a161", "a162", "a163", "a164", "a165", "a166", "a167", "a168", "a169", "a170", "a171", "a172", "a173", "a174", "a175", "a176", "a177", "a178", "a179", "a180", "a181", "a182", "a183", "a184", "a185", "a186", "a187", "a188", "a189", "a190", "a191", "a192", "a193", "a194", "a195", "a196", "a197", "a198", "a199", "a200", "a201", "a202", "a203", "a204", "a205", "a206", "a207", "a208", "a209", "a210", "a211", "a212", "a213", "a214", "a215", "a216", "a217", "a218", "a219", "a220", "a221", "a222", "a223", "s40", "s41", "s42", "s43", "s44", "s45", "s46", "s47", "s48", "s49", "s50", "s51", "s52", "s53", "s54", "s55", "scc", "memory");
  }
}

__device__ __forceinline__ void role_f1(rsrc_t ws, int grp, int hq) {
  const int tid = threadIdx.x, lane = tid & 63, w = __builtin_amdgcn_readfirstlane(tid >> 6), g = lane >> 4, c = lane & 15;
  const int h = 4 * hq + w;
  int budget = BUDGET;
  unsigned okm[7];
#pragma unroll
  for (int k = 0; k < 7; ++k) {
    const int c8 = 8 * k + 4 * ((c >> 1) & 1) + g, lc = c8 / 14, tile = (c8 % 14) >> 1;
    okm[k] = W_KQM + h * 16384 + lc * 2048 + (tile >> 2) * 1024 + (((tile & 2) + (c8 & 1)) * 16 + (c & 13) + 2 * (tile & 1)) * 16;
  }
  const unsigned okq = W_KQF + h * 65536 + lane * 16;
  const int kqf = FL_KQ + min((14 * h) / 8 + (lane & 3), (14 * h + 13) / 8);
  const unsigned far = far_half(ws, budget);
  auto stage = [&](int s) {
#pragma unroll
    for (int j = 0; j < 7; ++j) dma(ws, (s >> 2) % 3 * 28672 + (7 * w + j) * 1024, W_XF + s * 28672 + (7 * w + j) * 1024 + lane * 16);
  };
  unsigned seen = 0;
  f32x4 an[2][2] = {};
  float mn[2] = {}, zn[2] = {};
  auto prep = [&](int s) {
    const unsigned xb = (s >> 2) % 3 * 28672;
    if (__builtin_amdgcn_ballot_w64(lane >= 32 && seen <= (unsigned)s)) wait_flags(ws, kqf, s + 1, budget);
    v4u K[14][2];
#pragma unroll
    for (int k = 0; k < 14; ++k) { K[k][0] = ld4c(ws, okq + seg1(s, far) + ex(k * 1024)); K[k][1] = ld4c(ws, okq + seg1(s, far) + ex(k * 1024) + 32768); }
    asm volatile("s_waitcnt vmcnt(28)" ::: "memory");
    bar();
    v4u Y[14][2];
#pragma unroll
    for (int k = 0; k < 14; ++k)
#pragma unroll
      for (int t = 0; t < 2; ++t) Y[k][t] = lds4(xb + (14 * t + k) * 1024 + lane * 16);
    __builtin_amdgcn_sched_barrier(0);
    an[0][0] = an[0][1] = an[1][0] = an[1][1] = (f32x4){};
#pragma unroll
    for (int k = 0; k < 14; ++k)
#pragma unroll
      for (int t = 0; t < 2; ++t) {
        an[t][0] = mma(K[k][0], Y[k][t], an[t][0]);
        if (t) an[t][1] = mma(K[k][1], Y[k][t], an[t][1]);
      }
#pragma unroll
    for (int t = 0; t < 2; ++t) {
      const int lim = 16 * t + c - 4 * g;
      mn[t] = -3.0e38f; zn[t] = 0.f;
#pragma unroll
      for (int e = 0; e < 8; ++e) {
        an[t][e >> 2][e & 3] = 16 * (e >> 2) + (e & 3) <= lim ? an[t][e >> 2][e & 3] : -1.44269504e9f;
        mn[t] = fmaxf(mn[t], an[t][e >> 2][e & 3]);
      }
#pragma unroll
      for (int e = 0; e < 8; ++e) { an[t][e >> 2][e & 3] = __builtin_amdgcn_exp2f(an[t][e >> 2][e & 3] - mn[t]); zn[t] += an[t][e >> 2][e & 3]; }
      asm volatile("" : "+v"(mn[t]), "+v"(zn[t]), "+v"(an[t][0]), "+v"(an[t][1]));
    }
  };
  if (grp >= 1) {
    const v4u ones = {~0u, ~0u, ~0u, ~0u};
#pragma unroll
    for (int t = 0; t < 2; ++t) {
      const unsigned pb = W_PF + seg2(NSEG - 4 + grp, far) + t * ex(24576);
      st4c(ws, pb + ex(h * 1024) + lane * 16, ones);
      st4c(ws, g == 0 ? pb + ex(16384) + h * 256 + c * 16 : OOB, ones);
    }
  }
  stage(grp); stage(grp + 4);
#pragma unroll 1
  for (int s = grp - 4; s < NSEG; s += 4) {
    const f32x4 at[2][2] = {{an[0][0], an[0][1]}, {an[1][0], an[1][1]}};
    const float mx[2] = {mn[0], mn[1]}, zl[2] = {zn[0], zn[1]};
    if (s + 4 < NSEG) prep(s + 4);
    if (s < 0) continue;
    v4u X[14][2];
#pragma unroll
    for (int k = 0; k < 14; ++k)
#pragma unroll
      for (int t = 0; t < 2; ++t) X[k][t] = lds4((s >> 2) % 3 * 28672 + (14 * t + k) * 1024 + lane * 16);
    __builtin_amdgcn_sched_barrier(0);
    bool sure = s + 3 < NSEG;
    seen = wait_flags(ws, lane < 32 ? 4 * h + (lane & 3) + (sure ? FL_CHAIN : s + 1 < NSEG ? FL_PROBE + HINT : FL_CHAIN + HINT) : kqf, lane < 32 ? s + 1 : 0, budget);
    f32x2 sm[2];
    int tries = 0;
    for (;;) {
      v4u KM[7];
#pragma unroll
      for (int k = 0; k < 7; ++k) KM[k] = ld4c(ws, okm[k] + seg_out(s, far));
      __builtin_amdgcn_sched_barrier(0);
      f32x4 am[2][2] = {};
#pragma unroll
      for (int k = 0; k < 14; ++k) {
        if (k == 13) {
          __builtin_amdgcn_sched_barrier(0);
          st1c(ws, tid == 0 && !sure ? flagA(FL_P + 4 * grp + hq + HINT) : OOB, NSEG);
          __builtin_amdgcn_sched_barrier(0);
        }
#pragma unroll
        for (int t = 0; t < 2; ++t) am[t][k & 1] = mma(KM[k >> 1], X[k][t], am[t][k & 1]);
      }
#pragma unroll
      for (int t = 0; t < 2; ++t) sm[t] = (f32x2){am[t][0][0] + am[t][1][2], am[t][0][1] + am[t][1][3]};
      if (sure || __builtin_amdgcn_ballot_w64(__builtin_isunordered(sm[0][0], sm[0][1])) == 0) break;
      if (++tries == 3) { wait_flags(ws, FL_CHAIN + 4 * h + (lane & 3), s + 1, budget); sure = true; }
      asm volatile("" ::: "memory");
    }
#pragma unroll
    for (int t = 0; t < 2; ++t) {
      const float m = over_rows<true>(fmaxf(mx[t], fmaxf(sm[t][0], sm[t][1])));
      const float al = __builtin_amdgcn_exp2f(mx[t] - m), m0 = __builtin_amdgcn_exp2f(sm[t][0] - m), m1 = __builtin_amdgcn_exp2f(sm[t][1] - m);
      const float rz = __builtin_amdgcn_rcpf(over_rows<false>(al * zl[t] + m0 + m1)), ar = al * rz;
      const unsigned pb = W_PF + seg2(s, far) + t * ex(24576);
      st4c(ws, pb + ex(h * 1024) + lane * 16, pk8(at[t][0] * ar, at[t][1] * ar));
      st4c(ws, g == 0 ? pb + ex(16384) + h * 256 + c * 16 : OOB, rows4(pk2(m0 * rz, m1 * rz)));
    }
    if (s + 8 < NSEG) { stage(s + 8); asm volatile("s_waitcnt vmcnt(7)" ::: "memory"); } else drain();
    bar();
    if (tid == 0) st1c(ws, W_FLAG + (FL_P + 4 * grp + hq) * 256, s + 1);
  }
}

__device__ __forceinline__ void load_cols(rsrc_t r, unsigned first, unsigned pitch, int w, int lane, v4u (&t0)[14], v4u (&t1)[14]) {
  const unsigned base = w * 28672, src = first + (lane >> 2) * pitch + (lane & 3) * 16;
#pragma unroll
  for (int i = 0; i < 28; ++i) dma(r, base + i * 1024, src + i * 16 * pitch);
  drain();
  const unsigned a = base + (8 * (lane >> 4) + ((lane & 15) >> 2)) * 64 + (lane & 3) * 8;
#pragma unroll
  for (int k = 0; k < 14; ++k) { t0[k] = ldst(a + k * 2048, 256); t1[k] = ldst(a + k * 2048 + 32, 256); }
  asm volatile("s_waitcnt lgkmcnt(0)" ::: "memory");
#pragma unroll
  for (int k = 0; k < 14; ++k) asm volatile("" : "+a"(t0[k]), "+a"(t1[k]));
}

__device__ __forceinline__ void role_f2(const Args& a, rsrc_t ws, int grp, int cb) {
  const int tid = threadIdx.x, lane = tid & 63, w = __builtin_amdgcn_readfirstlane(tid >> 6), g = lane >> 4, c = lane & 15;
  int budget = BUDGET;
  v4u W[4][14];
  const rsrc_t rd = mk(a.wd, F * D * 2), rx = mk(a.x, S * D * 2), ro = mk(a.out, S * D * 2);
  const unsigned far = far_half(ws, budget);
  load_cols(rd, w * 448 * (D * 2) + cb * 128, D * 2, w, lane, W[0], W[1]);
  load_cols(rd, w * 448 * (D * 2) + cb * 128 + 64, D * 2, w, lane, W[2], W[3]);
  bar();
  constexpr int RP = 68 * 4, RB = 4 * 32 * RP;
  auto put = [&](unsigned base, const f32x4 (&acc)[4][2]) {
#pragma unroll
    for (int t = 0; t < 4; ++t)
#pragma unroll
      for (int tt = 0; tt < 2; ++tt) *(LAS f32x4*)(base + (w * 32 + tt * 16 + c) * RP + t * 64 + g * 16) = acc[t][tt];
  };
  auto get = [&](unsigned base, f32x4& o0, f32x4& o1) {
    o0 = o1 = (f32x4){};
#pragma unroll
    for (int p = 0; p < 4; ++p) {
      const unsigned r = base + (p * 32 + (tid >> 3)) * RP + (tid & 7) * 32;
      o0 += *(LAS const f32x4*)r; o1 += *(LAS const f32x4*)(r + 16);
    }
  };
  unsigned seen = 0;
  const int fp = lane < 16 ? FL_H + (14 * w) / 4 + (lane & 3) : FL_VO + 4 * cb + (lane & 3);
  f32x4 n0 = {}, n1 = {};
#pragma unroll 1
  for (int s = grp - 6; s < NSEG; s += 6) {
    const f32x4 g0 = n0, g1 = n1;
    const unsigned oo = s < 0 ? OOB : (s * 32 + (tid >> 3)) * (D * 2) + cb * 128 + (tid & 7) * 16;
    const unsigned np = lane < 16 ? min(s + 7, NSEG) : max(s + 1, 0);
    if (__builtin_amdgcn_ballot_w64(lane < 32 && seen < np)) wait_flags(ws, fp, np, budget);
    v4u R[4][5];
#pragma unroll
    for (int t = 0; t < 4; ++t)
#pragma unroll
      for (int k = 0; k < 4; ++k) R[t][k] = ld4c(ws, s < 0 ? OOB : W_VOF + seg1(s, far) + ex(((4 * cb + t) * 16 + 4 * w) * 1024) + k * 1024 + lane * 16);
    const v4u xv = ld4(rx, oo);
    if (s + 6 < NSEG) {
      v4u Hh[14][2];
#pragma unroll
      for (int k = 0; k < 14; ++k)
#pragma unroll
        for (int tt = 0; tt < 2; ++tt) Hh[k][tt] = ld4c(ws, W_HF + seg2(s + 6, far) + ex((tt * 56 + 14 * w + k) * 1024) + lane * 16);
      __builtin_amdgcn_sched_barrier(0);
      f32x4 acc[4][2] = {};
#pragma unroll
      for (int k = 0; k < 14; ++k)
#pragma unroll
        for (int t = 0; t < 4; ++t) { acc[t][0] = mma(W[t][k], Hh[k][0], acc[t][0]); acc[t][1] = mma(W[t][k], Hh[k][1], acc[t][1]); }
      put(0, acc);
      bar();
      get(0, n0, n1);
#pragma unroll
      for (int e = 0; e < 4; ++e) { n0[e] = rb(silu(rb(n0[e]))); n1[e] = rb(silu(rb(n1[e]))); }
      asm volatile("" : "+v"(n0), "+v"(n1));
    } else bar();
    if (s < 0) { bar(); continue; }
    bool sure = s + 3 < NSEG;
    const int fs = lane < 48 ? FL_P + 4 * (s & 3) + w : FL_CHAIN + 32 * (w >> 1) + 4 * cb + (lane & 3);
    seen = wait_flags(ws, lane < 32 ? fp : sure || lane >= 48 ? fs : fs + HINT, lane < 32 ? 0 : sure || lane < 48 ? s + 1 : 1, budget);
    f32x4 at[4][2];
    int tries = 0;
    for (;;) {
      v4u P[5][2];
#pragma unroll
      for (int k = 0; k < 5; ++k)
#pragma unroll
        for (int tt = 0; tt < 2; ++tt) P[k][tt] = ld4c(ws, W_PF + seg2(s, far) + ex((tt * 24 + (k < 4 ? 4 * w + k : 16 + w)) * 1024) + lane * 16);
#pragma unroll
      for (int t = 0; t < 4; ++t) R[t][4] = ld4c(ws, W_MBF + seg_out(s, far) + ex(((4 * cb + t) * 4 + w) * 1024) + lane * 16);
      __builtin_amdgcn_sched_barrier(0);
#pragma unroll
      for (int t = 0; t < 4; ++t) at[t][0] = at[t][1] = (f32x4){};
#pragma unroll
      for (int k = 0; k < 5; ++k)
#pragma unroll
        for (int t = 0; t < 4; ++t) { at[t][0] = mma(R[t][k], P[k][0], at[t][0]); at[t][1] = mma(R[t][k], P[k][1], at[t][1]); }
      if (sure) break;
      const f32x4 all = (at[0][0] + at[1][0]) + (at[2][1] + at[3][1]);
      if (__builtin_amdgcn_ballot_w64(__builtin_isunordered(all[0] + all[1], all[2] + all[3])) == 0) break;
      if (++tries == 3) { wait_flags(ws, fs, s + 1, budget); sure = true; }
      asm volatile("" ::: "memory");
    }
    put(RB, at);
    bar();
    f32x4 s0, s1;
    get(RB, s0, s1);
    v4u o;
#pragma unroll
    for (int e = 0; e < 2; ++e) {
      o[e] = pk2(lo16(xv[e]) + rb(rb(s0[2 * e]) * g0[2 * e]), hi16(xv[e]) + rb(rb(s0[2 * e + 1]) * g0[2 * e + 1]));
      o[2 + e] = pk2(lo16(xv[2 + e]) + rb(rb(s1[2 * e]) * g1[2 * e]), hi16(xv[2 + e]) + rb(rb(s1[2 * e + 1]) * g1[2 * e + 1]));
    }
    __builtin_amdgcn_raw_buffer_store_b128(o, ro, oo, 0, 16);
  }
}

__device__ __forceinline__ void page_probe(rsrc_t ws, int n) {
  const int lane = threadIdx.x;
  v4u mine = {~0u, ~0u, ~0u, ~0u};
#pragma unroll
  for (int j = 0; j < 6; ++j) st4c(ws, W_RING + n * 24576 + j * 4096 + lane * 16, mine);
  drain(); bar();
  if (lane >= 64) return;
#pragma unroll 1
  for (int k = 0; k < 5; ++k) {
    const int pair = (k + n) & 3;
    const unsigned long long t0 = __builtin_amdgcn_s_memtime();
    st1c(ws, lane == 0 ? W_RING + (n >> 4) * 32768 + (2 * pair + ((n >> 2) & 1)) * 4096 + (n & 15) * 256 : OOB, ~0u);
    drain();
    const unsigned dt = (unsigned)(__builtin_amdgcn_s_memtime() - t0) | 1u;
#pragma unroll
    for (int j = 0; j < 4; ++j) mine[j] = j == pair ? min(mine[j], dt) : mine[j];
  }
  st4c(ws, lane == 0 ? W_FLAG + (FL_PROBE + n) * 256 : OOB, mine);
  if (n != 63) return;
  v4u v;
  for (int budget = BUDGET; budget > 0; --budget) {
    asm volatile("" ::: "memory");
    v = ld4c(ws, W_FLAG + (FL_PROBE + lane) * 256);
    if (__builtin_amdgcn_ballot_w64(min(min(v.x, v.y), min(v.z, v.w)) == 0) == 0) break;
  }
#pragma unroll
  for (int j = 0; j < 4; ++j) {
    unsigned x = v[j];
    x = min(x, (unsigned)__shfl_xor(x, 1)); x = min(x, (unsigned)__shfl_xor(x, 2)); x = min(x, (unsigned)__shfl_xor(x, 8)); x = min(x, (unsigned)__shfl_xor(x, 16));
    x += (unsigned)__shfl_xor(x, 4); x += (unsigned)__shfl_xor(x, 32);
    v[j] = x;
  }
  const unsigned odd = v.y + v.w < v.x + v.z, lo = odd ? v.y : v.x, hi = odd ? v.w : v.z;
  const unsigned pb = odd + (hi < lo ? 2 : 0), pa = pb ^ 2;
  st1c(ws, lane == 0 ? W_FLAG + FL_CTL * 256 : OOB, 0x11010u + pa * 0x22u + pb * 0x2200u);
  int budget = BUDGET;
  wait_flags(ws, FL_KQ + min(lane, 55), 1, budget);
  const v2u sch = {((unsigned)__builtin_amdgcn_s_memrealtime() + 230) | 1u, 116 | 54 << 8};
  __builtin_amdgcn_raw_buffer_store_b64(sch, ws, lane == 0 ? W_FLAG + FL_SCH * 256 : OOB, 0, 16);
}

__device__ __forceinline__ void role_prefetch(rsrc_t ws, int y, int n) {
  const int lane = threadIdx.x & 63, u = 4 * n + __builtin_amdgcn_readfirstlane(threadIdx.x >> 6);
  if (u >= 14) return;
  int budget = BUDGET;
  const unsigned far = far_half(ws, budget);
#pragma unroll 1
  for (int s = 1; s < NSEG - 1; ++s) {
    wait_flags(ws, FL_VO + 14 * y + u, s + 1, budget);
    v4u v[16];
#pragma unroll
    for (int k = 0; k < 16; ++k) v[k] = ld4c(ws, W_VOF + seg1(s, far) + (14 * y + u) * 32768 + ex(k * 1024) + lane * 16);
#pragma unroll
    for (int k = 0; k < 16; ++k) asm volatile("" :: "v"(v[k]));
  }
}

__global__ void __launch_bounds__(256, 1) k_main(Args a) {
  extern __shared__ __attribute__((aligned(16))) unsigned char smem[];
  const int grp = blockIdx.x & 7, l = blockIdx.x >> 3;
  const rsrc_t ws = mk((const void*)ws_base(a.ws), W_END);
  if (grp >= 4) {
    const int y = grp & 1, np = grp < 6 ? 5 : 9, p0 = grp < 6 ? 5 * y : 10 + 9 * y, r = l - 2 * np;
    if (1 && grp < 6 && r >= 0 && (r < 7 || r >= 14)) {
      const rsrc_t rg = mk(a.wg, D * F * 2), ru = mk(a.wu, D * F * 2);
      const int n = r < 7 ? r : r - 7, pc = threadIdx.x & 127;
#pragma unroll 1
      for (int row = 2 * n + (threadIdx.x >> 7); row < D; row += 30) {
        const unsigned o = pc < 112 ? row * (F * 2) + y * F + pc * 16 : OOB;
        asm volatile("buffer_load_dwordx4 a[252:255], %0, %1, 0 offen\n\tbuffer_load_dwordx4 a[252:255], %0, %2, 0 offen" :: "v"(o), "s"(rg), "s"(ru) : "a252", "a253", "a254", "a255");
      }
      drain();
    }
    if (l < np) producer<0>(a, ws, p0 + l);
    else if (r < 0) producer<1>(a, ws, p0 + l - np);
    else if (grp >= 6 || r < 7) role_f2(a, ws, grp < 6 ? y : 2 + 2 * y + r / 7, r % 7);
    else if (r < 14) producer<2>(a, ws, 7 * y + r - 7);
    else role_f1(ws, 2 * y + ((r - 14) >> 2), (r - 14) & 3);
    return;
  }
  const bool isA = !(grp & 1);
  if (isA) {
    const int q = (grp >> 1) * 32 + l, w = threadIdx.x >> 6;
    const unsigned wa = W_WF + (7 * q + 2 * w) * 14336 + (threadIdx.x & 63) * 16;
    asm volatile(
  "s_mov_b32 s40, 0\n"
  "buffer_load_dwordx4 a[0:3], %[wa0], %[ws], s40 offen\n"
  "s_mov_b32 s40, 0\n"
  "buffer_load_dwordx4 a[112:115], %[wb], %[ws], s40 offen\n"
  "s_mov_b32 s40, 1024\n"
  "buffer_load_dwordx4 a[4:7], %[wa0], %[ws], s40 offen\n"
  "s_mov_b32 s40, 1024\n"
  "buffer_load_dwordx4 a[116:119], %[wb], %[ws], s40 offen\n"
  "s_mov_b32 s40, 2048\n"
  "buffer_load_dwordx4 a[8:11], %[wa0], %[ws], s40 offen\n"
  "s_mov_b32 s40, 2048\n"
  "buffer_load_dwordx4 a[120:123], %[wb], %[ws], s40 offen\n"
  "s_mov_b32 s40, 3072\n"
  "buffer_load_dwordx4 a[12:15], %[wa0], %[ws], s40 offen\n"
  "s_mov_b32 s40, 3072\n"
  "buffer_load_dwordx4 a[124:127], %[wb], %[ws], s40 offen\n"
  "s_mov_b32 s40, 4096\n"
  "buffer_load_dwordx4 a[16:19], %[wa0], %[ws], s40 offen\n"
  "s_mov_b32 s40, 4096\n"
  "buffer_load_dwordx4 a[128:131], %[wb], %[ws], s40 offen\n"
  "s_mov_b32 s40, 5120\n"
  "buffer_load_dwordx4 a[20:23], %[wa0], %[ws], s40 offen\n"
  "s_mov_b32 s40, 5120\n"
  "buffer_load_dwordx4 a[132:135], %[wb], %[ws], s40 offen\n"
  "s_mov_b32 s40, 6144\n"
  "buffer_load_dwordx4 a[24:27], %[wa0], %[ws], s40 offen\n"
  "s_mov_b32 s40, 6144\n"
  "buffer_load_dwordx4 a[136:139], %[wb], %[ws], s40 offen\n"
  "s_mov_b32 s40, 7168\n"
  "buffer_load_dwordx4 a[28:31], %[wa0], %[ws], s40 offen\n"
  "s_mov_b32 s40, 7168\n"
  "buffer_load_dwordx4 a[140:143], %[wb], %[ws], s40 offen\n"
  "s_mov_b32 s40, 8192\n"
  "buffer_load_dwordx4 a[32:35], %[wa0], %[ws], s40 offen\n"
  "s_mov_b32 s40, 8192\n"
  "buffer_load_dwordx4 a[144:147], %[wb], %[ws], s40 offen\n"
  "s_mov_b32 s40, 9216\n"
  "buffer_load_dwordx4 a[36:39], %[wa0], %[ws], s40 offen\n"
  "s_mov_b32 s40, 9216\n"
  "buffer_load_dwordx4 a[148:151], %[wb], %[ws], s40 offen\n"
  "s_mov_b32 s40, 10240\n"
  "buffer_load_dwordx4 a[40:43], %[wa0], %[ws], s40 offen\n"
  "s_mov_b32 s40, 10240\n"
  "buffer_load_dwordx4 a[152:155], %[wb], %[ws], s40 offen\n"
  "s_mov_b32 s40, 11264\n"
  "buffer_load_dwordx4 a[44:47], %[wa0], %[ws], s40 offen\n"
  "s_mov_b32 s40, 11264\n"
  "buffer_load_dwordx4 a[156:159], %[wb], %[ws], s40 offen\n"
  "s_mov_b32 s40, 12288\n"
  "buffer_load_dwordx4 a[48:51], %[wa0], %[ws], s40 offen\n"
  "s_mov_b32 s40, 12288\n"
  "buffer_load_dwordx4 a[160:163], %[wb], %[ws], s40 offen\n"
  "s_mov_b32 s40, 13312\n"
  "buffer_load_dwordx4 a[52:55], %[wa0], %[ws], s40 offen\n"
  "s_mov_b32 s40, 13312\n"
  "buffer_load_dwordx4 a[164:167], %[wb], %[ws], s40 offen\n"
  "s_mov_b32 s40, 14336\n"
  "buffer_load_dwordx4 a[56:59], %[wa1], %[ws], s40 offen\n"
  "s_mov_b32 s40, 401408\n"
  "buffer_load_dwordx4 a[168:171], %[wb], %[ws], s40 offen\n"
  "s_mov_b32 s40, 15360\n"
  "buffer_load_dwordx4 a[60:63], %[wa1], %[ws], s40 offen\n"
  "s_mov_b32 s40, 402432\n"
  "buffer_load_dwordx4 a[172:175], %[wb], %[ws], s40 offen\n"
  "s_mov_b32 s40, 16384\n"
  "buffer_load_dwordx4 a[64:67], %[wa1], %[ws], s40 offen\n"
  "s_mov_b32 s40, 403456\n"
  "buffer_load_dwordx4 a[176:179], %[wb], %[ws], s40 offen\n"
  "s_mov_b32 s40, 17408\n"
  "buffer_load_dwordx4 a[68:71], %[wa1], %[ws], s40 offen\n"
  "s_mov_b32 s40, 404480\n"
  "buffer_load_dwordx4 a[180:183], %[wb], %[ws], s40 offen\n"
  "s_mov_b32 s40, 18432\n"
  "buffer_load_dwordx4 a[72:75], %[wa1], %[ws], s40 offen\n"
  "s_mov_b32 s40, 405504\n"
  "buffer_load_dwordx4 a[184:187], %[wb], %[ws], s40 offen\n"
  "s_mov_b32 s40, 19456\n"
  "buffer_load_dwordx4 a[76:79], %[wa1], %[ws], s40 offen\n"
  "s_mov_b32 s40, 406528\n"
  "buffer_load_dwordx4 a[188:191], %[wb], %[ws], s40 offen\n"
  "s_mov_b32 s40, 20480\n"
  "buffer_load_dwordx4 a[80:83], %[wa1], %[ws], s40 offen\n"
  "s_mov_b32 s40, 407552\n"
  "buffer_load_dwordx4 a[192:195], %[wb], %[ws], s40 offen\n"
  "s_mov_b32 s40, 21504\n"
  "buffer_load_dwordx4 a[84:87], %[wa1], %[ws], s40 offen\n"
  "s_mov_b32 s40, 408576\n"
  "buffer_load_dwordx4 a[196:199], %[wb], %[ws], s40 offen\n"
  "s_mov_b32 s40, 22528\n"
  "buffer_load_dwordx4 a[88:91], %[wa1], %[ws], s40 offen\n"
  "s_mov_b32 s40, 409600\n"
  "buffer_load_dwordx4 a[200:203], %[wb], %[ws], s40 offen\n"
  "s_mov_b32 s40, 23552\n"
  "buffer_load_dwordx4 a[92:95], %[wa1], %[ws], s40 offen\n"
  "s_mov_b32 s40, 410624\n"
  "buffer_load_dwordx4 a[204:207], %[wb], %[ws], s40 offen\n"
  "s_mov_b32 s40, 24576\n"
  "buffer_load_dwordx4 a[96:99], %[wa1], %[ws], s40 offen\n"
  "s_mov_b32 s40, 411648\n"
  "buffer_load_dwordx4 a[208:211], %[wb], %[ws], s40 offen\n"
  "s_mov_b32 s40, 25600\n"
  "buffer_load_dwordx4 a[100:103], %[wa1], %[ws], s40 offen\n"
  "s_mov_b32 s40, 412672\n"
  "buffer_load_dwordx4 a[212:215], %[wb], %[ws], s40 offen\n"
  "s_mov_b32 s40, 26624\n"
  "buffer_load_dwordx4 a[104:107], %[wa1], %[ws], s40 offen\n"
  "s_mov_b32 s40, 413696\n"
  "buffer_load_dwordx4 a[216:219], %[wb], %[ws], s40 offen\n"
  "s_mov_b32 s40, 27648\n"
  "buffer_load_dwordx4 a[108:111], %[wa1], %[ws], s40 offen\n"
  "s_mov_b32 s40, 414720\n"
  "buffer_load_dwordx4 a[220:223], %[wb], %[ws], s40 offen\n"
    :: [ws] "s"(ws), [wa0] "v"(wa), [wa1] "v"(w < 3 ? wa : OOB),
       [wb] "v"(l < 28 ? W_WF + ((16 + 4 * grp + 2 * w) * 28 + l) * 14336 + (threadIdx.x & 63) * 16 : OOB)
    : "a0", "a1", "a2", "a3", "a4", "a5", "a6", "a7", "a8", "a9", "a10", "a11", "a12", "a13", "a14", "a15", "a16", "a17", "a18", "a19", "a20", "a21", "a22", "a23", "a24", "a25", "a26", "a27", "a28", "a29", "a30", "a31", "a32", "a33", "a34", "a35", "a36", "a37", "a38", "a39", "a40", "a41", "a42", "a43", "a44", "a45", "a46", "a47", "a48", "a49", "a50", "a51", "a52", "a53", "a54", "a55", "a56", "a57", "a58", "a59", "a60", "a61", "a62", "a63", "a64", "a65", "a66", "a67", "a68", "a69", "a70", "a71", "a72", "a73", "a74", "a75", "a76", "a77", "a78", "a79", "a80", "a81", "a82", "a83", "a84", "a85", "a86", "a87", "a88", "a89", "a90", "a91", "a92", "a93", "a94", "a95", "a96", "a97", "a98", "a99", "a100", "a101", "a102", "a103", "a104", "a105", "a106", "a107", "a108", "a109", "a110", "a111", "a112", "a113", "a114", "a115", "a116", "a117", "a118", "a119", "a120", "a121", "a122", "a123", "a124", "a125", "a126", "a127", "a128", "a129", "a130", "a131", "a132", "a133", "a134", "a135", "a136", "a137", "a138", "a139", "a140", "a141", "a142", "a143", "a144", "a145", "a146", "a147", "a148", "a149", "a150", "a151", "a152", "a153", "a154", "a155", "a156", "a157", "a158", "a159", "a160", "a161", "a162", "a163", "a164", "a165", "a166", "a167", "a168", "a169", "a170", "a171", "a172", "a173", "a174", "a175", "a176", "a177", "a178", "a179", "a180", "a181", "a182", "a183", "a184", "a185", "a186", "a187", "a188", "a189", "a190", "a191", "a192", "a193", "a194", "a195", "a196", "a197", "a198", "a199", "a200", "a201", "a202", "a203", "a204", "a205", "a206", "a207", "a208", "a209", "a210", "a211", "a212", "a213", "a214", "a215", "a216", "a217", "a218", "a219", "a220", "a221", "a222", "a223", "s40", "memory");
    chain_setup_a((unsigned*)smem, q);
  } else {
    page_probe(ws, (grp >> 1) * 32 + l);
    if (l >= 28) { if (0) role_prefetch(ws, grp >> 1, l - 28); return; }
    chain_setup_b((unsigned*)smem, grp >> 1, l, (const unsigned*)a.mem0);
  }
  const unsigned long long b = ws_base(a.ws);
  asm volatile(
  "s_mov_b32 s40, %[r0]\n"
  "s_mov_b32 s41, %[r1]\n"
  "s_mov_b32 s42, %[r2]\n"
  "s_mov_b32 s43, 0x20000\n"
  "v_mbcnt_lo_u32_b32 v0, -1, 0\n"
  "v_mbcnt_hi_u32_b32 v0, -1, v0\n"
  "s_and_b32 s52, %[wave], 3\n"
  "s_lshl_b32 s52, s52, 8\n"
  "v_lshlrev_b32 v0, 2, v0\n"
  "v_add_u32 v0, s52, v0\n"
  "ds_read_b32 v200, v0 offset:10752\n"
  "ds_read_b32 v201, v0 offset:11776\n"
  "ds_read_b32 v202, v0 offset:12800\n"
  "ds_read_b32 v203, v0 offset:13824\n"
  "ds_read_b32 v204, v0 offset:14848\n"
  "ds_read_b32 v205, v0 offset:15872\n"
  "ds_read_b32 v206, v0 offset:16896\n"
  "ds_read_b32 v207, v0 offset:17920\n"
  "s_waitcnt lgkmcnt(0)\n"
  "ds_read_b32 v208, v0 offset:18944\n"
  "ds_read_b32 v209, v0 offset:19968\n"
  "ds_read_b32 v210, v0 offset:20992\n"
  "ds_read_b32 v211, v0 offset:22016\n"
  "ds_read_b32 v212, v0 offset:23040\n"
  "ds_read_b32 v213, v0 offset:24064\n"
  "ds_read_b32 v214, v0 offset:25088\n"
  "ds_read_b32 v215, v0 offset:26112\n"
  "s_waitcnt lgkmcnt(0)\n"
  "ds_read_b32 v216, v0 offset:27136\n"
  "ds_read_b32 v217, v0 offset:28160\n"
  "ds_read_b32 v218, v0 offset:29184\n"
  "ds_read_b32 v219, v0 offset:30208\n"
  "s_mov_b32 s44, 0\n"
  "s_mov_b32 s45, 0\n"
  "s_mov_b32 s46, 131072\n"
  "s_mov_b32 s47, 262144\n"
  "s_mov_b32 s72, 393216\n"
  "s_mov_b32 s48, 0\n"
  "s_mov_b32 s49, 0\n"
  "s_mov_b32 s50, 0\n"
  "s_mov_b32 s66, 16\n"
  "s_mov_b32 s51, 0x40000000\n"
  "s_mov_b32 s99, 0x40000000\n"
  "s_mov_b32 s74, 0\n"
  "s_mov_b32 s53, 20000\n"
  "s_waitcnt lgkmcnt(0)\n"
  "s_cmp_eq_u32 %[wave], 1\n"
  "s_cbranch_scc1 Lc1_start_%=\n"
  "s_cmp_eq_u32 %[wave], 2\n"
  "s_cbranch_scc1 Lc2_start_%=\n"
  "s_cmp_eq_u32 %[wave], 3\n"
  "s_cbranch_scc1 Lc3_start_%=\n"
  "s_cmp_eq_u32 %[wave], 4\n"
  "s_cbranch_scc1 Lc4_start_%=\n"
  "s_cmp_eq_u32 %[wave], 5\n"
  "s_cbranch_scc1 Lc5_start_%=\n"
  "s_cmp_eq_u32 %[wave], 6\n"
  "s_cbranch_scc1 Lc6_start_%=\n"
  "s_cmp_eq_u32 %[wave], 7\n"
  "s_cbranch_scc1 Lc7_start_%=\n"
  "Lc0_start_%=:\n"
  "v_mov_b32 v105, 0\n"
  "v_add_u32 v104, 32, v210\n"
  "v_mov_b32 v100, -1\n"
  "v_mov_b32 v101, -1\n"
  "v_mov_b32 v102, -1\n"
  "v_mov_b32 v103, -1\n"
  "v_mov_b32 v28, 0\n"
  "v_mov_b32 v31, 0x40000000\n"
  "v_mov_b32 v90, 2155520\n"
  "Lc0_cw_%=:\n"
  "buffer_load_dword v88, v90, s[40:43], 0 offen sc1\n"
  "s_waitcnt vmcnt(0)\n"
  "v_readfirstlane_b32 s73, v88\n"
  "s_nop 3\n"
  "s_cmp_lg_u32 s73, 0\n"
  "s_cbranch_scc1 Lc0_cg_%=\n"
  "s_sub_u32 s53, s53, 1\n"
  "s_cmp_lg_u32 s53, 0\n"
  "s_sleep 2\n"
  "s_cbranch_scc1 Lc0_cw_%=\n"
  "Lc0_cg_%=:\n"
  "s_lshl_b32 s75, s73, 12\n"
  "s_andn2_b32 s75, 0x2000, s75\n"
  "v_bfe_u32 v88, v200, 12, 2\n"
  "v_lshlrev_b32 v88, 2, v88\n"
  "v_lshrrev_b32 v88, v88, s73\n"
  "v_and_b32 v88, 7, v88\n"
  "v_and_b32 v200, 0xffff8fff, v200\n"
  "v_lshl_or_b32 v200, v88, 12, v200\n"
  "v_bfe_u32 v88, v201, 12, 2\n"
  "v_lshlrev_b32 v88, 2, v88\n"
  "v_lshrrev_b32 v88, v88, s73\n"
  "v_and_b32 v88, 7, v88\n"
  "v_and_b32 v201, 0xffff8fff, v201\n"
  "v_lshl_or_b32 v201, v88, 12, v201\n"
  "v_bfe_u32 v88, v206, 12, 2\n"
  "v_lshlrev_b32 v88, 2, v88\n"
  "v_lshrrev_b32 v88, v88, s73\n"
  "v_and_b32 v88, 7, v88\n"
  "v_and_b32 v206, 0xffff8fff, v206\n"
  "v_lshl_or_b32 v206, v88, 12, v206\n"
  "v_bfe_u32 v88, v208, 12, 2\n"
  "v_lshlrev_b32 v88, 2, v88\n"
  "v_lshrrev_b32 v88, v88, s73\n"
  "v_and_b32 v88, 7, v88\n"
  "v_and_b32 v208, 0xffff8fff, v208\n"
  "v_lshl_or_b32 v208, v88, 12, v208\n"
  "v_bfe_u32 v88, v209, 12, 2\n"
  "v_lshlrev_b32 v88, 2, v88\n"
  "v_lshrrev_b32 v88, v88, s73\n"
  "v_and_b32 v88, 7, v88\n"
  "v_and_b32 v209, 0xffff8fff, v209\n"
  "v_lshl_or_b32 v209, v88, 12, v209\n"
  "s_mov_b32 s49, s75\n"
  "s_xor_b32 s98, s75, 66068480\n"
  "s_xor_b32 s52, s75, 65011712\n"
  "s_xor_b32 s73, s75, 63971328\n"
  "buffer_store_dwordx4 v[100:103], v207, s[40:43], s98 offen offset:0 sc1\n"
  "buffer_store_dwordx4 v[100:103], v207, s[40:43], s52 offen offset:0 sc1\n"
  "buffer_store_dwordx4 v[100:103], v207, s[40:43], s73 offen offset:0 sc1\n"
  "ds_write_b32 v28, v100 offset:7424\n"
  "s_waitcnt lgkmcnt(0)\n"
  "s_branch Lc0_top_%=\n"
  "Lc0_s2_%=:\n"
  "s_add_u32 s49, s49, 2097152\n"
  "s_xor_b32 s49, s49, 0x2000\n"
  "s_mov_b32 s50, 1\n"
  "s_mov_b32 s74, 0\n"
  "ds_write_b32 v28, v28 offset:7428\n"
  "s_waitcnt lgkmcnt(0)\n"
  ".p2align 6\n"
  "Lc0_top_%=:\n"
  "v_mov_b32 v44, 0\n"
  "v_mov_b32 v45, 0\n"
  "v_mov_b32 v46, 0\n"
  "v_mov_b32 v47, 0\n"
  "v_mov_b32 v48, 0\n"
  "v_mov_b32 v49, 0\n"
  "v_mov_b32 v50, 0\n"
  "v_mov_b32 v51, 0\n"
  "v_mov_b32 v52, 0\n"
  "v_mov_b32 v53, 0\n"
  "v_mov_b32 v54, 0\n"
  "v_mov_b32 v55, 0\n"
  "v_mov_b32 v56, 0\n"
  "v_mov_b32 v57, 0\n"
  "v_mov_b32 v58, 0\n"
  "v_mov_b32 v59, 0\n"
  "v_mov_b32 v60, 0\n"
  "v_mov_b32 v61, 0\n"
  "v_mov_b32 v62, 0\n"
  "v_mov_b32 v63, 0\n"
  "Lc0_ge_%=:\n"
  "s_barrier\n"
  "buffer_load_dwordx4 v[0:3], v200, s[40:43], s45 offen offset:0 sc1\n"
  "s_nop 7\n"
  "buffer_load_dwordx4 v[4:7], v201, s[40:43], s45 offen offset:0 sc1\n"
  "ds_read_b32 v29, v28 offset:7428\n"
  "s_waitcnt lgkmcnt(0)\n"
  "v_cmp_ne_u32 vcc, 0, v29\n"
  "s_cbranch_vccnz Lc0_ev_%=\n"
  "s_waitcnt vmcnt(1)\n"
  "ds_write_b128 v202, v[0:3] offset:0\n"
  "v_smfmac_f32_16x16x64_bf16 v[44:47], v[0:3], a[0:7], v205\n"
  "v_smfmac_f32_16x16x64_bf16 v[48:51], v[0:3], a[56:63], v205\n"
  "s_waitcnt vmcnt(0)\n"
  "Lc0_evc_%=:\n"
  "ds_write_b128 v202, v[4:7] offset:1024\n"
  "s_waitcnt lgkmcnt(1)\n"
  "s_barrier\n"
  "ds_read_b128 v[8:11], v202 offset:2048\n"
  "ds_read_b128 v[16:19], v202 offset:4096\n"
  "ds_read_b128 v[24:27], v202 offset:6144\n"
  "v_smfmac_f32_16x16x64_bf16 v[44:47], v[4:7], a[8:15], v205\n"
  "v_smfmac_f32_16x16x64_bf16 v[48:51], v[4:7], a[64:71], v205\n"
  "s_waitcnt lgkmcnt(3)\n"
  "s_barrier\n"
  "ds_read_b128 v[12:15], v202 offset:3072\n"
  "ds_read_b128 v[20:23], v202 offset:5120\n"
  "s_waitcnt lgkmcnt(4)\n"
  "v_smfmac_f32_16x16x64_bf16 v[44:47], v[8:11], a[16:23], v205\n"
  "ds_read2st64_b32 v[72:73], v210 offset0:0 offset1:1\n"
  "v_smfmac_f32_16x16x64_bf16 v[48:51], v[8:11], a[72:79], v205\n"
  "ds_read2st64_b32 v[74:75], v104 offset0:0 offset1:1\n"
  "s_waitcnt lgkmcnt(5)\n"
  "v_smfmac_f32_16x16x64_bf16 v[44:47], v[16:19], a[32:39], v205\n"
  "ds_read2st64_b32 v[76:77], v210 offset0:2 offset1:3\n"
  "v_smfmac_f32_16x16x64_bf16 v[48:51], v[16:19], a[88:95], v205\n"
  "ds_read2st64_b32 v[78:79], v104 offset0:2 offset1:3\n"
  "s_waitcnt lgkmcnt(6)\n"
  "v_smfmac_f32_16x16x64_bf16 v[44:47], v[24:27], a[48:55], v205\n"
  "ds_read2st64_b32 v[80:81], v210 offset0:4 offset1:5\n"
  "v_smfmac_f32_16x16x64_bf16 v[48:51], v[24:27], a[104:111], v205\n"
  "ds_read2st64_b32 v[82:83], v104 offset0:4 offset1:5\n"
  "s_waitcnt lgkmcnt(7)\n"
  "v_smfmac_f32_16x16x64_bf16 v[44:47], v[12:15], a[24:31], v205\n"
  "ds_read2st64_b32 v[84:85], v210 offset0:6 offset1:7\n"
  "v_smfmac_f32_16x16x64_bf16 v[48:51], v[12:15], a[80:87], v205\n"
  "ds_read2st64_b32 v[86:87], v104 offset0:6 offset1:7\n"
  "s_waitcnt lgkmcnt(8)\n"
  "v_smfmac_f32_16x16x64_bf16 v[44:47], v[20:23], a[40:47], v205\n"
  "s_mov_b32 s51, 0x40000000\n"
  "v_smfmac_f32_16x16x64_bf16 v[48:51], v[20:23], a[96:103], v205\n"
  "s_nop 3\n"
  "v_add_f32 v88, v44, v46\n"
  "v_add_f32 v89, v45, v47\n"
  "v_cvt_pk_bf16_f32 v92, v88, v89\n"
  "s_nop 0\n"
  "v_pk_add_f32 v[90:91], v[48:49], v[50:51]\n"
  "v_cvt_pk_bf16_f32 v93, v90, v91\n"
  "ds_write_b64 v203, v[92:93]\n"
  "v_smfmac_f32_16x16x64_bf16 v[52:55], v[0:3], a[112:119], v205\n"
  "v_smfmac_f32_16x16x64_bf16 v[52:55], v[4:7], a[120:127], v205\n"
  "s_waitcnt lgkmcnt(0)\n"
  "s_barrier\n"
  "ds_read_b64_tr_b16 v[64:65], v204 offset:0\n"
  "ds_read_b64_tr_b16 v[66:67], v204 offset:160\n"
  "ds_read_b64_tr_b16 v[68:69], v204 offset:1280\n"
  "ds_read_b64_tr_b16 v[70:71], v204 offset:1440\n"
  "v_smfmac_f32_16x16x64_bf16 v[52:55], v[8:11], a[128:135], v205\n"
  "s_waitcnt lgkmcnt(2)\n"
  "v_smfmac_f32_16x16x64_bf16 v[60:63], v[64:67], v[72:79], v205\n"
  "s_waitcnt lgkmcnt(0)\n"
  "v_smfmac_f32_16x16x64_bf16 v[60:63], v[68:71], v[80:87], v205\n"
  "s_nop 7\n"
  "v_pk_add_f32 v[88:89], v[60:61], v[62:63]\n"
  "buffer_store_dwordx2 v[88:89], v208, s[40:43], s45 offen offset:0 sc1\n"
  "v_smfmac_f32_16x16x64_bf16 v[52:55], v[16:19], a[144:151], v205\n"
  "v_smfmac_f32_16x16x64_bf16 v[52:55], v[24:27], a[160:167], v205\n"
  "v_smfmac_f32_16x16x64_bf16 v[52:55], v[12:15], a[136:143], v205\n"
  "v_smfmac_f32_16x16x64_bf16 v[52:55], v[20:23], a[152:159], v205\n"
  "v_smfmac_f32_16x16x64_bf16 v[56:59], v[0:3], a[168:175], v205\n"
  "v_smfmac_f32_16x16x64_bf16 v[56:59], v[4:7], a[176:183], v205\n"
  "v_add_f32 v90, v52, v54\n"
  "v_smfmac_f32_16x16x64_bf16 v[56:59], v[8:11], a[184:191], v205\n"
  "v_add_f32 v91, v53, v55\n"
  "v_smfmac_f32_16x16x64_bf16 v[56:59], v[16:19], a[200:207], v205\n"
  "v_cvt_pk_bf16_f32 v112, v90, v91\n"
  "buffer_store_dword v112, v206, s[40:43], s45 offen offset:0 sc1\n"
  "v_smfmac_f32_16x16x64_bf16 v[56:59], v[24:27], a[216:223], v205\n"
  "v_smfmac_f32_16x16x64_bf16 v[56:59], v[12:15], a[192:199], v205\n"
  "v_smfmac_f32_16x16x64_bf16 v[56:59], v[20:23], a[208:215], v205\n"
  "s_nop 7\n"
  "v_pk_add_f32 v[90:91], v[56:57], v[58:59]\n"
  "v_cvt_pk_bf16_f32 v113, v90, v91\n"
  "buffer_store_dword v113, v206, s[40:43], s45 offen offset:256 sc1\n"
  "s_nop 1\n"
  "v_permlane16_swap_b32 v112, v113\n"
  "v_mov_b32 v114, v112\n"
  "v_mov_b32 v115, v113\n"
  "s_nop 0\n"
  "v_permlane32_swap_b32 v112, v114\n"
  "v_permlane32_swap_b32 v113, v115\n"
  "v_cmp_u_f32 vcc, v90, v91\n"
  "s_cbranch_vccnz Lc0_sm_%=\n"
  "Lc0_smb_%=:\n"
  "s_waitcnt lgkmcnt(0)\n"
  "s_barrier\n"
  "ds_read_b32 v29, v28 offset:7424\n"
  "s_waitcnt lgkmcnt(0)\n"
  "v_cmp_ne_u32 vcc, 0, v29\n"
  "s_cbranch_vccnz Lc0_rt_%=\n"
  "s_lshl_b32 s51, s44, 12\n"
  "s_andn2_b32 s51, 0x2000, s51\n"
  "s_xor_b32 s51, s51, s75\n"
  "s_lshl_b32 s52, s44, 20\n"
  "s_add_u32 s51, s51, s52\n"
  "s_cmp_ge_u32 s44, 63\n"
  "s_cbranch_scc1 Lc0_fin_%=\n"
  "s_nop 14\n"
  "buffer_store_dwordx4 v[112:115], v207, s[40:43], s51 offen offset:0 sc1\n"
  "buffer_store_dword v100, v209, s[40:43], s47 offen offset:0 sc1\n"
  "buffer_store_dword v100, v206, s[40:43], s47 offen offset:0 sc1\n"
  "buffer_store_dword v100, v206, s[40:43], s47 offen offset:256 sc1\n"
  "s_add_u32 s44, s44, 1\n"
  "s_mov_b32 s52, s45\n"
  "s_mov_b32 s45, s46\n"
  "s_mov_b32 s46, s47\n"
  "s_mov_b32 s47, s72\n"
  "s_mov_b32 s72, s52\n"
  "s_add_u32 s49, s49, 2097152\n"
  "s_xor_b32 s49, s49, 0x2000\n"
  "s_branch Lc0_top_%=\n"
  "Lc0_fin_%=:\n"
  "buffer_store_dwordx4 v[112:115], v207, s[40:43], s98 offen offset:0 sc1\n"
  "s_waitcnt vmcnt(0)\n"
  "s_barrier\n"
  "s_branch Lc_end_%=\n"
  "Lc0_ev_%=:\n"
  "buffer_store_dword v100, v209, s[40:43], s45 offen offset:0 sc1\n"
  "buffer_store_dword v100, v206, s[40:43], s45 offen offset:0 sc1\n"
  "buffer_store_dword v100, v206, s[40:43], s45 offen offset:256 sc1\n"
  "s_waitcnt vmcnt(4)\n"
  "ds_write_b128 v202, v[0:3] offset:0\n"
  "v_smfmac_f32_16x16x64_bf16 v[44:47], v[0:3], a[0:7], v205\n"
  "v_smfmac_f32_16x16x64_bf16 v[48:51], v[0:3], a[56:63], v205\n"
  "s_waitcnt vmcnt(3)\n"
  "s_branch Lc0_evc_%=\n"
  "Lc0_sm_%=:\n"
  "s_cmp_eq_u32 s53, 0\n"
  "s_cbranch_scc1 Lc0_smb_%=\n"
  "s_sub_u32 s53, s53, 1\n"
  "ds_write_b32 v28, v100 offset:7424\n"
  "s_branch Lc0_smb_%=\n"
  "Lc0_rt_%=:\n"
  "s_mov_b32 s74, 1\n"
  "s_waitcnt lgkmcnt(0)\n"
  "s_barrier\n"
  "ds_write_b32 v28, v28 offset:7424\n"
  "s_waitcnt lgkmcnt(0)\n"
  "s_nop 7\n"
  "s_cmp_eq_u32 s50, 0\n"
  "s_cbranch_scc1 Lc0_s2_%=\n"
  "s_branch Lc0_top_%=\n"
  "Lc1_start_%=:\n"
  "v_mov_b32 v105, 0\n"
  "v_add_u32 v104, 32, v210\n"
  "v_mov_b32 v100, -1\n"
  "v_mov_b32 v101, -1\n"
  "v_mov_b32 v102, -1\n"
  "v_mov_b32 v103, -1\n"
  "v_mov_b32 v28, 0\n"
  "v_mov_b32 v31, 0x40000000\n"
  "v_mov_b32 v90, 2155520\n"
  "Lc1_cw_%=:\n"
  "buffer_load_dword v88, v90, s[40:43], 0 offen sc1\n"
  "s_waitcnt vmcnt(0)\n"
  "v_readfirstlane_b32 s73, v88\n"
  "s_nop 3\n"
  "s_cmp_lg_u32 s73, 0\n"
  "s_cbranch_scc1 Lc1_cg_%=\n"
  "s_sub_u32 s53, s53, 1\n"
  "s_cmp_lg_u32 s53, 0\n"
  "s_sleep 2\n"
  "s_cbranch_scc1 Lc1_cw_%=\n"
  "Lc1_cg_%=:\n"
  "s_lshl_b32 s75, s73, 12\n"
  "s_andn2_b32 s75, 0x2000, s75\n"
  "v_bfe_u32 v88, v200, 12, 2\n"
  "v_lshlrev_b32 v88, 2, v88\n"
  "v_lshrrev_b32 v88, v88, s73\n"
  "v_and_b32 v88, 7, v88\n"
  "v_and_b32 v200, 0xffff8fff, v200\n"
  "v_lshl_or_b32 v200, v88, 12, v200\n"
  "v_bfe_u32 v88, v201, 12, 2\n"
  "v_lshlrev_b32 v88, 2, v88\n"
  "v_lshrrev_b32 v88, v88, s73\n"
  "v_and_b32 v88, 7, v88\n"
  "v_and_b32 v201, 0xffff8fff, v201\n"
  "v_lshl_or_b32 v201, v88, 12, v201\n"
  "v_bfe_u32 v88, v206, 12, 2\n"
  "v_lshlrev_b32 v88, 2, v88\n"
  "v_lshrrev_b32 v88, v88, s73\n"
  "v_and_b32 v88, 7, v88\n"
  "v_and_b32 v206, 0xffff8fff, v206\n"
  "v_lshl_or_b32 v206, v88, 12, v206\n"
  "s_mov_b32 s49, s75\n"
  "s_xor_b32 s98, s75, 66068480\n"
  "s_xor_b32 s52, s75, 65011712\n"
  "s_xor_b32 s73, s75, 63971328\n"
  "buffer_store_dwordx4 v[100:103], v207, s[40:43], s98 offen offset:0 sc1\n"
  "buffer_store_dwordx4 v[100:103], v207, s[40:43], s52 offen offset:0 sc1\n"
  "buffer_store_dwordx4 v[100:103], v207, s[40:43], s73 offen offset:0 sc1\n"
  "ds_write_b32 v28, v100 offset:7424\n"
  "s_waitcnt lgkmcnt(0)\n"
  "s_branch Lc1_top_%=\n"
  "Lc1_s2_%=:\n"
  "v_mov_b32 v90, 2155776\n"
  "Lc1_sw_%=:\n"
  "buffer_load_dwordx2 v[88:89], v90, s[40:43], 0 offen sc1\n"
  "s_waitcnt vmcnt(0)\n"
  "v_readfirstlane_b32 s71, v88\n"
  "v_readfirstlane_b32 s73, v89\n"
  "s_nop 3\n"
  "s_min_u32 s52, s73, s71\n"
  "s_cmp_lg_u32 s52, 0\n"
  "s_cbranch_scc1 Lc1_sg_%=\n"
  "s_cmp_eq_u32 s53, 0\n"
  "s_cbranch_scc1 Lc1_sf_%=\n"
  "s_sub_u32 s53, s53, 1\n"
  "s_branch Lc1_sw_%=\n"
  "Lc1_sf_%=:\n"
  "s_memrealtime s[76:77]\n"
  "s_waitcnt lgkmcnt(0)\n"
  "s_mov_b32 s71, s76\n"
  "s_mov_b32 s73, 0x4080\n"
  "Lc1_sg_%=:\n"
  "s_and_b32 s66, s73, 0xff\n"
  "s_sub_u32 s71, s71, s66\n"
  "s_mov_b32 s50, 3\n"
  "s_add_u32 s49, s49, 2097152\n"
  "s_xor_b32 s49, s49, 0x2000\n"
  "s_mov_b32 s50, 1\n"
  "s_mov_b32 s74, 0\n"
  "ds_write_b32 v28, v28 offset:7428\n"
  "s_waitcnt lgkmcnt(0)\n"
  ".p2align 6\n"
  "Lc1_top_%=:\n"
  "v_mov_b32 v44, 0\n"
  "v_mov_b32 v45, 0\n"
  "v_mov_b32 v46, 0\n"
  "v_mov_b32 v47, 0\n"
  "v_mov_b32 v48, 0\n"
  "v_mov_b32 v49, 0\n"
  "v_mov_b32 v50, 0\n"
  "v_mov_b32 v51, 0\n"
  "v_mov_b32 v52, 0\n"
  "v_mov_b32 v53, 0\n"
  "v_mov_b32 v54, 0\n"
  "v_mov_b32 v55, 0\n"
  "v_mov_b32 v56, 0\n"
  "v_mov_b32 v57, 0\n"
  "v_mov_b32 v58, 0\n"
  "v_mov_b32 v59, 0\n"
  "s_cmp_lg_u32 s50, 3\n"
  "s_cbranch_scc1 Lc1_gx_%=\n"
  "Lc1_gn_%=:\n"
  "s_add_u32 s71, s71, s66\n"
  "s_memrealtime s[76:77]\n"
  "s_waitcnt lgkmcnt(0)\n"
  "s_sub_i32 s73, s71, s76\n"
  "s_cmp_lt_i32 s73, 4\n"
  "s_cbranch_scc1 Lc1_gs_%=\n"
  "v_mov_b32 v29, s74\n"
  "s_mov_b32 s74, 0\n"
  "ds_write_b32 v28, v29 offset:7428\n"
  "Lc1_gl_%=:\n"
  "s_memrealtime s[76:77]\n"
  "s_waitcnt lgkmcnt(0)\n"
  "s_sub_i32 s73, s71, s76\n"
  "s_cmp_gt_i32 s73, 3\n"
  "s_cbranch_scc1 Lc1_gl_%=\n"
  "s_nop 13\n"
  "s_memrealtime s[78:79]\n"
  "s_nop 0\n"
  "s_memrealtime s[80:81]\n"
  "s_nop 0\n"
  "s_memrealtime s[82:83]\n"
  "s_nop 0\n"
  "s_memrealtime s[84:85]\n"
  "s_nop 0\n"
  "s_memrealtime s[86:87]\n"
  "s_nop 0\n"
  "s_memrealtime s[88:89]\n"
  "s_nop 0\n"
  "s_memrealtime s[90:91]\n"
  "s_nop 0\n"
  "s_memrealtime s[92:93]\n"
  "s_nop 0\n"
  "s_memrealtime s[94:95]\n"
  "s_nop 0\n"
  "s_memrealtime s[96:97]\n"
  "s_nop 0\n"
  "s_sub_i32 s73, s73, 0\n"
  "s_max_i32 s73, s73, 0\n"
  "s_mul_i32 s73, s73, 3\n"
  "s_add_u32 s76, s76, 8\n"
  "s_waitcnt lgkmcnt(0)\n"
  "s_cmp_lg_u32 s78, s76\n"
  "s_addc_u32 s73, s73, 0\n"
  "s_cmp_lg_u32 s80, s76\n"
  "s_addc_u32 s73, s73, 0\n"
  "s_cmp_lg_u32 s82, s76\n"
  "s_addc_u32 s73, s73, 0\n"
  "s_cmp_lg_u32 s84, s76\n"
  "s_addc_u32 s73, s73, 0\n"
  "s_cmp_lg_u32 s86, s76\n"
  "s_addc_u32 s73, s73, 0\n"
  "s_cmp_lg_u32 s88, s76\n"
  "s_addc_u32 s73, s73, 0\n"
  "s_cmp_lg_u32 s90, s76\n"
  "s_addc_u32 s73, s73, 0\n"
  "s_cmp_lg_u32 s92, s76\n"
  "s_addc_u32 s73, s73, 0\n"
  "s_cmp_lg_u32 s94, s76\n"
  "s_addc_u32 s73, s73, 0\n"
  "s_cmp_lg_u32 s96, s76\n"
  "s_addc_u32 s73, s73, 0\n"
  "s_lshl_b32 s73, s73, 2\n"
  "s_sub_u32 s73, 88, s73\n"
  "s_getpc_b64 s[78:79]\n"
  "s_add_u32 s78, s78, s73\n"
  "s_addc_u32 s79, s79, 0\n"
  "s_setpc_b64 s[78:79]\n"
  "s_nop 1\n"
  "s_nop 1\n"
  "s_nop 1\n"
  "s_nop 1\n"
  "s_nop 1\n"
  "s_nop 1\n"
  "s_nop 1\n"
  "s_nop 1\n"
  "s_nop 1\n"
  "s_nop 1\n"
  "s_nop 1\n"
  "s_nop 1\n"
  "s_nop 1\n"
  "s_nop 1\n"
  "s_nop 1\n"
  "s_nop 1\n"
  "s_nop 1\n"
  "s_nop 1\n"
  "s_nop 1\n"
  "Lc1_ge_%=:\n"
  "s_barrier\n"
  "buffer_load_dwordx4 v[8:11], v200, s[40:43], s45 offen offset:0 sc1\n"
  "s_nop 7\n"
  "buffer_load_dwordx4 v[12:15], v201, s[40:43], s45 offen offset:0 sc1\n"
  "ds_read_b32 v29, v28 offset:7428\n"
  "s_waitcnt lgkmcnt(0)\n"
  "v_cmp_ne_u32 vcc, 0, v29\n"
  "s_cbranch_vccnz Lc1_ev_%=\n"
  "s_waitcnt vmcnt(1)\n"
  "ds_write_b128 v202, v[8:11] offset:2048\n"
  "v_smfmac_f32_16x16x64_bf16 v[44:47], v[8:11], a[16:23], v205\n"
  "v_smfmac_f32_16x16x64_bf16 v[48:51], v[8:11], a[72:79], v205\n"
  "s_waitcnt vmcnt(0)\n"
  "Lc1_evc_%=:\n"
  "ds_write_b128 v202, v[12:15] offset:3072\n"
  "s_waitcnt lgkmcnt(1)\n"
  "s_barrier\n"
  "ds_read_b128 v[16:19], v202 offset:4096\n"
  "ds_read_b128 v[24:27], v202 offset:6144\n"
  "ds_read_b128 v[0:3], v202 offset:0\n"
  "v_smfmac_f32_16x16x64_bf16 v[44:47], v[12:15], a[24:31], v205\n"
  "v_smfmac_f32_16x16x64_bf16 v[48:51], v[12:15], a[80:87], v205\n"
  "s_waitcnt lgkmcnt(3)\n"
  "s_barrier\n"
  "ds_read_b128 v[20:23], v202 offset:5120\n"
  "ds_read_b128 v[4:7], v202 offset:1024\n"
  "s_waitcnt lgkmcnt(4)\n"
  "v_smfmac_f32_16x16x64_bf16 v[44:47], v[16:19], a[32:39], v205\n"
  "v_smfmac_f32_16x16x64_bf16 v[48:51], v[16:19], a[88:95], v205\n"
  "s_waitcnt lgkmcnt(3)\n"
  "v_smfmac_f32_16x16x64_bf16 v[44:47], v[24:27], a[48:55], v205\n"
  "v_smfmac_f32_16x16x64_bf16 v[48:51], v[24:27], a[104:111], v205\n"
  "s_waitcnt lgkmcnt(2)\n"
  "v_smfmac_f32_16x16x64_bf16 v[44:47], v[0:3], a[0:7], v205\n"
  "v_smfmac_f32_16x16x64_bf16 v[48:51], v[0:3], a[56:63], v205\n"
  "s_waitcnt lgkmcnt(1)\n"
  "v_smfmac_f32_16x16x64_bf16 v[44:47], v[20:23], a[40:47], v205\n"
  "v_smfmac_f32_16x16x64_bf16 v[48:51], v[20:23], a[96:103], v205\n"
  "s_waitcnt lgkmcnt(0)\n"
  "v_smfmac_f32_16x16x64_bf16 v[44:47], v[4:7], a[8:15], v205\n"
  "s_mov_b32 s51, 0x40000000\n"
  "v_smfmac_f32_16x16x64_bf16 v[48:51], v[4:7], a[64:71], v205\n"
  "s_nop 3\n"
  "v_add_f32 v88, v44, v46\n"
  "v_add_f32 v89, v45, v47\n"
  "v_cvt_pk_bf16_f32 v92, v88, v89\n"
  "s_nop 0\n"
  "v_pk_add_f32 v[90:91], v[48:49], v[50:51]\n"
  "v_cvt_pk_bf16_f32 v93, v90, v91\n"
  "ds_write_b64 v203, v[92:93]\n"
  "v_smfmac_f32_16x16x64_bf16 v[52:55], v[8:11], a[128:135], v205\n"
  "v_smfmac_f32_16x16x64_bf16 v[52:55], v[12:15], a[136:143], v205\n"
  "s_waitcnt lgkmcnt(0)\n"
  "s_barrier\n"
  "v_smfmac_f32_16x16x64_bf16 v[52:55], v[16:19], a[144:151], v205\n"
  "v_smfmac_f32_16x16x64_bf16 v[52:55], v[24:27], a[160:167], v205\n"
  "v_smfmac_f32_16x16x64_bf16 v[52:55], v[0:3], a[112:119], v205\n"
  "v_smfmac_f32_16x16x64_bf16 v[52:55], v[20:23], a[152:159], v205\n"
  "v_smfmac_f32_16x16x64_bf16 v[52:55], v[4:7], a[120:127], v205\n"
  "v_smfmac_f32_16x16x64_bf16 v[56:59], v[8:11], a[184:191], v205\n"
  "v_smfmac_f32_16x16x64_bf16 v[56:59], v[12:15], a[192:199], v205\n"
  "v_smfmac_f32_16x16x64_bf16 v[56:59], v[16:19], a[200:207], v205\n"
  "v_add_f32 v90, v52, v54\n"
  "v_smfmac_f32_16x16x64_bf16 v[56:59], v[24:27], a[216:223], v205\n"
  "v_add_f32 v91, v53, v55\n"
  "v_smfmac_f32_16x16x64_bf16 v[56:59], v[0:3], a[168:175], v205\n"
  "v_cvt_pk_bf16_f32 v112, v90, v91\n"
  "buffer_store_dword v112, v206, s[40:43], s45 offen offset:0 sc1\n"
  "v_smfmac_f32_16x16x64_bf16 v[56:59], v[20:23], a[208:215], v205\n"
  "v_smfmac_f32_16x16x64_bf16 v[56:59], v[4:7], a[176:183], v205\n"
  "s_nop 7\n"
  "v_pk_add_f32 v[90:91], v[56:57], v[58:59]\n"
  "v_cvt_pk_bf16_f32 v113, v90, v91\n"
  "buffer_store_dword v113, v206, s[40:43], s45 offen offset:256 sc1\n"
  "s_nop 1\n"
  "v_permlane16_swap_b32 v112, v113\n"
  "v_mov_b32 v114, v112\n"
  "v_mov_b32 v115, v113\n"
  "s_nop 0\n"
  "v_permlane32_swap_b32 v112, v114\n"
  "v_permlane32_swap_b32 v113, v115\n"
  "v_cmp_u_f32 vcc, v90, v91\n"
  "s_cbranch_vccnz Lc1_sm_%=\n"
  "Lc1_smb_%=:\n"
  "s_waitcnt lgkmcnt(0)\n"
  "s_barrier\n"
  "ds_read_b32 v29, v28 offset:7424\n"
  "s_waitcnt lgkmcnt(0)\n"
  "v_cmp_ne_u32 vcc, 0, v29\n"
  "s_cbranch_vccnz Lc1_rt_%=\n"
  "s_lshl_b32 s51, s44, 12\n"
  "s_andn2_b32 s51, 0x2000, s51\n"
  "s_xor_b32 s51, s51, s75\n"
  "s_lshl_b32 s52, s44, 20\n"
  "s_add_u32 s51, s51, s52\n"
  "s_cmp_ge_u32 s44, 63\n"
  "s_cbranch_scc1 Lc1_fin_%=\n"
  "s_nop 14\n"
  "buffer_store_dwordx4 v[112:115], v207, s[40:43], s51 offen offset:0 sc1\n"
  "buffer_store_dword v100, v206, s[40:43], s47 offen offset:0 sc1\n"
  "buffer_store_dword v100, v206, s[40:43], s47 offen offset:256 sc1\n"
  "s_add_u32 s44, s44, 1\n"
  "s_mov_b32 s52, s45\n"
  "s_mov_b32 s45, s46\n"
  "s_mov_b32 s46, s47\n"
  "s_mov_b32 s47, s72\n"
  "s_mov_b32 s72, s52\n"
  "s_add_u32 s49, s49, 2097152\n"
  "s_xor_b32 s49, s49, 0x2000\n"
  "s_branch Lc1_top_%=\n"
  "Lc1_fin_%=:\n"
  "buffer_store_dwordx4 v[112:115], v207, s[40:43], s98 offen offset:0 sc1\n"
  "s_waitcnt vmcnt(0)\n"
  "s_barrier\n"
  "s_branch Lc_end_%=\n"
  "Lc1_gx_%=:\n"
  "s_mul_i32 s50, s50, 3\n"
  "s_cmp_lg_u32 s50, 0\n"
  "s_cbranch_scc1 Lc1_ge_%=\n"
  "s_memrealtime s[76:77]\n"
  "s_waitcnt lgkmcnt(0)\n"
  "s_mov_b32 s71, s76\n"
  "s_branch Lc1_gn_%=\n"
  "Lc1_gs_%=:\n"
  "s_mov_b32 s74, 1\n"
  "s_branch Lc1_gn_%=\n"
  "Lc1_ev_%=:\n"
  "buffer_store_dword v100, v206, s[40:43], s45 offen offset:0 sc1\n"
  "buffer_store_dword v100, v206, s[40:43], s45 offen offset:256 sc1\n"
  "s_waitcnt vmcnt(3)\n"
  "ds_write_b128 v202, v[8:11] offset:2048\n"
  "v_smfmac_f32_16x16x64_bf16 v[44:47], v[8:11], a[16:23], v205\n"
  "v_smfmac_f32_16x16x64_bf16 v[48:51], v[8:11], a[72:79], v205\n"
  "s_waitcnt vmcnt(2)\n"
  "s_branch Lc1_evc_%=\n"
  "Lc1_sm_%=:\n"
  "s_cmp_eq_u32 s53, 0\n"
  "s_cbranch_scc1 Lc1_smb_%=\n"
  "s_sub_u32 s53, s53, 1\n"
  "ds_write_b32 v28, v100 offset:7424\n"
  "s_branch Lc1_smb_%=\n"
  "Lc1_rt_%=:\n"
  "s_mov_b32 s74, 1\n"
  "s_waitcnt lgkmcnt(0)\n"
  "s_barrier\n"
  "ds_write_b32 v28, v28 offset:7424\n"
  "s_waitcnt lgkmcnt(0)\n"
  "s_nop 7\n"
  "s_cmp_eq_u32 s50, 0\n"
  "s_cbranch_scc1 Lc1_s2_%=\n"
  "s_branch Lc1_top_%=\n"
  "Lc2_start_%=:\n"
  "v_mov_b32 v105, 0\n"
  "v_add_u32 v104, 32, v210\n"
  "v_mov_b32 v100, -1\n"
  "v_mov_b32 v101, -1\n"
  "v_mov_b32 v102, -1\n"
  "v_mov_b32 v103, -1\n"
  "v_mov_b32 v28, 0\n"
  "v_mov_b32 v31, 0x40000000\n"
  "v_mov_b32 v90, 2155520\n"
  "Lc2_cw_%=:\n"
  "buffer_load_dword v88, v90, s[40:43], 0 offen sc1\n"
  "s_waitcnt vmcnt(0)\n"
  "v_readfirstlane_b32 s73, v88\n"
  "s_nop 3\n"
  "s_cmp_lg_u32 s73, 0\n"
  "s_cbranch_scc1 Lc2_cg_%=\n"
  "s_sub_u32 s53, s53, 1\n"
  "s_cmp_lg_u32 s53, 0\n"
  "s_sleep 2\n"
  "s_cbranch_scc1 Lc2_cw_%=\n"
  "Lc2_cg_%=:\n"
  "s_lshl_b32 s75, s73, 12\n"
  "s_andn2_b32 s75, 0x2000, s75\n"
  "v_bfe_u32 v88, v200, 12, 2\n"
  "v_lshlrev_b32 v88, 2, v88\n"
  "v_lshrrev_b32 v88, v88, s73\n"
  "v_and_b32 v88, 7, v88\n"
  "v_and_b32 v200, 0xffff8fff, v200\n"
  "v_lshl_or_b32 v200, v88, 12, v200\n"
  "v_bfe_u32 v88, v201, 12, 2\n"
  "v_lshlrev_b32 v88, 2, v88\n"
  "v_lshrrev_b32 v88, v88, s73\n"
  "v_and_b32 v88, 7, v88\n"
  "v_and_b32 v201, 0xffff8fff, v201\n"
  "v_lshl_or_b32 v201, v88, 12, v201\n"
  "v_bfe_u32 v88, v206, 12, 2\n"
  "v_lshlrev_b32 v88, 2, v88\n"
  "v_lshrrev_b32 v88, v88, s73\n"
  "v_and_b32 v88, 7, v88\n"
  "v_and_b32 v206, 0xffff8fff, v206\n"
  "v_lshl_or_b32 v206, v88, 12, v206\n"
  "s_mov_b32 s49, s75\n"
  "s_xor_b32 s98, s75, 66068480\n"
  "s_xor_b32 s52, s75, 65011712\n"
  "s_xor_b32 s73, s75, 63971328\n"
  "buffer_store_dwordx4 v[100:103], v207, s[40:43], s98 offen offset:0 sc1\n"
  "buffer_store_dwordx4 v[100:103], v208, s[40:43], s98 offen offset:0 sc1\n"
  "buffer_store_dwordx4 v[100:103], v208, s[40:43], s98 offen offset:1024 sc1\n"
  "buffer_store_dwordx4 v[100:103], v207, s[40:43], s52 offen offset:0 sc1\n"
  "buffer_store_dwordx4 v[100:103], v208, s[40:43], s52 offen offset:0 sc1\n"
  "buffer_store_dwordx4 v[100:103], v208, s[40:43], s52 offen offset:1024 sc1\n"
  "buffer_store_dwordx4 v[100:103], v207, s[40:43], s73 offen offset:0 sc1\n"
  "buffer_store_dwordx4 v[100:103], v208, s[40:43], s73 offen offset:0 sc1\n"
  "buffer_store_dwordx4 v[100:103], v208, s[40:43], s73 offen offset:1024 sc1\n"
  "ds_write_b32 v28, v100 offset:7424\n"
  "s_waitcnt lgkmcnt(0)\n"
  "s_branch Lc2_top_%=\n"
  "Lc2_s2_%=:\n"
  "s_add_u32 s49, s49, 2097152\n"
  "s_xor_b32 s49, s49, 0x2000\n"
  "s_mov_b32 s50, 1\n"
  "s_mov_b32 s74, 0\n"
  "ds_write_b32 v28, v28 offset:7428\n"
  "s_waitcnt lgkmcnt(0)\n"
  ".p2align 6\n"
  "Lc2_top_%=:\n"
  "v_mov_b32 v44, 0\n"
  "v_mov_b32 v45, 0\n"
  "v_mov_b32 v46, 0\n"
  "v_mov_b32 v47, 0\n"
  "v_mov_b32 v48, 0\n"
  "v_mov_b32 v49, 0\n"
  "v_mov_b32 v50, 0\n"
  "v_mov_b32 v51, 0\n"
  "v_mov_b32 v52, 0\n"
  "v_mov_b32 v53, 0\n"
  "v_mov_b32 v54, 0\n"
  "v_mov_b32 v55, 0\n"
  "v_mov_b32 v56, 0\n"
  "v_mov_b32 v57, 0\n"
  "v_mov_b32 v58, 0\n"
  "v_mov_b32 v59, 0\n"
  "s_sub_i32 s52, s44, 0\n"
  "s_max_i32 s52, s52, 0\n"
  "v_mov_b32 v106, s52\n"
  "Lc2_ge_%=:\n"
  "s_barrier\n"
  "buffer_load_dwordx4 v[16:19], v200, s[40:43], s45 offen offset:0 sc1\n"
  "s_nop 7\n"
  "buffer_load_dwordx4 v[20:23], v201, s[40:43], s45 offen offset:0 sc1\n"
  "ds_read_b32 v29, v28 offset:7428\n"
  "s_waitcnt lgkmcnt(0)\n"
  "v_cmp_ne_u32 vcc, 0, v29\n"
  "s_cbranch_vccnz Lc2_ev_%=\n"
  "s_cmp_ge_u32 s44, 63\n"
  "s_cselect_b32 s52, 0, 0x40000000\n"
  "buffer_store_dword v100, v210, s[40:43], s52 offen offset:128 sc1\n"
  "s_waitcnt vmcnt(2)\n"
  "ds_write_b128 v202, v[16:19] offset:4096\n"
  "v_smfmac_f32_16x16x64_bf16 v[44:47], v[16:19], a[32:39], v205\n"
  "v_smfmac_f32_16x16x64_bf16 v[48:51], v[16:19], a[88:95], v205\n"
  "s_waitcnt vmcnt(1)\n"
  "Lc2_evc_%=:\n"
  "ds_write_b128 v202, v[20:23] offset:5120\n"
  "s_waitcnt lgkmcnt(1)\n"
  "s_barrier\n"
  "ds_read_b128 v[24:27], v202 offset:6144\n"
  "ds_read_b128 v[0:3], v202 offset:0\n"
  "ds_read_b128 v[8:11], v202 offset:2048\n"
  "v_smfmac_f32_16x16x64_bf16 v[44:47], v[20:23], a[40:47], v205\n"
  "v_smfmac_f32_16x16x64_bf16 v[48:51], v[20:23], a[96:103], v205\n"
  "s_waitcnt lgkmcnt(3)\n"
  "s_barrier\n"
  "ds_read_b128 v[4:7], v202 offset:1024\n"
  "ds_read_b128 v[12:15], v202 offset:3072\n"
  "s_waitcnt lgkmcnt(4)\n"
  "v_smfmac_f32_16x16x64_bf16 v[44:47], v[24:27], a[48:55], v205\n"
  "buffer_store_dword v106, v210, s[40:43], 0 offen offset:0 sc1\n"
  "v_smfmac_f32_16x16x64_bf16 v[48:51], v[24:27], a[104:111], v205\n"
  "s_waitcnt lgkmcnt(3)\n"
  "v_smfmac_f32_16x16x64_bf16 v[44:47], v[0:3], a[0:7], v205\n"
  "v_smfmac_f32_16x16x64_bf16 v[48:51], v[0:3], a[56:63], v205\n"
  "s_waitcnt lgkmcnt(2)\n"
  "v_smfmac_f32_16x16x64_bf16 v[44:47], v[8:11], a[16:23], v205\n"
  "v_smfmac_f32_16x16x64_bf16 v[48:51], v[8:11], a[72:79], v205\n"
  "s_waitcnt lgkmcnt(1)\n"
  "v_smfmac_f32_16x16x64_bf16 v[44:47], v[4:7], a[8:15], v205\n"
  "v_smfmac_f32_16x16x64_bf16 v[48:51], v[4:7], a[64:71], v205\n"
  "s_waitcnt lgkmcnt(0)\n"
  "v_smfmac_f32_16x16x64_bf16 v[44:47], v[12:15], a[24:31], v205\n"
  "s_mov_b32 s51, 0x40000000\n"
  "v_smfmac_f32_16x16x64_bf16 v[48:51], v[12:15], a[80:87], v205\n"
  "s_nop 3\n"
  "v_add_f32 v88, v44, v46\n"
  "v_add_f32 v89, v45, v47\n"
  "v_cvt_pk_bf16_f32 v92, v88, v89\n"
  "s_nop 0\n"
  "v_pk_add_f32 v[90:91], v[48:49], v[50:51]\n"
  "v_cvt_pk_bf16_f32 v93, v90, v91\n"
  "ds_write_b64 v203, v[92:93]\n"
  "v_smfmac_f32_16x16x64_bf16 v[52:55], v[16:19], a[144:151], v205\n"
  "v_smfmac_f32_16x16x64_bf16 v[52:55], v[20:23], a[152:159], v205\n"
  "s_waitcnt lgkmcnt(0)\n"
  "s_barrier\n"
  "ds_read_b64_tr_b16 v[64:65], v204 offset:0\n"
  "ds_read_b64_tr_b16 v[66:67], v204 offset:160\n"
  "ds_read_b64_tr_b16 v[68:69], v204 offset:1280\n"
  "ds_read_b64_tr_b16 v[70:71], v204 offset:1440\n"
  "v_smfmac_f32_16x16x64_bf16 v[52:55], v[24:27], a[160:167], v205\n"
  "v_smfmac_f32_16x16x64_bf16 v[52:55], v[0:3], a[112:119], v205\n"
  "v_smfmac_f32_16x16x64_bf16 v[52:55], v[8:11], a[128:135], v205\n"
  "s_cmp_ge_u32 s44, 63\n"
  "s_cbranch_scc1 Lc2_ek_%=\n"
  "Lc2_ekb_%=:\n"
  "v_smfmac_f32_16x16x64_bf16 v[52:55], v[4:7], a[120:127], v205\n"
  "v_smfmac_f32_16x16x64_bf16 v[52:55], v[12:15], a[136:143], v205\n"
  "v_smfmac_f32_16x16x64_bf16 v[56:59], v[16:19], a[200:207], v205\n"
  "v_smfmac_f32_16x16x64_bf16 v[56:59], v[20:23], a[208:215], v205\n"
  "v_add_f32 v90, v52, v54\n"
  "v_smfmac_f32_16x16x64_bf16 v[56:59], v[24:27], a[216:223], v205\n"
  "v_add_f32 v91, v53, v55\n"
  "v_smfmac_f32_16x16x64_bf16 v[56:59], v[0:3], a[168:175], v205\n"
  "v_cvt_pk_bf16_f32 v112, v90, v91\n"
  "buffer_store_dword v112, v206, s[40:43], s45 offen offset:0 sc1\n"
  "v_smfmac_f32_16x16x64_bf16 v[56:59], v[8:11], a[184:191], v205\n"
  "v_smfmac_f32_16x16x64_bf16 v[56:59], v[4:7], a[176:183], v205\n"
  "v_smfmac_f32_16x16x64_bf16 v[56:59], v[12:15], a[192:199], v205\n"
  "s_nop 7\n"
  "v_pk_add_f32 v[90:91], v[56:57], v[58:59]\n"
  "v_cvt_pk_bf16_f32 v113, v90, v91\n"
  "buffer_store_dword v113, v206, s[40:43], s45 offen offset:256 sc1\n"
  "s_nop 1\n"
  "v_permlane16_swap_b32 v112, v113\n"
  "v_mov_b32 v114, v112\n"
  "v_mov_b32 v115, v113\n"
  "s_nop 0\n"
  "v_permlane32_swap_b32 v112, v114\n"
  "v_permlane32_swap_b32 v113, v115\n"
  "v_cmp_u_f32 vcc, v90, v91\n"
  "s_cbranch_vccnz Lc2_sm_%=\n"
  "Lc2_smb_%=:\n"
  "s_waitcnt lgkmcnt(0)\n"
  "s_barrier\n"
  "ds_read_b32 v29, v28 offset:7424\n"
  "s_waitcnt lgkmcnt(0)\n"
  "v_cmp_ne_u32 vcc, 0, v29\n"
  "s_cbranch_vccnz Lc2_rt_%=\n"
  "s_lshl_b32 s51, s44, 12\n"
  "s_andn2_b32 s51, 0x2000, s51\n"
  "s_xor_b32 s51, s51, s75\n"
  "s_lshl_b32 s52, s44, 20\n"
  "s_add_u32 s51, s51, s52\n"
  "s_cmp_ge_u32 s44, 63\n"
  "s_cbranch_scc1 Lc2_fin_%=\n"
  "s_nop 14\n"
  "v_add_u32 v32, 1, v106\n"
  "s_cmp_ge_u32 s44, 61\n"
  "s_cselect_b32 s52, 41984, s99\n"
  "buffer_store_dword v32, v210, s[40:43], s52 offen offset:128 sc1\n"
  "buffer_store_dwordx4 v[112:115], v207, s[40:43], s51 offen offset:0 sc1\n"
  "buffer_store_dwordx4 v[64:67], v208, s[40:43], s51 offen offset:0 sc1\n"
  "buffer_store_dwordx4 v[68:71], v208, s[40:43], s51 offen offset:1024 sc1\n"
  "buffer_store_dword v100, v206, s[40:43], s47 offen offset:0 sc1\n"
  "buffer_store_dword v100, v206, s[40:43], s47 offen offset:256 sc1\n"
  "s_add_u32 s44, s44, 1\n"
  "s_mov_b32 s52, s45\n"
  "s_mov_b32 s45, s46\n"
  "s_mov_b32 s46, s47\n"
  "s_mov_b32 s47, s72\n"
  "s_mov_b32 s72, s52\n"
  "s_add_u32 s49, s49, 2097152\n"
  "s_xor_b32 s49, s49, 0x2000\n"
  "s_branch Lc2_top_%=\n"
  "Lc2_fin_%=:\n"
  "buffer_store_dwordx4 v[112:115], v207, s[40:43], s98 offen offset:0 sc1\n"
  "s_waitcnt vmcnt(0)\n"
  "s_barrier\n"
  "v_mov_b32 v106, 64\n"
  "buffer_store_dword v106, v210, s[40:43], 0 offen offset:0 sc1\n"
  "buffer_store_dword v106, v210, s[40:43], 0 offen offset:128 sc1\n"
  "s_branch Lc_end_%=\n"
  "Lc2_ev_%=:\n"
  "buffer_store_dword v100, v206, s[40:43], s45 offen offset:0 sc1\n"
  "buffer_store_dword v100, v206, s[40:43], s45 offen offset:256 sc1\n"
  "s_waitcnt vmcnt(3)\n"
  "ds_write_b128 v202, v[16:19] offset:4096\n"
  "v_smfmac_f32_16x16x64_bf16 v[44:47], v[16:19], a[32:39], v205\n"
  "v_smfmac_f32_16x16x64_bf16 v[48:51], v[16:19], a[88:95], v205\n"
  "s_waitcnt vmcnt(2)\n"
  "s_branch Lc2_evc_%=\n"
  "Lc2_ek_%=:\n"
  "s_waitcnt lgkmcnt(0)\n"
  "buffer_store_dwordx4 v[64:67], v208, s[40:43], s98 offen offset:0 sc1\n"
  "buffer_store_dwordx4 v[68:71], v208, s[40:43], s98 offen offset:1024 sc1\n"
  "s_branch Lc2_ekb_%=\n"
  "Lc2_sm_%=:\n"
  "s_cmp_eq_u32 s53, 0\n"
  "s_cbranch_scc1 Lc2_smb_%=\n"
  "s_sub_u32 s53, s53, 1\n"
  "ds_write_b32 v28, v100 offset:7424\n"
  "s_branch Lc2_smb_%=\n"
  "Lc2_rt_%=:\n"
  "s_mov_b32 s74, 1\n"
  "s_waitcnt lgkmcnt(0)\n"
  "s_barrier\n"
  "ds_write_b32 v28, v28 offset:7424\n"
  "s_waitcnt lgkmcnt(0)\n"
  "s_nop 7\n"
  "s_cmp_eq_u32 s50, 0\n"
  "s_cbranch_scc1 Lc2_s2_%=\n"
  "s_branch Lc2_top_%=\n"
  "Lc3_start_%=:\n"
  "v_mov_b32 v105, 0\n"
  "v_add_u32 v104, 32, v210\n"
  "v_mov_b32 v100, -1\n"
  "v_mov_b32 v101, -1\n"
  "v_mov_b32 v102, -1\n"
  "v_mov_b32 v103, -1\n"
  "v_mov_b32 v28, 0\n"
  "v_mov_b32 v31, 0x40000000\n"
  "v_mov_b32 v90, 2155520\n"
  "Lc3_cw_%=:\n"
  "buffer_load_dword v88, v90, s[40:43], 0 offen sc1\n"
  "s_waitcnt vmcnt(0)\n"
  "v_readfirstlane_b32 s73, v88\n"
  "s_nop 3\n"
  "s_cmp_lg_u32 s73, 0\n"
  "s_cbranch_scc1 Lc3_cg_%=\n"
  "s_sub_u32 s53, s53, 1\n"
  "s_cmp_lg_u32 s53, 0\n"
  "s_sleep 2\n"
  "s_cbranch_scc1 Lc3_cw_%=\n"
  "Lc3_cg_%=:\n"
  "s_lshl_b32 s75, s73, 12\n"
  "s_andn2_b32 s75, 0x2000, s75\n"
  "v_bfe_u32 v88, v200, 12, 2\n"
  "v_lshlrev_b32 v88, 2, v88\n"
  "v_lshrrev_b32 v88, v88, s73\n"
  "v_and_b32 v88, 7, v88\n"
  "v_and_b32 v200, 0xffff8fff, v200\n"
  "v_lshl_or_b32 v200, v88, 12, v200\n"
  "v_bfe_u32 v88, v201, 12, 2\n"
  "v_lshlrev_b32 v88, 2, v88\n"
  "v_lshrrev_b32 v88, v88, s73\n"
  "v_and_b32 v88, 7, v88\n"
  "v_and_b32 v201, 0xffff8fff, v201\n"
  "v_lshl_or_b32 v201, v88, 12, v201\n"
  "v_bfe_u32 v88, v206, 12, 2\n"
  "v_lshlrev_b32 v88, 2, v88\n"
  "v_lshrrev_b32 v88, v88, s73\n"
  "v_and_b32 v88, 7, v88\n"
  "v_and_b32 v206, 0xffff8fff, v206\n"
  "v_lshl_or_b32 v206, v88, 12, v206\n"
  "v_bfe_u32 v88, v208, 12, 2\n"
  "v_lshlrev_b32 v88, 2, v88\n"
  "v_lshrrev_b32 v88, v88, s73\n"
  "v_and_b32 v88, 7, v88\n"
  "v_and_b32 v208, 0xffff8fff, v208\n"
  "v_lshl_or_b32 v208, v88, 12, v208\n"
  "s_mov_b32 s49, s75\n"
  "s_xor_b32 s98, s75, 66068480\n"
  "s_xor_b32 s52, s75, 65011712\n"
  "s_xor_b32 s73, s75, 63971328\n"
  "buffer_store_dwordx4 v[100:103], v207, s[40:43], s98 offen offset:0 sc1\n"
  "buffer_store_dwordx4 v[100:103], v207, s[40:43], s52 offen offset:0 sc1\n"
  "buffer_store_dwordx4 v[100:103], v207, s[40:43], s73 offen offset:0 sc1\n"
  "v_accvgpr_write_b32 a56, v100\n"
  "v_accvgpr_write_b32 a64, v100\n"
  "v_accvgpr_write_b32 a72, v100\n"
  "v_accvgpr_write_b32 a80, v100\n"
  "v_accvgpr_write_b32 a88, v100\n"
  "v_accvgpr_write_b32 a96, v100\n"
  "v_accvgpr_write_b32 a104, v100\n"
  "ds_write_b32 v28, v100 offset:7424\n"
  "s_waitcnt lgkmcnt(0)\n"
  "s_branch Lc3_top_%=\n"
  "Lc3_s2_%=:\n"
  "s_add_u32 s52, s44, 1\n"
  "v_cmp_gt_u32 vcc, s52, v105\n"
  "s_add_u32 s68, s49, 4096\n"
  "s_add_u32 s69, s49, 16384\n"
  "s_add_u32 s70, s49, 20480\n"
  "s_cbranch_vccnz Lc3_k0_%=\n"
  "Lc3_k0b_%=:\n"
  "buffer_load_dwordx4 a[56:59], v211, s[40:43], s49 offen offset:0 sc1\n"
  "buffer_load_dwordx4 a[60:63], v211, s[40:43], s49 offen offset:1024 sc1\n"
  "buffer_load_dwordx4 a[64:67], v211, s[40:43], s49 offen offset:2048 sc1\n"
  "buffer_load_dwordx4 a[68:71], v211, s[40:43], s49 offen offset:3072 sc1\n"
  "buffer_load_dwordx4 a[72:75], v211, s[40:43], s68 offen offset:0 sc1\n"
  "buffer_load_dwordx4 a[76:79], v211, s[40:43], s68 offen offset:1024 sc1\n"
  "buffer_load_dwordx4 a[80:83], v211, s[40:43], s68 offen offset:2048 sc1\n"
  "buffer_load_dwordx4 a[84:87], v211, s[40:43], s68 offen offset:3072 sc1\n"
  "buffer_load_dwordx4 a[88:91], v211, s[40:43], s69 offen offset:0 sc1\n"
  "buffer_load_dwordx4 a[92:95], v211, s[40:43], s69 offen offset:1024 sc1\n"
  "buffer_load_dwordx4 a[96:99], v211, s[40:43], s69 offen offset:2048 sc1\n"
  "buffer_load_dwordx4 a[100:103], v211, s[40:43], s69 offen offset:3072 sc1\n"
  "buffer_load_dwordx4 a[104:107], v211, s[40:43], s70 offen offset:0 sc1\n"
  "buffer_load_dwordx4 a[108:111], v211, s[40:43], s70 offen offset:1024 sc1\n"
  "s_waitcnt vmcnt(0)\n"
  "s_add_u32 s49, s49, 2097152\n"
  "s_xor_b32 s49, s49, 0x2000\n"
  "s_mov_b32 s50, 1\n"
  "s_mov_b32 s74, 0\n"
  "ds_write_b32 v28, v28 offset:7428\n"
  "s_waitcnt lgkmcnt(0)\n"
  ".p2align 6\n"
  "Lc3_top_%=:\n"
  "v_mov_b32 v44, 0\n"
  "v_mov_b32 v45, 0\n"
  "v_mov_b32 v46, 0\n"
  "v_mov_b32 v47, 0\n"
  "v_mov_b32 v48, 0\n"
  "v_mov_b32 v49, 0\n"
  "v_mov_b32 v50, 0\n"
  "v_mov_b32 v51, 0\n"
  "v_mov_b32 v52, 0\n"
  "v_mov_b32 v53, 0\n"
  "v_mov_b32 v54, 0\n"
  "v_mov_b32 v55, 0\n"
  "v_mov_b32 v56, 0\n"
  "v_mov_b32 v57, 0\n"
  "v_mov_b32 v58, 0\n"
  "v_mov_b32 v59, 0\n"
  "Lc3_ge_%=:\n"
  "s_barrier\n"
  "buffer_load_dwordx4 v[24:27], v200, s[40:43], s45 offen offset:0 sc1\n"
  "ds_read_b32 v29, v28 offset:7428\n"
  "s_waitcnt vmcnt(1)\n"
  "s_add_u32 s52, s44, 2\n"
  "s_min_u32 s52, s52, 64\n"
  "v_cmp_gt_u32 vcc, s52, v105\n"
  "s_nop 4\n"
  "s_cmp_lg_u64 vcc, 0\n"
  "s_cselect_b32 s48, s99, 0\n"
  "s_or_b32 s67, s49, s48\n"
  "s_add_u32 s68, s67, 4096\n"
  "s_add_u32 s69, s67, 16384\n"
  "s_add_u32 s70, s67, 20480\n"
  "s_waitcnt lgkmcnt(0)\n"
  "v_cmp_ne_u32 vcc, 0, v29\n"
  "s_cbranch_vccnz Lc3_ev_%=\n"
  "buffer_load_dwordx4 v[116:119], v211, s[40:43], s67 offen offset:0 sc1\n"
  "buffer_load_dwordx4 v[120:123], v211, s[40:43], s67 offen offset:1024 sc1\n"
  "buffer_load_dwordx4 v[124:127], v211, s[40:43], s67 offen offset:2048 sc1\n"
  "buffer_load_dwordx4 v[128:131], v211, s[40:43], s67 offen offset:3072 sc1\n"
  "buffer_load_dwordx4 v[132:135], v211, s[40:43], s68 offen offset:0 sc1\n"
  "buffer_load_dwordx4 v[136:139], v211, s[40:43], s68 offen offset:1024 sc1\n"
  "buffer_load_dwordx4 v[140:143], v211, s[40:43], s68 offen offset:2048 sc1\n"
  "buffer_load_dwordx4 v[144:147], v211, s[40:43], s68 offen offset:3072 sc1\n"
  "buffer_load_dwordx4 v[148:151], v211, s[40:43], s69 offen offset:0 sc1\n"
  "buffer_load_dwordx4 v[152:155], v211, s[40:43], s69 offen offset:1024 sc1\n"
  "buffer_load_dwordx4 v[156:159], v211, s[40:43], s69 offen offset:2048 sc1\n"
  "buffer_load_dwordx4 v[160:163], v211, s[40:43], s69 offen offset:3072 sc1\n"
  "buffer_load_dwordx4 v[164:167], v211, s[40:43], s70 offen offset:0 sc1\n"
  "buffer_load_dwordx4 v[168:171], v211, s[40:43], s70 offen offset:1024 sc1\n"
  "buffer_load_dword v105, v212, s[40:43], 0 offen offset:0 sc1\n"
  "s_waitcnt vmcnt(15)\n"
  "ds_write_b128 v202, v[24:27] offset:6144\n"
  "v_smfmac_f32_16x16x64_bf16 v[44:47], v[24:27], a[48:55], v205\n"
  "v_smfmac_f32_16x16x64_bf16 v[48:51], v[24:27], a[104:111], v205\n"
  "Lc3_evc_%=:\n"
  "s_waitcnt lgkmcnt(0)\n"
  "s_barrier\n"
  "ds_read_b128 v[0:3], v202 offset:0\n"
  "ds_read_b128 v[8:11], v202 offset:2048\n"
  "ds_read_b128 v[16:19], v202 offset:4096\n"
  "s_waitcnt lgkmcnt(3)\n"
  "s_barrier\n"
  "ds_read_b128 v[4:7], v202 offset:1024\n"
  "ds_read_b128 v[12:15], v202 offset:3072\n"
  "ds_read_b128 v[20:23], v202 offset:5120\n"
  "s_waitcnt lgkmcnt(5)\n"
  "v_smfmac_f32_16x16x64_bf16 v[44:47], v[0:3], a[0:7], v205\n"
  "s_waitcnt lgkmcnt(4)\n"
  "v_smfmac_f32_16x16x64_bf16 v[44:47], v[8:11], a[16:23], v205\n"
  "s_waitcnt lgkmcnt(3)\n"
  "v_smfmac_f32_16x16x64_bf16 v[44:47], v[16:19], a[32:39], v205\n"
  "v_smfmac_f32_16x16x64_bf16 v[48:51], v[0:3], a[56:63], v205\n"
  "s_waitcnt lgkmcnt(2)\n"
  "v_smfmac_f32_16x16x64_bf16 v[44:47], v[4:7], a[8:15], v205\n"
  "s_waitcnt lgkmcnt(1)\n"
  "v_smfmac_f32_16x16x64_bf16 v[44:47], v[12:15], a[24:31], v205\n"
  "s_waitcnt lgkmcnt(0)\n"
  "v_smfmac_f32_16x16x64_bf16 v[44:47], v[20:23], a[40:47], v205\n"
  "v_smfmac_f32_16x16x64_bf16 v[48:51], v[8:11], a[72:79], v205\n"
  "v_smfmac_f32_16x16x64_bf16 v[48:51], v[16:19], a[88:95], v205\n"
  "s_mov_b32 s51, 0x40000000\n"
  "v_add_f32 v88, v44, v46\n"
  "v_smfmac_f32_16x16x64_bf16 v[48:51], v[4:7], a[64:71], v205\n"
  "v_add_f32 v89, v45, v47\n"
  "v_smfmac_f32_16x16x64_bf16 v[48:51], v[12:15], a[80:87], v205\n"
  "v_cvt_pk_bf16_f32 v92, v88, v89\n"
  "ds_write_b32 v203, v92\n"
  "v_smfmac_f32_16x16x64_bf16 v[48:51], v[20:23], a[96:103], v205\n"
  "s_nop 7\n"
  "v_pk_add_f32 v[90:91], v[48:49], v[50:51]\n"
  "buffer_store_dword v90, v208, s[40:43], s45 offen offset:0 sc1\n"
  "buffer_store_dword v91, v208, s[40:43], s45 offen offset:128 sc1\n"
  "s_waitcnt lgkmcnt(0)\n"
  "s_barrier\n"
  "v_smfmac_f32_16x16x64_bf16 v[52:55], v[24:27], a[160:167], v205\n"
  "v_smfmac_f32_16x16x64_bf16 v[52:55], v[0:3], a[112:119], v205\n"
  "v_smfmac_f32_16x16x64_bf16 v[52:55], v[8:11], a[128:135], v205\n"
  "v_smfmac_f32_16x16x64_bf16 v[52:55], v[16:19], a[144:151], v205\n"
  "v_smfmac_f32_16x16x64_bf16 v[52:55], v[4:7], a[120:127], v205\n"
  "v_smfmac_f32_16x16x64_bf16 v[52:55], v[12:15], a[136:143], v205\n"
  "v_smfmac_f32_16x16x64_bf16 v[52:55], v[20:23], a[152:159], v205\n"
  "v_smfmac_f32_16x16x64_bf16 v[56:59], v[24:27], a[216:223], v205\n"
  "v_smfmac_f32_16x16x64_bf16 v[56:59], v[0:3], a[168:175], v205\n"
  "v_add_f32 v90, v52, v54\n"
  "v_smfmac_f32_16x16x64_bf16 v[56:59], v[8:11], a[184:191], v205\n"
  "v_add_f32 v91, v53, v55\n"
  "v_smfmac_f32_16x16x64_bf16 v[56:59], v[16:19], a[200:207], v205\n"
  "v_cvt_pk_bf16_f32 v112, v90, v91\n"
  "buffer_store_dword v112, v206, s[40:43], s45 offen offset:0 sc1\n"
  "v_smfmac_f32_16x16x64_bf16 v[56:59], v[4:7], a[176:183], v205\n"
  "v_smfmac_f32_16x16x64_bf16 v[56:59], v[12:15], a[192:199], v205\n"
  "v_smfmac_f32_16x16x64_bf16 v[56:59], v[20:23], a[208:215], v205\n"
  "s_nop 7\n"
  "v_pk_add_f32 v[90:91], v[56:57], v[58:59]\n"
  "v_cvt_pk_bf16_f32 v113, v90, v91\n"
  "buffer_store_dword v113, v206, s[40:43], s45 offen offset:256 sc1\n"
  "s_nop 1\n"
  "v_permlane16_swap_b32 v112, v113\n"
  "v_mov_b32 v114, v112\n"
  "v_mov_b32 v115, v113\n"
  "s_nop 0\n"
  "v_permlane32_swap_b32 v112, v114\n"
  "v_permlane32_swap_b32 v113, v115\n"
  "v_cmp_u_f32 vcc, v90, v91\n"
  "s_cbranch_vccnz Lc3_sm_%=\n"
  "Lc3_smb_%=:\n"
  "s_waitcnt lgkmcnt(0)\n"
  "s_barrier\n"
  "ds_read_b32 v29, v28 offset:7424\n"
  "s_waitcnt lgkmcnt(0)\n"
  "v_cmp_ne_u32 vcc, 0, v29\n"
  "s_cbranch_vccnz Lc3_rt_%=\n"
  "s_lshl_b32 s51, s44, 12\n"
  "s_andn2_b32 s51, 0x2000, s51\n"
  "s_xor_b32 s51, s51, s75\n"
  "s_lshl_b32 s52, s44, 20\n"
  "s_add_u32 s51, s51, s52\n"
  "s_cmp_ge_u32 s44, 63\n"
  "s_cbranch_scc1 Lc3_fin_%=\n"
  "s_cmp_lg_u32 s48, 0\n"
  "s_cbranch_scc1 Lc3_lk_%=\n"
  "Lc3_lkb_%=:\n"
  "s_nop 14\n"
  "buffer_store_dwordx4 v[112:115], v207, s[40:43], s51 offen offset:0 sc1\n"
  "buffer_store_dword v100, v208, s[40:43], s47 offen offset:0 sc1\n"
  "buffer_store_dword v100, v208, s[40:43], s47 offen offset:128 sc1\n"
  "buffer_store_dword v100, v206, s[40:43], s47 offen offset:0 sc1\n"
  "buffer_store_dword v100, v206, s[40:43], s47 offen offset:256 sc1\n"
  "s_cmp_lg_u32 s48, 0\n"
  "s_cbranch_scc1 Lc3_nc_%=\n"
  "s_waitcnt vmcnt(9)\n"
  "v_accvgpr_write_b32 a56, v116\n"
  "v_accvgpr_write_b32 a57, v117\n"
  "v_accvgpr_write_b32 a58, v118\n"
  "v_accvgpr_write_b32 a59, v119\n"
  "v_accvgpr_write_b32 a60, v120\n"
  "v_accvgpr_write_b32 a61, v121\n"
  "v_accvgpr_write_b32 a62, v122\n"
  "v_accvgpr_write_b32 a63, v123\n"
  "v_accvgpr_write_b32 a64, v124\n"
  "v_accvgpr_write_b32 a65, v125\n"
  "v_accvgpr_write_b32 a66, v126\n"
  "v_accvgpr_write_b32 a67, v127\n"
  "v_accvgpr_write_b32 a68, v128\n"
  "v_accvgpr_write_b32 a69, v129\n"
  "v_accvgpr_write_b32 a70, v130\n"
  "v_accvgpr_write_b32 a71, v131\n"
  "v_accvgpr_write_b32 a72, v132\n"
  "v_accvgpr_write_b32 a73, v133\n"
  "v_accvgpr_write_b32 a74, v134\n"
  "v_accvgpr_write_b32 a75, v135\n"
  "v_accvgpr_write_b32 a76, v136\n"
  "v_accvgpr_write_b32 a77, v137\n"
  "v_accvgpr_write_b32 a78, v138\n"
  "v_accvgpr_write_b32 a79, v139\n"
  "v_accvgpr_write_b32 a80, v140\n"
  "v_accvgpr_write_b32 a81, v141\n"
  "v_accvgpr_write_b32 a82, v142\n"
  "v_accvgpr_write_b32 a83, v143\n"
  "v_accvgpr_write_b32 a84, v144\n"
  "v_accvgpr_write_b32 a85, v145\n"
  "v_accvgpr_write_b32 a86, v146\n"
  "v_accvgpr_write_b32 a87, v147\n"
  "v_accvgpr_write_b32 a88, v148\n"
  "v_accvgpr_write_b32 a89, v149\n"
  "v_accvgpr_write_b32 a90, v150\n"
  "v_accvgpr_write_b32 a91, v151\n"
  "v_accvgpr_write_b32 a92, v152\n"
  "v_accvgpr_write_b32 a93, v153\n"
  "v_accvgpr_write_b32 a94, v154\n"
  "v_accvgpr_write_b32 a95, v155\n"
  "v_accvgpr_write_b32 a96, v156\n"
  "v_accvgpr_write_b32 a97, v157\n"
  "v_accvgpr_write_b32 a98, v158\n"
  "v_accvgpr_write_b32 a99, v159\n"
  "v_accvgpr_write_b32 a100, v160\n"
  "v_accvgpr_write_b32 a101, v161\n"
  "v_accvgpr_write_b32 a102, v162\n"
  "v_accvgpr_write_b32 a103, v163\n"
  "v_accvgpr_write_b32 a104, v164\n"
  "v_accvgpr_write_b32 a105, v165\n"
  "v_accvgpr_write_b32 a106, v166\n"
  "v_accvgpr_write_b32 a107, v167\n"
  "v_accvgpr_write_b32 a108, v168\n"
  "v_accvgpr_write_b32 a109, v169\n"
  "v_accvgpr_write_b32 a110, v170\n"
  "v_accvgpr_write_b32 a111, v171\n"
  "Lc3_nc_%=:\n"
  "buffer_load_dword v105, v212, s[40:43], 0 offen offset:0 sc1\n"
  "s_add_u32 s44, s44, 1\n"
  "s_mov_b32 s52, s45\n"
  "s_mov_b32 s45, s46\n"
  "s_mov_b32 s46, s47\n"
  "s_mov_b32 s47, s72\n"
  "s_mov_b32 s72, s52\n"
  "s_add_u32 s49, s49, 2097152\n"
  "s_xor_b32 s49, s49, 0x2000\n"
  "s_branch Lc3_top_%=\n"
  "Lc3_fin_%=:\n"
  "buffer_store_dwordx4 v[112:115], v207, s[40:43], s98 offen offset:0 sc1\n"
  "s_waitcnt vmcnt(0)\n"
  "s_barrier\n"
  "s_branch Lc_end_%=\n"
  "Lc3_k0_%=:\n"
  "s_cmp_eq_u32 s53, 0\n"
  "s_cbranch_scc1 Lc3_k0b_%=\n"
  "s_sub_u32 s53, s53, 1\n"
  "buffer_load_dword v105, v212, s[40:43], 0 offen offset:0 sc1\n"
  "s_waitcnt vmcnt(0)\n"
  "v_cmp_gt_u32 vcc, s52, v105\n"
  "s_cbranch_vccnz Lc3_k0_%=\n"
  "s_branch Lc3_k0b_%=\n"
  "Lc3_ev_%=:\n"
  "buffer_store_dword v100, v208, s[40:43], s45 offen offset:0 sc1\n"
  "buffer_store_dword v100, v208, s[40:43], s45 offen offset:128 sc1\n"
  "buffer_store_dword v100, v206, s[40:43], s45 offen offset:0 sc1\n"
  "buffer_store_dword v100, v206, s[40:43], s45 offen offset:256 sc1\n"
  "buffer_load_dwordx4 v[116:119], v211, s[40:43], s67 offen offset:0 sc1\n"
  "buffer_load_dwordx4 v[120:123], v211, s[40:43], s67 offen offset:1024 sc1\n"
  "buffer_load_dwordx4 v[124:127], v211, s[40:43], s67 offen offset:2048 sc1\n"
  "buffer_load_dwordx4 v[128:131], v211, s[40:43], s67 offen offset:3072 sc1\n"
  "buffer_load_dwordx4 v[132:135], v211, s[40:43], s68 offen offset:0 sc1\n"
  "buffer_load_dwordx4 v[136:139], v211, s[40:43], s68 offen offset:1024 sc1\n"
  "buffer_load_dwordx4 v[140:143], v211, s[40:43], s68 offen offset:2048 sc1\n"
  "buffer_load_dwordx4 v[144:147], v211, s[40:43], s68 offen offset:3072 sc1\n"
  "buffer_load_dwordx4 v[148:151], v211, s[40:43], s69 offen offset:0 sc1\n"
  "buffer_load_dwordx4 v[152:155], v211, s[40:43], s69 offen offset:1024 sc1\n"
  "buffer_load_dwordx4 v[156:159], v211, s[40:43], s69 offen offset:2048 sc1\n"
  "buffer_load_dwordx4 v[160:163], v211, s[40:43], s69 offen offset:3072 sc1\n"
  "buffer_load_dwordx4 v[164:167], v211, s[40:43], s70 offen offset:0 sc1\n"
  "buffer_load_dwordx4 v[168:171], v211, s[40:43], s70 offen offset:1024 sc1\n"
  "buffer_load_dword v105, v212, s[40:43], 0 offen offset:0 sc1\n"
  "s_waitcnt vmcnt(19)\n"
  "ds_write_b128 v202, v[24:27] offset:6144\n"
  "v_smfmac_f32_16x16x64_bf16 v[44:47], v[24:27], a[48:55], v205\n"
  "v_smfmac_f32_16x16x64_bf16 v[48:51], v[24:27], a[104:111], v205\n"
  "s_waitcnt vmcnt(19)\n"
  "s_branch Lc3_evc_%=\n"
  "Lc3_sm_%=:\n"
  "s_cmp_eq_u32 s53, 0\n"
  "s_cbranch_scc1 Lc3_smb_%=\n"
  "s_sub_u32 s53, s53, 1\n"
  "ds_write_b32 v28, v100 offset:7424\n"
  "s_branch Lc3_smb_%=\n"
  "Lc3_rt_%=:\n"
  "s_mov_b32 s74, 1\n"
  "s_waitcnt lgkmcnt(0)\n"
  "s_barrier\n"
  "ds_write_b32 v28, v28 offset:7424\n"
  "s_waitcnt lgkmcnt(0)\n"
  "s_nop 7\n"
  "s_cmp_eq_u32 s50, 0\n"
  "s_cbranch_scc1 Lc3_s2_%=\n"
  "s_branch Lc3_top_%=\n"
  "Lc3_fk_%=:\n"
  "s_cmp_eq_u32 s53, 0\n"
  "s_cbranch_scc1 Lc3_fkb_%=\n"
  "s_sub_u32 s53, s53, 1\n"
  "buffer_load_dword v105, v212, s[40:43], 0 offen offset:0 sc1\n"
  "s_waitcnt vmcnt(0)\n"
  "v_cmp_gt_u32 vcc, s52, v105\n"
  "s_cbranch_vccnz Lc3_fk_%=\n"
  "s_branch Lc3_fkb_%=\n"
  "Lc3_lk_%=:\n"
  "s_waitcnt vmcnt(4)\n"
  "s_add_u32 s52, s44, 2\n"
  "v_cmp_gt_u32 vcc, s52, v105\n"
  "s_add_u32 s68, s49, 4096\n"
  "s_add_u32 s69, s49, 16384\n"
  "s_add_u32 s70, s49, 20480\n"
  "s_cbranch_vccnz Lc3_fk_%=\n"
  "Lc3_fkb_%=:\n"
  "buffer_load_dwordx4 a[56:59], v211, s[40:43], s49 offen offset:0 sc1\n"
  "buffer_load_dwordx4 a[60:63], v211, s[40:43], s49 offen offset:1024 sc1\n"
  "buffer_load_dwordx4 a[64:67], v211, s[40:43], s49 offen offset:2048 sc1\n"
  "buffer_load_dwordx4 a[68:71], v211, s[40:43], s49 offen offset:3072 sc1\n"
  "buffer_load_dwordx4 a[72:75], v211, s[40:43], s68 offen offset:0 sc1\n"
  "buffer_load_dwordx4 a[76:79], v211, s[40:43], s68 offen offset:1024 sc1\n"
  "buffer_load_dwordx4 a[80:83], v211, s[40:43], s68 offen offset:2048 sc1\n"
  "buffer_load_dwordx4 a[84:87], v211, s[40:43], s68 offen offset:3072 sc1\n"
  "buffer_load_dwordx4 a[88:91], v211, s[40:43], s69 offen offset:0 sc1\n"
  "buffer_load_dwordx4 a[92:95], v211, s[40:43], s69 offen offset:1024 sc1\n"
  "buffer_load_dwordx4 a[96:99], v211, s[40:43], s69 offen offset:2048 sc1\n"
  "buffer_load_dwordx4 a[100:103], v211, s[40:43], s69 offen offset:3072 sc1\n"
  "buffer_load_dwordx4 a[104:107], v211, s[40:43], s70 offen offset:0 sc1\n"
  "buffer_load_dwordx4 a[108:111], v211, s[40:43], s70 offen offset:1024 sc1\n"
  "s_branch Lc3_lkb_%=\n"
  "Lc4_start_%=:\n"
  "v_mov_b32 v66, 0\n"
  "v_mov_b32 v64, -1\n"
  "v_mov_b32 v24, 0\n"
  "v_mov_b32 v44, 0\n"
  "v_mov_b32 v45, 0\n"
  "v_mov_b32 v51, 0\n"
  "v_mov_b32 v54, 2155520\n"
  "Lc4_cw_%=:\n"
  "buffer_load_dword v52, v54, s[40:43], 0 offen sc1\n"
  "s_waitcnt vmcnt(0)\n"
  "v_readfirstlane_b32 s73, v52\n"
  "s_nop 3\n"
  "s_cmp_lg_u32 s73, 0\n"
  "s_cbranch_scc1 Lc4_cg_%=\n"
  "s_sub_u32 s53, s53, 1\n"
  "s_cmp_lg_u32 s53, 0\n"
  "s_sleep 2\n"
  "s_cbranch_scc1 Lc4_cw_%=\n"
  "Lc4_cg_%=:\n"
  "s_lshl_b32 s75, s73, 12\n"
  "s_andn2_b32 s75, 0x2000, s75\n"
  "v_bfe_u32 v52, v200, 12, 2\n"
  "v_lshlrev_b32 v52, 2, v52\n"
  "v_lshrrev_b32 v52, v52, s73\n"
  "v_and_b32 v52, 7, v52\n"
  "v_and_b32 v200, 0xffff8fff, v200\n"
  "v_lshl_or_b32 v200, v52, 12, v200\n"
  "v_bfe_u32 v52, v201, 12, 2\n"
  "v_lshlrev_b32 v52, 2, v52\n"
  "v_lshrrev_b32 v52, v52, s73\n"
  "v_and_b32 v52, 7, v52\n"
  "v_and_b32 v201, 0xffff8fff, v201\n"
  "v_lshl_or_b32 v201, v52, 12, v201\n"
  "v_bfe_u32 v52, v202, 12, 2\n"
  "v_lshlrev_b32 v52, 2, v52\n"
  "v_lshrrev_b32 v52, v52, s73\n"
  "v_and_b32 v52, 7, v52\n"
  "v_and_b32 v202, 0xffff8fff, v202\n"
  "v_lshl_or_b32 v202, v52, 12, v202\n"
  "v_bfe_u32 v52, v203, 12, 2\n"
  "v_lshlrev_b32 v52, 2, v52\n"
  "v_lshrrev_b32 v52, v52, s73\n"
  "v_and_b32 v52, 7, v52\n"
  "v_and_b32 v203, 0xffff8fff, v203\n"
  "v_lshl_or_b32 v203, v52, 12, v203\n"
  "v_bfe_u32 v52, v204, 12, 2\n"
  "v_lshlrev_b32 v52, 2, v52\n"
  "v_lshrrev_b32 v52, v52, s73\n"
  "v_and_b32 v52, 7, v52\n"
  "v_and_b32 v204, 0xffff8fff, v204\n"
  "v_lshl_or_b32 v204, v52, 12, v204\n"
  "v_bfe_u32 v52, v209, 12, 2\n"
  "v_lshlrev_b32 v52, 2, v52\n"
  "v_lshrrev_b32 v52, v52, s73\n"
  "v_and_b32 v52, 7, v52\n"
  "v_and_b32 v209, 0xffff8fff, v209\n"
  "v_lshl_or_b32 v209, v52, 12, v209\n"
  "s_mov_b32 s48, s99\n"
  "v_mov_b32 v66, -1\n"
  "v_mov_b32 v76, -1\n"
  "v_mov_b32 v77, -1\n"
  "v_mov_b32 v78, -1\n"
  "v_mov_b32 v79, -1\n"
  "v_mov_b32 v80, -1\n"
  "v_mov_b32 v81, -1\n"
  "v_mov_b32 v82, -1\n"
  "v_mov_b32 v83, -1\n"
  "v_mov_b32 v84, -1\n"
  "v_mov_b32 v85, -1\n"
  "v_mov_b32 v86, -1\n"
  "v_mov_b32 v87, -1\n"
  "v_mov_b32 v88, -1\n"
  "v_mov_b32 v89, -1\n"
  "v_mov_b32 v90, -1\n"
  "v_mov_b32 v91, -1\n"
  "v_mov_b32 v92, -1\n"
  "v_mov_b32 v93, -1\n"
  "v_mov_b32 v94, -1\n"
  "v_mov_b32 v95, -1\n"
  "v_mov_b32 v96, -1\n"
  "v_mov_b32 v97, -1\n"
  "v_mov_b32 v98, -1\n"
  "v_mov_b32 v99, -1\n"
  "v_mov_b32 v100, -1\n"
  "v_mov_b32 v101, -1\n"
  "v_mov_b32 v102, -1\n"
  "v_mov_b32 v103, -1\n"
  "v_mov_b32 v104, -1\n"
  "v_mov_b32 v105, -1\n"
  "v_mov_b32 v106, -1\n"
  "v_mov_b32 v107, -1\n"
  "s_branch Lc4a_top_%=\n"
  "Lc4_s2_%=:\n"
  "s_mov_b32 s48, s75\n"
  "v_mov_b32 v66, 0\n"
  "s_add_u32 s52, s44, 1\n"
  "v_cmp_gt_u32 vcc, s52, v66\n"
  "s_cbranch_vccnz Lc4_v0_%=\n"
  "Lc4_v0b_%=:\n"
  "buffer_load_dwordx4 v[76:79], v205, s[40:43], s48 offen offset:0 sc1\n"
  "buffer_load_dwordx4 v[80:83], v205, s[40:43], s48 offen offset:2048 sc1\n"
  "buffer_load_dwordx4 v[84:87], v205, s[40:43], s48 offen offset:512 sc1\n"
  "buffer_load_dwordx4 v[88:91], v205, s[40:43], s48 offen offset:2560 sc1\n"
  "s_add_u32 s48, s48, 2097152\n"
  "s_xor_b32 s48, s48, 0x2000\n"
  "s_mov_b32 s50, 3\n"
  "s_add_u32 s52, s44, 2\n"
  "v_cmp_gt_u32 vcc, s52, v66\n"
  "s_cbranch_vccnz Lc4_v1_%=\n"
  "Lc4_v1b_%=:\n"
  "Lc4a_nxt_%=:\n"
  "Lc4a_top_%=:\n"
  "v_mov_b32 v32, 0\n"
  "v_mov_b32 v33, 0\n"
  "v_mov_b32 v34, 0\n"
  "v_mov_b32 v35, 0\n"
  "v_mov_b32 v36, 0\n"
  "v_mov_b32 v37, 0\n"
  "v_mov_b32 v38, 0\n"
  "v_mov_b32 v39, 0\n"
  "Lc4a_ge_%=:\n"
  "s_barrier\n"
  "buffer_load_dwordx4 v[16:19], v202, s[40:43], s45 offen offset:0 sc1\n"
  "s_nop 7\n"
  "buffer_load_dwordx4 v[20:23], v203, s[40:43], s45 offen offset:0 sc1\n"
  "s_nop 15\n"
  "s_nop 7\n"
  "buffer_load_dwordx4 v[8:11], v200, s[40:43], s45 offen offset:0 sc1\n"
  "s_nop 8\n"
  "buffer_load_dwordx4 v[12:15], v201, s[40:43], s45 offen offset:0 sc1\n"
  "s_nop 6\n"
  "s_waitcnt vmcnt(4)\n"
  "s_add_u32 s52, s44, 2\n"
  "v_cmp_gt_u32 vcc, s52, v66\n"
  "s_cbranch_vccnz Lc4a_fv_%=\n"
  "Lc4a_fvb_%=:\n"
  "s_nop 5\n"
  "s_nop 2\n"
  "v_mov_b32 v28, 0\n"
  "s_nop 2\n"
  "v_mov_b32 v28, 0\n"
  "s_nop 2\n"
  "v_mov_b32 v28, 0\n"
  "s_nop 2\n"
  "v_mov_b32 v28, 0\n"
  "s_nop 1\n"
  "v_mov_b32 v28, 0\n"
  "buffer_load_dword v40, v204, s[40:43], s45 offen offset:0 sc1\n"
  "v_mov_b32 v28, 0\n"
  "buffer_load_dword v41, v204, s[40:43], s45 offen offset:64 sc1\n"
  "v_mov_b32 v28, 0\n"
  "buffer_load_dword v42, v204, s[40:43], s45 offen offset:128 sc1\n"
  "v_mov_b32 v28, 0\n"
  "buffer_load_dword v43, v204, s[40:43], s45 offen offset:192 sc1\n"
  "v_mov_b32 v28, 0\n"
  "v_mov_b32 v28, 0\n"
  "s_barrier\n"
  "v_mov_b32 v28, 0\n"
  "buffer_load_dwordx4 v[92:95], v205, s[40:43], s48 offen offset:0 sc1\n"
  "v_mov_b32 v28, 0\n"
  "s_nop 2\n"
  "v_mov_b32 v28, 0\n"
  "s_nop 1\n"
  "v_mov_b32 v28, 0\n"
  "buffer_load_dwordx4 v[96:99], v205, s[40:43], s48 offen offset:2048 sc1\n"
  "v_mov_b32 v28, 0\n"
  "s_nop 2\n"
  "v_mov_b32 v28, 0\n"
  "s_nop 2\n"
  "v_mov_b32 v28, 0\n"
  "buffer_load_dwordx4 v[100:103], v205, s[40:43], s48 offen offset:512 sc1\n"
  "v_mov_b32 v28, 0\n"
  "s_nop 2\n"
  "v_mov_b32 v28, 0\n"
  "s_nop 2\n"
  "v_mov_b32 v28, 0\n"
  "s_nop 2\n"
  "v_mov_b32 v28, 0\n"
  "s_waitcnt vmcnt(9)\n"
  "v_max3_f32 v58, v16, v17, v18\n"
  "v_max3_f32 v58, v58, v19, v20\n"
  "v_max3_f32 v58, v58, v21, v22\n"
  "v_max_f32 v58, v58, v23\n"
  "v_pk_add_f32 v[0:1], v[16:17], v[58:59] op_sel_hi:[1,0] neg_lo:[0,1] neg_hi:[0,1]\n"
  "v_pk_add_f32 v[2:3], v[18:19], v[58:59] op_sel_hi:[1,0] neg_lo:[0,1] neg_hi:[0,1]\n"
  "v_pk_add_f32 v[4:5], v[20:21], v[58:59] op_sel_hi:[1,0] neg_lo:[0,1] neg_hi:[0,1]\n"
  "v_pk_add_f32 v[6:7], v[22:23], v[58:59] op_sel_hi:[1,0] neg_lo:[0,1] neg_hi:[0,1]\n"
  "v_exp_f32 v0, v0\n"
  "v_exp_f32 v1, v1\n"
  "v_exp_f32 v2, v2\n"
  "v_exp_f32 v3, v3\n"
  "v_exp_f32 v4, v4\n"
  "v_exp_f32 v5, v5\n"
  "v_exp_f32 v6, v6\n"
  "v_exp_f32 v7, v7\n"
  "v_pk_add_f32 v[52:53], v[0:1], v[2:3]\n"
  "v_pk_add_f32 v[54:55], v[4:5], v[6:7]\n"
  "v_pk_add_f32 v[52:53], v[52:53], v[54:55]\n"
  "v_add_f32 v59, v52, v53\n"
  "s_waitcnt vmcnt(8)\n"
  "v_add_f32_dpp v52, v10, v8 row_ror:8 row_mask:0xf bank_mask:0xf\n"
  "v_add_f32_dpp v53, v11, v9 row_ror:8 row_mask:0xf bank_mask:0xf\n"
  "s_nop 0\n"
  "v_mov_b32 v28, 0\n"
  "s_waitcnt vmcnt(7)\n"
  "v_add_f32_dpp v54, v14, v12 row_ror:8 row_mask:0xf bank_mask:0xf\n"
  "v_add_f32_dpp v55, v15, v13 row_ror:8 row_mask:0xf bank_mask:0xf\n"
  "v_pk_add_f32 v[52:53], v[52:53], v[54:55]\n"
  "v_max3_f32 v56, v52, v53, v58\n"
  "s_nop 1\n"
  "v_max_f32_dpp v56, v56, v56 quad_perm:[1,0,3,2] row_mask:0xf bank_mask:0xf\n"
  "s_nop 1\n"
  "v_max_f32_dpp v56, v56, v56 quad_perm:[2,3,0,1] row_mask:0xf bank_mask:0xf\n"
  "v_sub_f32 v54, v58, v56\n"
  "v_pk_add_f32 v[52:53], v[52:53], v[56:57] op_sel_hi:[1,0] neg_lo:[0,1] neg_hi:[0,1]\n"
  "v_exp_f32 v54, v54\n"
  "v_exp_f32 v52, v52\n"
  "v_exp_f32 v53, v53\n"
  "v_fma_f32 v56, v54, v59, v52\n"
  "v_add_f32 v56, v56, v53\n"
  "s_nop 1\n"
  "v_add_f32_dpp v56, v56, v56 quad_perm:[1,0,3,2] row_mask:0xf bank_mask:0xf\n"
  "s_nop 1\n"
  "v_add_f32_dpp v56, v56, v56 quad_perm:[2,3,0,1] row_mask:0xf bank_mask:0xf\n"
  "v_rcp_f32 v56, v56\n"
  "s_nop 0\n"
  "v_mul_f32 v54, v54, v56\n"
  "v_pk_mul_f32 v[0:1], v[0:1], v[54:55] op_sel_hi:[1,0]\n"
  "v_pk_mul_f32 v[2:3], v[2:3], v[54:55] op_sel_hi:[1,0]\n"
  "v_pk_mul_f32 v[4:5], v[4:5], v[54:55] op_sel_hi:[1,0]\n"
  "v_pk_mul_f32 v[6:7], v[6:7], v[54:55] op_sel_hi:[1,0]\n"
  "v_cvt_pk_bf16_f32 v46, v0, v1\n"
  "v_cvt_pk_bf16_f32 v48, v2, v3\n"
  "v_cvt_pk_bf16_f32 v47, v4, v5\n"
  "v_cvt_pk_bf16_f32 v49, v6, v7\n"
  "v_pk_mul_f32 v[52:53], v[52:53], v[56:57] op_sel_hi:[1,0]\n"
  "v_cvt_pk_bf16_f32 v50, v52, v53\n"
  "v_smfmac_f32_16x16x64_bf16 v[32:35], v[46:49], v[76:83], v208\n"
  "v_smfmac_f32_16x16x64_bf16 v[36:39], v[46:49], v[84:91], v208\n"
  "s_waitcnt vmcnt(3)\n"
  "v_smfmac_f32_16x16x32_bf16 v[32:35], v[50:51], v[40:43], v208\n"
  "v_smfmac_f32_16x16x32_bf16 v[36:39], v[50:51], v[42:45], v208\n"
  "s_nop 3\n"
  "v_add_f32 v52, v32, v33\n"
  "s_nop 2\n"
  "v_add_f32 v53, v38, v39\n"
  "v_add_f32 v52, v52, v53\n"
  "ds_write_b32 v206, v52\n"
  "v_cmp_u_f32 vcc, v52, v52\n"
  "s_waitcnt lgkmcnt(0)\n"
  "s_barrier\n"
  "s_cbranch_vccnz Lc4a_sb_%=\n"
  "Lc4a_sbb_%=:\n"
  "s_waitcnt lgkmcnt(0)\n"
  "s_barrier\n"
  "ds_read_b32 v25, v24 offset:7424\n"
  "s_waitcnt lgkmcnt(0)\n"
  "v_cmp_ne_u32 vcc, 0, v25\n"
  "s_cbranch_vccnz Lc4a_rt_%=\n"
  "s_add_u32 s44, s44, 1\n"
  "s_mov_b32 s52, s45\n"
  "s_mov_b32 s45, s46\n"
  "s_mov_b32 s46, s47\n"
  "s_mov_b32 s47, s72\n"
  "s_mov_b32 s72, s52\n"
  "s_cmp_ge_u32 s44, 63\n"
  "s_cbranch_scc1 Lc4_out_%=\n"
  "s_nop 15\n"
  "s_nop 13\n"
  "buffer_load_dwordx4 v[104:107], v205, s[40:43], s48 offen offset:2560 sc1\n"
  "buffer_load_dword v66, v207, s[40:43], 0 offen offset:0 sc1\n"
  "s_add_u32 s48, s48, 2097152\n"
  "s_xor_b32 s48, s48, 0x2000\n"
  "Lc4b_nxt_%=:\n"
  "Lc4b_top_%=:\n"
  "v_mov_b32 v32, 0\n"
  "v_mov_b32 v33, 0\n"
  "v_mov_b32 v34, 0\n"
  "v_mov_b32 v35, 0\n"
  "v_mov_b32 v36, 0\n"
  "v_mov_b32 v37, 0\n"
  "v_mov_b32 v38, 0\n"
  "v_mov_b32 v39, 0\n"
  "Lc4b_ge_%=:\n"
  "s_barrier\n"
  "buffer_load_dwordx4 v[16:19], v202, s[40:43], s45 offen offset:0 sc1\n"
  "s_nop 7\n"
  "buffer_load_dwordx4 v[20:23], v203, s[40:43], s45 offen offset:0 sc1\n"
  "s_nop 15\n"
  "s_nop 7\n"
  "buffer_load_dwordx4 v[8:11], v200, s[40:43], s45 offen offset:0 sc1\n"
  "s_nop 8\n"
  "buffer_load_dwordx4 v[12:15], v201, s[40:43], s45 offen offset:0 sc1\n"
  "s_nop 6\n"
  "s_waitcnt vmcnt(4)\n"
  "s_add_u32 s52, s44, 2\n"
  "v_cmp_gt_u32 vcc, s52, v66\n"
  "s_cbranch_vccnz Lc4b_fv_%=\n"
  "Lc4b_fvb_%=:\n"
  "s_nop 5\n"
  "s_nop 2\n"
  "v_mov_b32 v28, 0\n"
  "s_nop 2\n"
  "v_mov_b32 v28, 0\n"
  "s_nop 2\n"
  "v_mov_b32 v28, 0\n"
  "s_nop 2\n"
  "v_mov_b32 v28, 0\n"
  "s_nop 1\n"
  "v_mov_b32 v28, 0\n"
  "buffer_load_dword v40, v204, s[40:43], s45 offen offset:0 sc1\n"
  "v_mov_b32 v28, 0\n"
  "buffer_load_dword v41, v204, s[40:43], s45 offen offset:64 sc1\n"
  "v_mov_b32 v28, 0\n"
  "buffer_load_dword v42, v204, s[40:43], s45 offen offset:128 sc1\n"
  "v_mov_b32 v28, 0\n"
  "buffer_load_dword v43, v204, s[40:43], s45 offen offset:192 sc1\n"
  "v_mov_b32 v28, 0\n"
  "v_mov_b32 v28, 0\n"
  "s_barrier\n"
  "v_mov_b32 v28, 0\n"
  "buffer_load_dwordx4 v[76:79], v205, s[40:43], s48 offen offset:0 sc1\n"
  "v_mov_b32 v28, 0\n"
  "s_nop 2\n"
  "v_mov_b32 v28, 0\n"
  "s_nop 1\n"
  "v_mov_b32 v28, 0\n"
  "buffer_load_dwordx4 v[80:83], v205, s[40:43], s48 offen offset:2048 sc1\n"
  "v_mov_b32 v28, 0\n"
  "s_nop 2\n"
  "v_mov_b32 v28, 0\n"
  "s_nop 2\n"
  "v_mov_b32 v28, 0\n"
  "buffer_load_dwordx4 v[84:87], v205, s[40:43], s48 offen offset:512 sc1\n"
  "v_mov_b32 v28, 0\n"
  "s_nop 2\n"
  "v_mov_b32 v28, 0\n"
  "s_nop 2\n"
  "v_mov_b32 v28, 0\n"
  "s_nop 2\n"
  "v_mov_b32 v28, 0\n"
  "s_waitcnt vmcnt(9)\n"
  "v_max3_f32 v58, v16, v17, v18\n"
  "v_max3_f32 v58, v58, v19, v20\n"
  "v_max3_f32 v58, v58, v21, v22\n"
  "v_max_f32 v58, v58, v23\n"
  "v_pk_add_f32 v[0:1], v[16:17], v[58:59] op_sel_hi:[1,0] neg_lo:[0,1] neg_hi:[0,1]\n"
  "v_pk_add_f32 v[2:3], v[18:19], v[58:59] op_sel_hi:[1,0] neg_lo:[0,1] neg_hi:[0,1]\n"
  "v_pk_add_f32 v[4:5], v[20:21], v[58:59] op_sel_hi:[1,0] neg_lo:[0,1] neg_hi:[0,1]\n"
  "v_pk_add_f32 v[6:7], v[22:23], v[58:59] op_sel_hi:[1,0] neg_lo:[0,1] neg_hi:[0,1]\n"
  "v_exp_f32 v0, v0\n"
  "v_exp_f32 v1, v1\n"
  "v_exp_f32 v2, v2\n"
  "v_exp_f32 v3, v3\n"
  "v_exp_f32 v4, v4\n"
  "v_exp_f32 v5, v5\n"
  "v_exp_f32 v6, v6\n"
  "v_exp_f32 v7, v7\n"
  "v_pk_add_f32 v[52:53], v[0:1], v[2:3]\n"
  "v_pk_add_f32 v[54:55], v[4:5], v[6:7]\n"
  "v_pk_add_f32 v[52:53], v[52:53], v[54:55]\n"
  "v_add_f32 v59, v52, v53\n"
  "s_waitcnt vmcnt(8)\n"
  "v_add_f32_dpp v52, v10, v8 row_ror:8 row_mask:0xf bank_mask:0xf\n"
  "v_add_f32_dpp v53, v11, v9 row_ror:8 row_mask:0xf bank_mask:0xf\n"
  "s_nop 0\n"
  "v_mov_b32 v28, 0\n"
  "s_waitcnt vmcnt(7)\n"
  "v_add_f32_dpp v54, v14, v12 row_ror:8 row_mask:0xf bank_mask:0xf\n"
  "v_add_f32_dpp v55, v15, v13 row_ror:8 row_mask:0xf bank_mask:0xf\n"
  "v_pk_add_f32 v[52:53], v[52:53], v[54:55]\n"
  "v_max3_f32 v56, v52, v53, v58\n"
  "s_nop 1\n"
  "v_max_f32_dpp v56, v56, v56 quad_perm:[1,0,3,2] row_mask:0xf bank_mask:0xf\n"
  "s_nop 1\n"
  "v_max_f32_dpp v56, v56, v56 quad_perm:[2,3,0,1] row_mask:0xf bank_mask:0xf\n"
  "v_sub_f32 v54, v58, v56\n"
  "v_pk_add_f32 v[52:53], v[52:53], v[56:57] op_sel_hi:[1,0] neg_lo:[0,1] neg_hi:[0,1]\n"
  "v_exp_f32 v54, v54\n"
  "v_exp_f32 v52, v52\n"
  "v_exp_f32 v53, v53\n"
  "v_fma_f32 v56, v54, v59, v52\n"
  "v_add_f32 v56, v56, v53\n"
  "s_nop 1\n"
  "v_add_f32_dpp v56, v56, v56 quad_perm:[1,0,3,2] row_mask:0xf bank_mask:0xf\n"
  "s_nop 1\n"
  "v_add_f32_dpp v56, v56, v56 quad_perm:[2,3,0,1] row_mask:0xf bank_mask:0xf\n"
  "v_rcp_f32 v56, v56\n"
  "s_nop 0\n"
  "v_mul_f32 v54, v54, v56\n"
  "v_pk_mul_f32 v[0:1], v[0:1], v[54:55] op_sel_hi:[1,0]\n"
  "v_pk_mul_f32 v[2:3], v[2:3], v[54:55] op_sel_hi:[1,0]\n"
  "v_pk_mul_f32 v[4:5], v[4:5], v[54:55] op_sel_hi:[1,0]\n"
  "v_pk_mul_f32 v[6:7], v[6:7], v[54:55] op_sel_hi:[1,0]\n"
  "v_cvt_pk_bf16_f32 v46, v0, v1\n"
  "v_cvt_pk_bf16_f32 v48, v2, v3\n"
  "v_cvt_pk_bf16_f32 v47, v4, v5\n"
  "v_cvt_pk_bf16_f32 v49, v6, v7\n"
  "v_pk_mul_f32 v[52:53], v[52:53], v[56:57] op_sel_hi:[1,0]\n"
  "v_cvt_pk_bf16_f32 v50, v52, v53\n"
  "v_smfmac_f32_16x16x64_bf16 v[32:35], v[46:49], v[92:99], v208\n"
  "v_smfmac_f32_16x16x64_bf16 v[36:39], v[46:49], v[100:107], v208\n"
  "s_waitcnt vmcnt(3)\n"
  "v_smfmac_f32_16x16x32_bf16 v[32:35], v[50:51], v[40:43], v208\n"
  "v_smfmac_f32_16x16x32_bf16 v[36:39], v[50:51], v[42:45], v208\n"
  "s_nop 3\n"
  "v_add_f32 v52, v32, v33\n"
  "s_nop 2\n"
  "v_add_f32 v53, v38, v39\n"
  "v_add_f32 v52, v52, v53\n"
  "ds_write_b32 v206, v52\n"
  "v_cmp_u_f32 vcc, v52, v52\n"
  "s_waitcnt lgkmcnt(0)\n"
  "s_barrier\n"
  "s_cbranch_vccnz Lc4b_sb_%=\n"
  "Lc4b_sbb_%=:\n"
  "s_waitcnt lgkmcnt(0)\n"
  "s_barrier\n"
  "ds_read_b32 v25, v24 offset:7424\n"
  "s_waitcnt lgkmcnt(0)\n"
  "v_cmp_ne_u32 vcc, 0, v25\n"
  "s_cbranch_vccnz Lc4b_rt_%=\n"
  "s_add_u32 s44, s44, 1\n"
  "s_mov_b32 s52, s45\n"
  "s_mov_b32 s45, s46\n"
  "s_mov_b32 s46, s47\n"
  "s_mov_b32 s47, s72\n"
  "s_mov_b32 s72, s52\n"
  "s_cmp_ge_u32 s44, 63\n"
  "s_cbranch_scc1 Lc4_out_%=\n"
  "s_nop 15\n"
  "s_nop 13\n"
  "buffer_load_dwordx4 v[88:91], v205, s[40:43], s48 offen offset:2560 sc1\n"
  "buffer_load_dword v66, v207, s[40:43], 0 offen offset:0 sc1\n"
  "s_add_u32 s48, s48, 2097152\n"
  "s_xor_b32 s48, s48, 0x2000\n"
  "s_branch Lc4a_nxt_%=\n"
  "Lc4_out_%=:\n"
  "s_branch Lc_end_%=\n"
  "Lc4_v0_%=:\n"
  "s_cmp_eq_u32 s53, 0\n"
  "s_cbranch_scc1 Lc4_v0b_%=\n"
  "s_sub_u32 s53, s53, 1\n"
  "buffer_load_dword v66, v207, s[40:43], 0 offen offset:0 sc1\n"
  "s_waitcnt vmcnt(0)\n"
  "v_cmp_gt_u32 vcc, s52, v66\n"
  "s_cbranch_vccnz Lc4_v0_%=\n"
  "s_branch Lc4_v0b_%=\n"
  "Lc4_v1_%=:\n"
  "s_cmp_eq_u32 s53, 0\n"
  "s_cbranch_scc1 Lc4_v1b_%=\n"
  "s_sub_u32 s53, s53, 1\n"
  "buffer_load_dword v66, v207, s[40:43], 0 offen offset:0 sc1\n"
  "s_waitcnt vmcnt(0)\n"
  "v_cmp_gt_u32 vcc, s52, v66\n"
  "s_cbranch_vccnz Lc4_v1_%=\n"
  "s_branch Lc4_v1b_%=\n"
  "Lc4a_fv_%=:\n"
  "s_cmp_eq_u32 s53, 0\n"
  "s_cbranch_scc1 Lc4a_fvb_%=\n"
  "s_sub_u32 s53, s53, 1\n"
  "buffer_load_dword v66, v207, s[40:43], 0 offen offset:0 sc1\n"
  "s_waitcnt vmcnt(0)\n"
  "v_cmp_gt_u32 vcc, s52, v66\n"
  "s_cbranch_vccnz Lc4a_fv_%=\n"
  "s_branch Lc4a_fvb_%=\n"
  "Lc4a_sb_%=:\n"
  "s_cmp_eq_u32 s53, 0\n"
  "s_cbranch_scc1 Lc4a_sbb_%=\n"
  "s_sub_u32 s53, s53, 1\n"
  "ds_write_b32 v24, v64 offset:7424\n"
  "s_branch Lc4a_sbb_%=\n"
  "Lc4a_rt_%=:\n"
  "s_mov_b32 s74, 1\n"
  "s_waitcnt lgkmcnt(0)\n"
  "s_barrier\n"
  "ds_write_b32 v24, v24 offset:7424\n"
  "s_waitcnt lgkmcnt(0)\n"
  "s_nop 7\n"
  "s_cmp_eq_u32 s50, 0\n"
  "s_cbranch_scc1 Lc4b_top_%=\n"
  "s_branch Lc4a_top_%=\n"
  "Lc4b_fv_%=:\n"
  "s_cmp_eq_u32 s53, 0\n"
  "s_cbranch_scc1 Lc4b_fvb_%=\n"
  "s_sub_u32 s53, s53, 1\n"
  "buffer_load_dword v66, v207, s[40:43], 0 offen offset:0 sc1\n"
  "s_waitcnt vmcnt(0)\n"
  "v_cmp_gt_u32 vcc, s52, v66\n"
  "s_cbranch_vccnz Lc4b_fv_%=\n"
  "s_branch Lc4b_fvb_%=\n"
  "Lc4b_sb_%=:\n"
  "s_cmp_eq_u32 s53, 0\n"
  "s_cbranch_scc1 Lc4b_sbb_%=\n"
  "s_sub_u32 s53, s53, 1\n"
  "ds_write_b32 v24, v64 offset:7424\n"
  "s_branch Lc4b_sbb_%=\n"
  "Lc4b_rt_%=:\n"
  "s_mov_b32 s74, 1\n"
  "s_waitcnt lgkmcnt(0)\n"
  "s_barrier\n"
  "ds_write_b32 v24, v24 offset:7424\n"
  "s_waitcnt lgkmcnt(0)\n"
  "s_nop 7\n"
  "s_cmp_eq_u32 s50, 0\n"
  "s_cbranch_scc1 Lc4_s2_%=\n"
  "s_branch Lc4b_top_%=\n"
  "Lc5_start_%=:\n"
  "v_mov_b32 v66, 0\n"
  "v_mov_b32 v64, -1\n"
  "v_mov_b32 v24, 0\n"
  "v_mov_b32 v44, 0\n"
  "v_mov_b32 v45, 0\n"
  "v_mov_b32 v51, 0\n"
  "v_mov_b32 v54, 2155520\n"
  "Lc5_cw_%=:\n"
  "buffer_load_dword v52, v54, s[40:43], 0 offen sc1\n"
  "s_waitcnt vmcnt(0)\n"
  "v_readfirstlane_b32 s73, v52\n"
  "s_nop 3\n"
  "s_cmp_lg_u32 s73, 0\n"
  "s_cbranch_scc1 Lc5_cg_%=\n"
  "s_sub_u32 s53, s53, 1\n"
  "s_cmp_lg_u32 s53, 0\n"
  "s_sleep 2\n"
  "s_cbranch_scc1 Lc5_cw_%=\n"
  "Lc5_cg_%=:\n"
  "s_lshl_b32 s75, s73, 12\n"
  "s_andn2_b32 s75, 0x2000, s75\n"
  "v_bfe_u32 v52, v200, 12, 2\n"
  "v_lshlrev_b32 v52, 2, v52\n"
  "v_lshrrev_b32 v52, v52, s73\n"
  "v_and_b32 v52, 7, v52\n"
  "v_and_b32 v200, 0xffff8fff, v200\n"
  "v_lshl_or_b32 v200, v52, 12, v200\n"
  "v_bfe_u32 v52, v201, 12, 2\n"
  "v_lshlrev_b32 v52, 2, v52\n"
  "v_lshrrev_b32 v52, v52, s73\n"
  "v_and_b32 v52, 7, v52\n"
  "v_and_b32 v201, 0xffff8fff, v201\n"
  "v_lshl_or_b32 v201, v52, 12, v201\n"
  "v_bfe_u32 v52, v202, 12, 2\n"
  "v_lshlrev_b32 v52, 2, v52\n"
  "v_lshrrev_b32 v52, v52, s73\n"
  "v_and_b32 v52, 7, v52\n"
  "v_and_b32 v202, 0xffff8fff, v202\n"
  "v_lshl_or_b32 v202, v52, 12, v202\n"
  "v_bfe_u32 v52, v203, 12, 2\n"
  "v_lshlrev_b32 v52, 2, v52\n"
  "v_lshrrev_b32 v52, v52, s73\n"
  "v_and_b32 v52, 7, v52\n"
  "v_and_b32 v203, 0xffff8fff, v203\n"
  "v_lshl_or_b32 v203, v52, 12, v203\n"
  "v_bfe_u32 v52, v204, 12, 2\n"
  "v_lshlrev_b32 v52, 2, v52\n"
  "v_lshrrev_b32 v52, v52, s73\n"
  "v_and_b32 v52, 7, v52\n"
  "v_and_b32 v204, 0xffff8fff, v204\n"
  "v_lshl_or_b32 v204, v52, 12, v204\n"
  "v_bfe_u32 v52, v209, 12, 2\n"
  "v_lshlrev_b32 v52, 2, v52\n"
  "v_lshrrev_b32 v52, v52, s73\n"
  "v_and_b32 v52, 7, v52\n"
  "v_and_b32 v209, 0xffff8fff, v209\n"
  "v_lshl_or_b32 v209, v52, 12, v209\n"
  "buffer_store_dword v211, v209, s[40:43], 0 offen offset:0 sc1\n"
  "s_mov_b32 s48, s99\n"
  "v_mov_b32 v66, -1\n"
  "v_mov_b32 v76, -1\n"
  "v_mov_b32 v77, -1\n"
  "v_mov_b32 v78, -1\n"
  "v_mov_b32 v79, -1\n"
  "v_mov_b32 v80, -1\n"
  "v_mov_b32 v81, -1\n"
  "v_mov_b32 v82, -1\n"
  "v_mov_b32 v83, -1\n"
  "v_mov_b32 v84, -1\n"
  "v_mov_b32 v85, -1\n"
  "v_mov_b32 v86, -1\n"
  "v_mov_b32 v87, -1\n"
  "v_mov_b32 v88, -1\n"
  "v_mov_b32 v89, -1\n"
  "v_mov_b32 v90, -1\n"
  "v_mov_b32 v91, -1\n"
  "v_mov_b32 v92, -1\n"
  "v_mov_b32 v93, -1\n"
  "v_mov_b32 v94, -1\n"
  "v_mov_b32 v95, -1\n"
  "v_mov_b32 v96, -1\n"
  "v_mov_b32 v97, -1\n"
  "v_mov_b32 v98, -1\n"
  "v_mov_b32 v99, -1\n"
  "v_mov_b32 v100, -1\n"
  "v_mov_b32 v101, -1\n"
  "v_mov_b32 v102, -1\n"
  "v_mov_b32 v103, -1\n"
  "v_mov_b32 v104, -1\n"
  "v_mov_b32 v105, -1\n"
  "v_mov_b32 v106, -1\n"
  "v_mov_b32 v107, -1\n"
  "s_branch Lc5a_top_%=\n"
  "Lc5_s2_%=:\n"
  "s_mov_b32 s48, s75\n"
  "v_mov_b32 v66, 0\n"
  "s_add_u32 s52, s44, 1\n"
  "v_cmp_gt_u32 vcc, s52, v66\n"
  "s_cbranch_vccnz Lc5_v0_%=\n"
  "Lc5_v0b_%=:\n"
  "buffer_load_dwordx4 v[76:79], v205, s[40:43], s48 offen offset:0 sc1\n"
  "buffer_load_dwordx4 v[80:83], v205, s[40:43], s48 offen offset:2048 sc1\n"
  "buffer_load_dwordx4 v[84:87], v205, s[40:43], s48 offen offset:512 sc1\n"
  "buffer_load_dwordx4 v[88:91], v205, s[40:43], s48 offen offset:2560 sc1\n"
  "s_add_u32 s48, s48, 2097152\n"
  "s_xor_b32 s48, s48, 0x2000\n"
  "s_mov_b32 s50, 3\n"
  "s_add_u32 s52, s44, 2\n"
  "v_cmp_gt_u32 vcc, s52, v66\n"
  "s_cbranch_vccnz Lc5_v1_%=\n"
  "Lc5_v1b_%=:\n"
  "Lc5a_nxt_%=:\n"
  "Lc5a_top_%=:\n"
  "v_mov_b32 v32, 0\n"
  "v_mov_b32 v33, 0\n"
  "v_mov_b32 v34, 0\n"
  "v_mov_b32 v35, 0\n"
  "v_mov_b32 v36, 0\n"
  "v_mov_b32 v37, 0\n"
  "v_mov_b32 v38, 0\n"
  "v_mov_b32 v39, 0\n"
  "Lc5a_ge_%=:\n"
  "s_barrier\n"
  "buffer_load_dwordx4 v[16:19], v202, s[40:43], s45 offen offset:0 sc1\n"
  "s_nop 7\n"
  "buffer_load_dwordx4 v[20:23], v203, s[40:43], s45 offen offset:0 sc1\n"
  "s_nop 0\n"
  "ds_read_b32 v25, v24 offset:7428\n"
  "s_nop 15\n"
  "s_nop 5\n"
  "buffer_load_dwordx4 v[8:11], v200, s[40:43], s45 offen offset:0 sc1\n"
  "s_nop 8\n"
  "buffer_load_dwordx4 v[12:15], v201, s[40:43], s45 offen offset:0 sc1\n"
  "s_nop 0\n"
  "s_waitcnt lgkmcnt(0)\n"
  "v_xor_b32 v25, 1, v25\n"
  "v_lshl_add_u32 v26, v25, 30, v209\n"
  "s_nop 2\n"
  "s_waitcnt vmcnt(4)\n"
  "s_add_u32 s52, s44, 2\n"
  "v_cmp_gt_u32 vcc, s52, v66\n"
  "s_cbranch_vccnz Lc5a_fv_%=\n"
  "Lc5a_fvb_%=:\n"
  "s_nop 5\n"
  "s_nop 1\n"
  "v_mov_b32 v28, 0\n"
  "buffer_load_dword v40, v204, s[40:43], s45 offen offset:0 sc1\n"
  "v_mov_b32 v28, 0\n"
  "buffer_load_dword v41, v204, s[40:43], s45 offen offset:64 sc1\n"
  "v_mov_b32 v28, 0\n"
  "buffer_load_dword v42, v204, s[40:43], s45 offen offset:128 sc1\n"
  "v_mov_b32 v28, 0\n"
  "buffer_load_dword v43, v204, s[40:43], s45 offen offset:192 sc1\n"
  "v_mov_b32 v28, 0\n"
  "v_mov_b32 v28, 0\n"
  "s_nop 2\n"
  "v_mov_b32 v28, 0\n"
  "s_nop 2\n"
  "v_mov_b32 v28, 0\n"
  "s_nop 2\n"
  "v_mov_b32 v28, 0\n"
  "s_nop 2\n"
  "v_mov_b32 v28, 0\n"
  "s_barrier\n"
  "v_mov_b32 v28, 0\n"
  "buffer_store_dword v64, v26, s[40:43], s46 offen sc1\n"
  "v_mov_b32 v28, 0\n"
  "buffer_load_dwordx4 v[92:95], v205, s[40:43], s48 offen offset:0 sc1\n"
  "v_mov_b32 v28, 0\n"
  "s_nop 2\n"
  "v_mov_b32 v28, 0\n"
  "s_nop 0\n"
  "v_mov_b32 v28, 0\n"
  "buffer_load_dwordx4 v[96:99], v205, s[40:43], s48 offen offset:2048 sc1\n"
  "v_mov_b32 v28, 0\n"
  "s_nop 2\n"
  "v_mov_b32 v28, 0\n"
  "s_nop 2\n"
  "v_mov_b32 v28, 0\n"
  "buffer_load_dwordx4 v[100:103], v205, s[40:43], s48 offen offset:512 sc1\n"
  "v_mov_b32 v28, 0\n"
  "s_nop 2\n"
  "v_mov_b32 v28, 0\n"
  "s_nop 2\n"
  "v_mov_b32 v28, 0\n"
  "s_waitcnt vmcnt(10)\n"
  "v_max3_f32 v58, v16, v17, v18\n"
  "v_max3_f32 v58, v58, v19, v20\n"
  "v_max3_f32 v58, v58, v21, v22\n"
  "v_max_f32 v58, v58, v23\n"
  "v_pk_add_f32 v[0:1], v[16:17], v[58:59] op_sel_hi:[1,0] neg_lo:[0,1] neg_hi:[0,1]\n"
  "v_pk_add_f32 v[2:3], v[18:19], v[58:59] op_sel_hi:[1,0] neg_lo:[0,1] neg_hi:[0,1]\n"
  "v_pk_add_f32 v[4:5], v[20:21], v[58:59] op_sel_hi:[1,0] neg_lo:[0,1] neg_hi:[0,1]\n"
  "v_pk_add_f32 v[6:7], v[22:23], v[58:59] op_sel_hi:[1,0] neg_lo:[0,1] neg_hi:[0,1]\n"
  "v_exp_f32 v0, v0\n"
  "v_exp_f32 v1, v1\n"
  "v_exp_f32 v2, v2\n"
  "v_exp_f32 v3, v3\n"
  "v_exp_f32 v4, v4\n"
  "v_exp_f32 v5, v5\n"
  "v_exp_f32 v6, v6\n"
  "v_exp_f32 v7, v7\n"
  "v_pk_add_f32 v[52:53], v[0:1], v[2:3]\n"
  "v_pk_add_f32 v[54:55], v[4:5], v[6:7]\n"
  "v_pk_add_f32 v[52:53], v[52:53], v[54:55]\n"
  "v_add_f32 v59, v52, v53\n"
  "s_waitcnt vmcnt(9)\n"
  "v_add_f32_dpp v52, v10, v8 row_ror:8 row_mask:0xf bank_mask:0xf\n"
  "v_add_f32_dpp v53, v11, v9 row_ror:8 row_mask:0xf bank_mask:0xf\n"
  "s_nop 0\n"
  "v_mov_b32 v28, 0\n"
  "s_waitcnt vmcnt(8)\n"
  "v_add_f32_dpp v54, v14, v12 row_ror:8 row_mask:0xf bank_mask:0xf\n"
  "v_add_f32_dpp v55, v15, v13 row_ror:8 row_mask:0xf bank_mask:0xf\n"
  "v_pk_add_f32 v[52:53], v[52:53], v[54:55]\n"
  "v_max3_f32 v56, v52, v53, v58\n"
  "s_nop 1\n"
  "v_max_f32_dpp v56, v56, v56 quad_perm:[1,0,3,2] row_mask:0xf bank_mask:0xf\n"
  "s_nop 1\n"
  "v_max_f32_dpp v56, v56, v56 quad_perm:[2,3,0,1] row_mask:0xf bank_mask:0xf\n"
  "v_sub_f32 v54, v58, v56\n"
  "v_pk_add_f32 v[52:53], v[52:53], v[56:57] op_sel_hi:[1,0] neg_lo:[0,1] neg_hi:[0,1]\n"
  "v_exp_f32 v54, v54\n"
  "v_exp_f32 v52, v52\n"
  "v_exp_f32 v53, v53\n"
  "v_fma_f32 v56, v54, v59, v52\n"
  "v_add_f32 v56, v56, v53\n"
  "s_nop 1\n"
  "v_add_f32_dpp v56, v56, v56 quad_perm:[1,0,3,2] row_mask:0xf bank_mask:0xf\n"
  "s_nop 1\n"
  "v_add_f32_dpp v56, v56, v56 quad_perm:[2,3,0,1] row_mask:0xf bank_mask:0xf\n"
  "v_rcp_f32 v56, v56\n"
  "s_nop 0\n"
  "v_mul_f32 v54, v54, v56\n"
  "v_pk_mul_f32 v[0:1], v[0:1], v[54:55] op_sel_hi:[1,0]\n"
  "v_pk_mul_f32 v[2:3], v[2:3], v[54:55] op_sel_hi:[1,0]\n"
  "v_pk_mul_f32 v[4:5], v[4:5], v[54:55] op_sel_hi:[1,0]\n"
  "v_pk_mul_f32 v[6:7], v[6:7], v[54:55] op_sel_hi:[1,0]\n"
  "v_cvt_pk_bf16_f32 v46, v0, v1\n"
  "v_cvt_pk_bf16_f32 v48, v2, v3\n"
  "v_cvt_pk_bf16_f32 v47, v4, v5\n"
  "v_cvt_pk_bf16_f32 v49, v6, v7\n"
  "v_pk_mul_f32 v[52:53], v[52:53], v[56:57] op_sel_hi:[1,0]\n"
  "v_cvt_pk_bf16_f32 v50, v52, v53\n"
  "v_smfmac_f32_16x16x64_bf16 v[32:35], v[46:49], v[76:83], v208\n"
  "v_smfmac_f32_16x16x64_bf16 v[36:39], v[46:49], v[84:91], v208\n"
  "s_waitcnt vmcnt(4)\n"
  "v_smfmac_f32_16x16x32_bf16 v[32:35], v[50:51], v[40:43], v208\n"
  "v_smfmac_f32_16x16x32_bf16 v[36:39], v[50:51], v[42:45], v208\n"
  "s_nop 3\n"
  "v_add_f32 v52, v32, v33\n"
  "s_nop 2\n"
  "v_add_f32 v53, v38, v39\n"
  "v_add_f32 v52, v52, v53\n"
  "ds_write_b32 v206, v52\n"
  "v_cmp_u_f32 vcc, v52, v52\n"
  "s_waitcnt lgkmcnt(0)\n"
  "s_barrier\n"
  "ds_read_b128 v[0:3], v210\n"
  "ds_read_b128 v[4:7], v210 offset:512\n"
  "s_waitcnt lgkmcnt(1)\n"
  "v_pk_add_f32 v[52:53], v[0:1], v[2:3]\n"
  "s_waitcnt lgkmcnt(0)\n"
  "v_pk_add_f32 v[54:55], v[4:5], v[6:7]\n"
  "v_pk_add_f32 v[52:53], v[52:53], v[54:55]\n"
  "v_cvt_pk_bf16_f32 v54, v52, v53\n"
  "buffer_store_dword v54, v209, s[40:43], s46 offen offset:0 sc1\n"
  "s_cbranch_vccnz Lc5a_sb_%=\n"
  "Lc5a_sbb_%=:\n"
  "s_waitcnt lgkmcnt(0)\n"
  "s_barrier\n"
  "ds_read_b32 v25, v24 offset:7424\n"
  "s_waitcnt lgkmcnt(0)\n"
  "v_cmp_ne_u32 vcc, 0, v25\n"
  "s_cbranch_vccnz Lc5a_rt_%=\n"
  "s_add_u32 s44, s44, 1\n"
  "s_mov_b32 s52, s45\n"
  "s_mov_b32 s45, s46\n"
  "s_mov_b32 s46, s47\n"
  "s_mov_b32 s47, s72\n"
  "s_mov_b32 s72, s52\n"
  "s_cmp_ge_u32 s44, 63\n"
  "s_cbranch_scc1 Lc5_out_%=\n"
  "s_nop 15\n"
  "s_nop 13\n"
  "buffer_load_dwordx4 v[104:107], v205, s[40:43], s48 offen offset:2560 sc1\n"
  "buffer_load_dword v66, v207, s[40:43], 0 offen offset:0 sc1\n"
  "s_add_u32 s48, s48, 2097152\n"
  "s_xor_b32 s48, s48, 0x2000\n"
  "buffer_store_dword v64, v209, s[40:43], s47 offen offset:0 sc1\n"
  "Lc5b_nxt_%=:\n"
  "Lc5b_top_%=:\n"
  "v_mov_b32 v32, 0\n"
  "v_mov_b32 v33, 0\n"
  "v_mov_b32 v34, 0\n"
  "v_mov_b32 v35, 0\n"
  "v_mov_b32 v36, 0\n"
  "v_mov_b32 v37, 0\n"
  "v_mov_b32 v38, 0\n"
  "v_mov_b32 v39, 0\n"
  "Lc5b_ge_%=:\n"
  "s_barrier\n"
  "buffer_load_dwordx4 v[16:19], v202, s[40:43], s45 offen offset:0 sc1\n"
  "s_nop 7\n"
  "buffer_load_dwordx4 v[20:23], v203, s[40:43], s45 offen offset:0 sc1\n"
  "s_nop 0\n"
  "ds_read_b32 v25, v24 offset:7428\n"
  "s_nop 15\n"
  "s_nop 5\n"
  "buffer_load_dwordx4 v[8:11], v200, s[40:43], s45 offen offset:0 sc1\n"
  "s_nop 8\n"
  "buffer_load_dwordx4 v[12:15], v201, s[40:43], s45 offen offset:0 sc1\n"
  "s_nop 0\n"
  "s_waitcnt lgkmcnt(0)\n"
  "v_xor_b32 v25, 1, v25\n"
  "v_lshl_add_u32 v26, v25, 30, v209\n"
  "s_nop 2\n"
  "s_waitcnt vmcnt(4)\n"
  "s_add_u32 s52, s44, 2\n"
  "v_cmp_gt_u32 vcc, s52, v66\n"
  "s_cbranch_vccnz Lc5b_fv_%=\n"
  "Lc5b_fvb_%=:\n"
  "s_nop 5\n"
  "s_nop 1\n"
  "v_mov_b32 v28, 0\n"
  "buffer_load_dword v40, v204, s[40:43], s45 offen offset:0 sc1\n"
  "v_mov_b32 v28, 0\n"
  "buffer_load_dword v41, v204, s[40:43], s45 offen offset:64 sc1\n"
  "v_mov_b32 v28, 0\n"
  "buffer_load_dword v42, v204, s[40:43], s45 offen offset:128 sc1\n"
  "v_mov_b32 v28, 0\n"
  "buffer_load_dword v43, v204, s[40:43], s45 offen offset:192 sc1\n"
  "v_mov_b32 v28, 0\n"
  "v_mov_b32 v28, 0\n"
  "s_nop 2\n"
  "v_mov_b32 v28, 0\n"
  "s_nop 2\n"
  "v_mov_b32 v28, 0\n"
  "s_nop 2\n"
  "v_mov_b32 v28, 0\n"
  "s_nop 2\n"
  "v_mov_b32 v28, 0\n"
  "s_barrier\n"
  "v_mov_b32 v28, 0\n"
  "buffer_store_dword v64, v26, s[40:43], s46 offen sc1\n"
  "v_mov_b32 v28, 0\n"
  "buffer_load_dwordx4 v[76:79], v205, s[40:43], s48 offen offset:0 sc1\n"
  "v_mov_b32 v28, 0\n"
  "s_nop 2\n"
  "v_mov_b32 v28, 0\n"
  "s_nop 0\n"
  "v_mov_b32 v28, 0\n"
  "buffer_load_dwordx4 v[80:83], v205, s[40:43], s48 offen offset:2048 sc1\n"
  "v_mov_b32 v28, 0\n"
  "s_nop 2\n"
  "v_mov_b32 v28, 0\n"
  "s_nop 2\n"
  "v_mov_b32 v28, 0\n"
  "buffer_load_dwordx4 v[84:87], v205, s[40:43], s48 offen offset:512 sc1\n"
  "v_mov_b32 v28, 0\n"
  "s_nop 2\n"
  "v_mov_b32 v28, 0\n"
  "s_nop 2\n"
  "v_mov_b32 v28, 0\n"
  "s_waitcnt vmcnt(10)\n"
  "v_max3_f32 v58, v16, v17, v18\n"
  "v_max3_f32 v58, v58, v19, v20\n"
  "v_max3_f32 v58, v58, v21, v22\n"
  "v_max_f32 v58, v58, v23\n"
  "v_pk_add_f32 v[0:1], v[16:17], v[58:59] op_sel_hi:[1,0] neg_lo:[0,1] neg_hi:[0,1]\n"
  "v_pk_add_f32 v[2:3], v[18:19], v[58:59] op_sel_hi:[1,0] neg_lo:[0,1] neg_hi:[0,1]\n"
  "v_pk_add_f32 v[4:5], v[20:21], v[58:59] op_sel_hi:[1,0] neg_lo:[0,1] neg_hi:[0,1]\n"
  "v_pk_add_f32 v[6:7], v[22:23], v[58:59] op_sel_hi:[1,0] neg_lo:[0,1] neg_hi:[0,1]\n"
  "v_exp_f32 v0, v0\n"
  "v_exp_f32 v1, v1\n"
  "v_exp_f32 v2, v2\n"
  "v_exp_f32 v3, v3\n"
  "v_exp_f32 v4, v4\n"
  "v_exp_f32 v5, v5\n"
  "v_exp_f32 v6, v6\n"
  "v_exp_f32 v7, v7\n"
  "v_pk_add_f32 v[52:53], v[0:1], v[2:3]\n"
  "v_pk_add_f32 v[54:55], v[4:5], v[6:7]\n"
  "v_pk_add_f32 v[52:53], v[52:53], v[54:55]\n"
  "v_add_f32 v59, v52, v53\n"
  "s_waitcnt vmcnt(9)\n"
  "v_add_f32_dpp v52, v10, v8 row_ror:8 row_mask:0xf bank_mask:0xf\n"
  "v_add_f32_dpp v53, v11, v9 row_ror:8 row_mask:0xf bank_mask:0xf\n"
  "s_nop 0\n"
  "v_mov_b32 v28, 0\n"
  "s_waitcnt vmcnt(8)\n"
  "v_add_f32_dpp v54, v14, v12 row_ror:8 row_mask:0xf bank_mask:0xf\n"
  "v_add_f32_dpp v55, v15, v13 row_ror:8 row_mask:0xf bank_mask:0xf\n"
  "v_pk_add_f32 v[52:53], v[52:53], v[54:55]\n"
  "v_max3_f32 v56, v52, v53, v58\n"
  "s_nop 1\n"
  "v_max_f32_dpp v56, v56, v56 quad_perm:[1,0,3,2] row_mask:0xf bank_mask:0xf\n"
  "s_nop 1\n"
  "v_max_f32_dpp v56, v56, v56 quad_perm:[2,3,0,1] row_mask:0xf bank_mask:0xf\n"
  "v_sub_f32 v54, v58, v56\n"
  "v_pk_add_f32 v[52:53], v[52:53], v[56:57] op_sel_hi:[1,0] neg_lo:[0,1] neg_hi:[0,1]\n"
  "v_exp_f32 v54, v54\n"
  "v_exp_f32 v52, v52\n"
  "v_exp_f32 v53, v53\n"
  "v_fma_f32 v56, v54, v59, v52\n"
  "v_add_f32 v56, v56, v53\n"
  "s_nop 1\n"
  "v_add_f32_dpp v56, v56, v56 quad_perm:[1,0,3,2] row_mask:0xf bank_mask:0xf\n"
  "s_nop 1\n"
  "v_add_f32_dpp v56, v56, v56 quad_perm:[2,3,0,1] row_mask:0xf bank_mask:0xf\n"
  "v_rcp_f32 v56, v56\n"
  "s_nop 0\n"
  "v_mul_f32 v54, v54, v56\n"
  "v_pk_mul_f32 v[0:1], v[0:1], v[54:55] op_sel_hi:[1,0]\n"
  "v_pk_mul_f32 v[2:3], v[2:3], v[54:55] op_sel_hi:[1,0]\n"
  "v_pk_mul_f32 v[4:5], v[4:5], v[54:55] op_sel_hi:[1,0]\n"
  "v_pk_mul_f32 v[6:7], v[6:7], v[54:55] op_sel_hi:[1,0]\n"
  "v_cvt_pk_bf16_f32 v46, v0, v1\n"
  "v_cvt_pk_bf16_f32 v48, v2, v3\n"
  "v_cvt_pk_bf16_f32 v47, v4, v5\n"
  "v_cvt_pk_bf16_f32 v49, v6, v7\n"
  "v_pk_mul_f32 v[52:53], v[52:53], v[56:57] op_sel_hi:[1,0]\n"
  "v_cvt_pk_bf16_f32 v50, v52, v53\n"
  "v_smfmac_f32_16x16x64_bf16 v[32:35], v[46:49], v[92:99], v208\n"
  "v_smfmac_f32_16x16x64_bf16 v[36:39], v[46:49], v[100:107], v208\n"
  "s_waitcnt vmcnt(4)\n"
  "v_smfmac_f32_16x16x32_bf16 v[32:35], v[50:51], v[40:43], v208\n"
  "v_smfmac_f32_16x16x32_bf16 v[36:39], v[50:51], v[42:45], v208\n"
  "s_nop 3\n"
  "v_add_f32 v52, v32, v33\n"
  "s_nop 2\n"
  "v_add_f32 v53, v38, v39\n"
  "v_add_f32 v52, v52, v53\n"
  "ds_write_b32 v206, v52\n"
  "v_cmp_u_f32 vcc, v52, v52\n"
  "s_waitcnt lgkmcnt(0)\n"
  "s_barrier\n"
  "ds_read_b128 v[0:3], v210\n"
  "ds_read_b128 v[4:7], v210 offset:512\n"
  "s_waitcnt lgkmcnt(1)\n"
  "v_pk_add_f32 v[52:53], v[0:1], v[2:3]\n"
  "s_waitcnt lgkmcnt(0)\n"
  "v_pk_add_f32 v[54:55], v[4:5], v[6:7]\n"
  "v_pk_add_f32 v[52:53], v[52:53], v[54:55]\n"
  "v_cvt_pk_bf16_f32 v54, v52, v53\n"
  "buffer_store_dword v54, v209, s[40:43], s46 offen offset:0 sc1\n"
  "s_cbranch_vccnz Lc5b_sb_%=\n"
  "Lc5b_sbb_%=:\n"
  "s_waitcnt lgkmcnt(0)\n"
  "s_barrier\n"
  "ds_read_b32 v25, v24 offset:7424\n"
  "s_waitcnt lgkmcnt(0)\n"
  "v_cmp_ne_u32 vcc, 0, v25\n"
  "s_cbranch_vccnz Lc5b_rt_%=\n"
  "s_add_u32 s44, s44, 1\n"
  "s_mov_b32 s52, s45\n"
  "s_mov_b32 s45, s46\n"
  "s_mov_b32 s46, s47\n"
  "s_mov_b32 s47, s72\n"
  "s_mov_b32 s72, s52\n"
  "s_cmp_ge_u32 s44, 63\n"
  "s_cbranch_scc1 Lc5_out_%=\n"
  "s_nop 15\n"
  "s_nop 13\n"
  "buffer_load_dwordx4 v[88:91], v205, s[40:43], s48 offen offset:2560 sc1\n"
  "buffer_load_dword v66, v207, s[40:43], 0 offen offset:0 sc1\n"
  "s_add_u32 s48, s48, 2097152\n"
  "s_xor_b32 s48, s48, 0x2000\n"
  "buffer_store_dword v64, v209, s[40:43], s47 offen offset:0 sc1\n"
  "s_branch Lc5a_nxt_%=\n"
  "Lc5_out_%=:\n"
  "s_branch Lc_end_%=\n"
  "Lc5_v0_%=:\n"
  "s_cmp_eq_u32 s53, 0\n"
  "s_cbranch_scc1 Lc5_v0b_%=\n"
  "s_sub_u32 s53, s53, 1\n"
  "buffer_load_dword v66, v207, s[40:43], 0 offen offset:0 sc1\n"
  "s_waitcnt vmcnt(0)\n"
  "v_cmp_gt_u32 vcc, s52, v66\n"
  "s_cbranch_vccnz Lc5_v0_%=\n"
  "s_branch Lc5_v0b_%=\n"
  "Lc5_v1_%=:\n"
  "s_cmp_eq_u32 s53, 0\n"
  "s_cbranch_scc1 Lc5_v1b_%=\n"
  "s_sub_u32 s53, s53, 1\n"
  "buffer_load_dword v66, v207, s[40:43], 0 offen offset:0 sc1\n"
  "s_waitcnt vmcnt(0)\n"
  "v_cmp_gt_u32 vcc, s52, v66\n"
  "s_cbranch_vccnz Lc5_v1_%=\n"
  "s_branch Lc5_v1b_%=\n"
  "Lc5a_fv_%=:\n"
  "s_cmp_eq_u32 s53, 0\n"
  "s_cbranch_scc1 Lc5a_fvb_%=\n"
  "s_sub_u32 s53, s53, 1\n"
  "buffer_load_dword v66, v207, s[40:43], 0 offen offset:0 sc1\n"
  "s_waitcnt vmcnt(0)\n"
  "v_cmp_gt_u32 vcc, s52, v66\n"
  "s_cbranch_vccnz Lc5a_fv_%=\n"
  "s_branch Lc5a_fvb_%=\n"
  "Lc5a_sb_%=:\n"
  "s_cmp_eq_u32 s53, 0\n"
  "s_cbranch_scc1 Lc5a_sbb_%=\n"
  "s_sub_u32 s53, s53, 1\n"
  "ds_write_b32 v24, v64 offset:7424\n"
  "s_branch Lc5a_sbb_%=\n"
  "Lc5a_rt_%=:\n"
  "s_mov_b32 s74, 1\n"
  "s_waitcnt lgkmcnt(0)\n"
  "s_barrier\n"
  "ds_write_b32 v24, v24 offset:7424\n"
  "s_waitcnt lgkmcnt(0)\n"
  "s_nop 7\n"
  "s_cmp_eq_u32 s50, 0\n"
  "s_cbranch_scc1 Lc5b_top_%=\n"
  "s_branch Lc5a_top_%=\n"
  "Lc5b_fv_%=:\n"
  "s_cmp_eq_u32 s53, 0\n"
  "s_cbranch_scc1 Lc5b_fvb_%=\n"
  "s_sub_u32 s53, s53, 1\n"
  "buffer_load_dword v66, v207, s[40:43], 0 offen offset:0 sc1\n"
  "s_waitcnt vmcnt(0)\n"
  "v_cmp_gt_u32 vcc, s52, v66\n"
  "s_cbranch_vccnz Lc5b_fv_%=\n"
  "s_branch Lc5b_fvb_%=\n"
  "Lc5b_sb_%=:\n"
  "s_cmp_eq_u32 s53, 0\n"
  "s_cbranch_scc1 Lc5b_sbb_%=\n"
  "s_sub_u32 s53, s53, 1\n"
  "ds_write_b32 v24, v64 offset:7424\n"
  "s_branch Lc5b_sbb_%=\n"
  "Lc5b_rt_%=:\n"
  "s_mov_b32 s74, 1\n"
  "s_waitcnt lgkmcnt(0)\n"
  "s_barrier\n"
  "ds_write_b32 v24, v24 offset:7424\n"
  "s_waitcnt lgkmcnt(0)\n"
  "s_nop 7\n"
  "s_cmp_eq_u32 s50, 0\n"
  "s_cbranch_scc1 Lc5_s2_%=\n"
  "s_branch Lc5b_top_%=\n"
  "Lc6_start_%=:\n"
  "v_mov_b32 v66, 0\n"
  "v_mov_b32 v64, -1\n"
  "v_mov_b32 v24, 0\n"
  "v_mov_b32 v44, 0\n"
  "v_mov_b32 v45, 0\n"
  "v_mov_b32 v51, 0\n"
  "v_mov_b32 v54, 2155520\n"
  "Lc6_cw_%=:\n"
  "buffer_load_dword v52, v54, s[40:43], 0 offen sc1\n"
  "s_waitcnt vmcnt(0)\n"
  "v_readfirstlane_b32 s73, v52\n"
  "s_nop 3\n"
  "s_cmp_lg_u32 s73, 0\n"
  "s_cbranch_scc1 Lc6_cg_%=\n"
  "s_sub_u32 s53, s53, 1\n"
  "s_cmp_lg_u32 s53, 0\n"
  "s_sleep 2\n"
  "s_cbranch_scc1 Lc6_cw_%=\n"
  "Lc6_cg_%=:\n"
  "s_lshl_b32 s75, s73, 12\n"
  "s_andn2_b32 s75, 0x2000, s75\n"
  "v_bfe_u32 v52, v200, 12, 2\n"
  "v_lshlrev_b32 v52, 2, v52\n"
  "v_lshrrev_b32 v52, v52, s73\n"
  "v_and_b32 v52, 7, v52\n"
  "v_and_b32 v200, 0xffff8fff, v200\n"
  "v_lshl_or_b32 v200, v52, 12, v200\n"
  "v_bfe_u32 v52, v201, 12, 2\n"
  "v_lshlrev_b32 v52, 2, v52\n"
  "v_lshrrev_b32 v52, v52, s73\n"
  "v_and_b32 v52, 7, v52\n"
  "v_and_b32 v201, 0xffff8fff, v201\n"
  "v_lshl_or_b32 v201, v52, 12, v201\n"
  "v_bfe_u32 v52, v202, 12, 2\n"
  "v_lshlrev_b32 v52, 2, v52\n"
  "v_lshrrev_b32 v52, v52, s73\n"
  "v_and_b32 v52, 7, v52\n"
  "v_and_b32 v202, 0xffff8fff, v202\n"
  "v_lshl_or_b32 v202, v52, 12, v202\n"
  "v_bfe_u32 v52, v203, 12, 2\n"
  "v_lshlrev_b32 v52, 2, v52\n"
  "v_lshrrev_b32 v52, v52, s73\n"
  "v_and_b32 v52, 7, v52\n"
  "v_and_b32 v203, 0xffff8fff, v203\n"
  "v_lshl_or_b32 v203, v52, 12, v203\n"
  "v_bfe_u32 v52, v204, 12, 2\n"
  "v_lshlrev_b32 v52, 2, v52\n"
  "v_lshrrev_b32 v52, v52, s73\n"
  "v_and_b32 v52, 7, v52\n"
  "v_and_b32 v204, 0xffff8fff, v204\n"
  "v_lshl_or_b32 v204, v52, 12, v204\n"
  "v_bfe_u32 v52, v209, 12, 2\n"
  "v_lshlrev_b32 v52, 2, v52\n"
  "v_lshrrev_b32 v52, v52, s73\n"
  "v_and_b32 v52, 7, v52\n"
  "v_and_b32 v209, 0xffff8fff, v209\n"
  "v_lshl_or_b32 v209, v52, 12, v209\n"
  "s_mov_b32 s48, s99\n"
  "v_mov_b32 v66, -1\n"
  "v_mov_b32 v76, -1\n"
  "v_mov_b32 v77, -1\n"
  "v_mov_b32 v78, -1\n"
  "v_mov_b32 v79, -1\n"
  "v_mov_b32 v80, -1\n"
  "v_mov_b32 v81, -1\n"
  "v_mov_b32 v82, -1\n"
  "v_mov_b32 v83, -1\n"
  "v_mov_b32 v84, -1\n"
  "v_mov_b32 v85, -1\n"
  "v_mov_b32 v86, -1\n"
  "v_mov_b32 v87, -1\n"
  "v_mov_b32 v88, -1\n"
  "v_mov_b32 v89, -1\n"
  "v_mov_b32 v90, -1\n"
  "v_mov_b32 v91, -1\n"
  "v_mov_b32 v92, -1\n"
  "v_mov_b32 v93, -1\n"
  "v_mov_b32 v94, -1\n"
  "v_mov_b32 v95, -1\n"
  "v_mov_b32 v96, -1\n"
  "v_mov_b32 v97, -1\n"
  "v_mov_b32 v98, -1\n"
  "v_mov_b32 v99, -1\n"
  "v_mov_b32 v100, -1\n"
  "v_mov_b32 v101, -1\n"
  "v_mov_b32 v102, -1\n"
  "v_mov_b32 v103, -1\n"
  "v_mov_b32 v104, -1\n"
  "v_mov_b32 v105, -1\n"
  "v_mov_b32 v106, -1\n"
  "v_mov_b32 v107, -1\n"
  "s_branch Lc6a_top_%=\n"
  "Lc6_s2_%=:\n"
  "s_mov_b32 s48, s75\n"
  "v_mov_b32 v66, 0\n"
  "s_add_u32 s52, s44, 1\n"
  "v_cmp_gt_u32 vcc, s52, v66\n"
  "s_cbranch_vccnz Lc6_v0_%=\n"
  "Lc6_v0b_%=:\n"
  "buffer_load_dwordx4 v[76:79], v205, s[40:43], s48 offen offset:0 sc1\n"
  "buffer_load_dwordx4 v[80:83], v205, s[40:43], s48 offen offset:2048 sc1\n"
  "buffer_load_dwordx4 v[84:87], v205, s[40:43], s48 offen offset:512 sc1\n"
  "buffer_load_dwordx4 v[88:91], v205, s[40:43], s48 offen offset:2560 sc1\n"
  "s_add_u32 s48, s48, 2097152\n"
  "s_xor_b32 s48, s48, 0x2000\n"
  "v_mov_b32 v54, 2155776\n"
  "Lc6_sw_%=:\n"
  "buffer_load_dwordx2 v[52:53], v54, s[40:43], 0 offen sc1\n"
  "s_waitcnt vmcnt(0)\n"
  "v_readfirstlane_b32 s71, v52\n"
  "v_readfirstlane_b32 s73, v53\n"
  "s_nop 3\n"
  "s_min_u32 s52, s73, s71\n"
  "s_cmp_lg_u32 s52, 0\n"
  "s_cbranch_scc1 Lc6_sg_%=\n"
  "s_cmp_eq_u32 s53, 0\n"
  "s_cbranch_scc1 Lc6_sf_%=\n"
  "s_sub_u32 s53, s53, 1\n"
  "s_branch Lc6_sw_%=\n"
  "Lc6_sf_%=:\n"
  "s_memrealtime s[76:77]\n"
  "s_waitcnt lgkmcnt(0)\n"
  "s_mov_b32 s71, s76\n"
  "s_mov_b32 s73, 0x4080\n"
  "Lc6_sg_%=:\n"
  "s_and_b32 s66, s73, 0xff\n"
  "s_sub_u32 s71, s71, s66\n"
  "s_lshr_b32 s73, s73, 8\n"
  "s_sub_u32 s71, s71, s66\n"
  "s_add_u32 s71, s71, s73\n"
  "s_mov_b32 s50, 3\n"
  "s_add_u32 s52, s44, 2\n"
  "v_cmp_gt_u32 vcc, s52, v66\n"
  "s_cbranch_vccnz Lc6_v1_%=\n"
  "Lc6_v1b_%=:\n"
  "Lc6a_nxt_%=:\n"
  "Lc6a_top_%=:\n"
  "v_mov_b32 v32, 0\n"
  "v_mov_b32 v33, 0\n"
  "v_mov_b32 v34, 0\n"
  "v_mov_b32 v35, 0\n"
  "v_mov_b32 v36, 0\n"
  "v_mov_b32 v37, 0\n"
  "v_mov_b32 v38, 0\n"
  "v_mov_b32 v39, 0\n"
  "s_cmp_lg_u32 s50, 3\n"
  "s_cbranch_scc1 Lc6a_gx_%=\n"
  "Lc6a_gn_%=:\n"
  "s_add_u32 s71, s71, s66\n"
  "s_memrealtime s[76:77]\n"
  "s_waitcnt lgkmcnt(0)\n"
  "s_sub_i32 s73, s71, s76\n"
  "s_cmp_lt_i32 s73, 4\n"
  "s_cbranch_scc1 Lc6a_gs_%=\n"
  "v_mov_b32 v25, s74\n"
  "s_mov_b32 s74, 0\n"
  "ds_write_b32 v24, v25 offset:7428\n"
  "Lc6a_gl_%=:\n"
  "s_memrealtime s[76:77]\n"
  "s_waitcnt lgkmcnt(0)\n"
  "s_sub_i32 s73, s71, s76\n"
  "s_cmp_gt_i32 s73, 3\n"
  "s_cbranch_scc1 Lc6a_gl_%=\n"
  "s_nop 13\n"
  "s_memrealtime s[78:79]\n"
  "s_nop 0\n"
  "s_memrealtime s[80:81]\n"
  "s_nop 0\n"
  "s_memrealtime s[82:83]\n"
  "s_nop 0\n"
  "s_memrealtime s[84:85]\n"
  "s_nop 0\n"
  "s_memrealtime s[86:87]\n"
  "s_nop 0\n"
  "s_memrealtime s[88:89]\n"
  "s_nop 0\n"
  "s_memrealtime s[90:91]\n"
  "s_nop 0\n"
  "s_memrealtime s[92:93]\n"
  "s_nop 0\n"
  "s_memrealtime s[94:95]\n"
  "s_nop 0\n"
  "s_memrealtime s[96:97]\n"
  "s_nop 0\n"
  "s_sub_i32 s73, s73, 0\n"
  "s_max_i32 s73, s73, 0\n"
  "s_mul_i32 s73, s73, 3\n"
  "s_add_u32 s76, s76, 8\n"
  "s_waitcnt lgkmcnt(0)\n"
  "s_cmp_lg_u32 s78, s76\n"
  "s_addc_u32 s73, s73, 0\n"
  "s_cmp_lg_u32 s80, s76\n"
  "s_addc_u32 s73, s73, 0\n"
  "s_cmp_lg_u32 s82, s76\n"
  "s_addc_u32 s73, s73, 0\n"
  "s_cmp_lg_u32 s84, s76\n"
  "s_addc_u32 s73, s73, 0\n"
  "s_cmp_lg_u32 s86, s76\n"
  "s_addc_u32 s73, s73, 0\n"
  "s_cmp_lg_u32 s88, s76\n"
  "s_addc_u32 s73, s73, 0\n"
  "s_cmp_lg_u32 s90, s76\n"
  "s_addc_u32 s73, s73, 0\n"
  "s_cmp_lg_u32 s92, s76\n"
  "s_addc_u32 s73, s73, 0\n"
  "s_cmp_lg_u32 s94, s76\n"
  "s_addc_u32 s73, s73, 0\n"
  "s_cmp_lg_u32 s96, s76\n"
  "s_addc_u32 s73, s73, 0\n"
  "s_lshl_b32 s73, s73, 2\n"
  "s_sub_u32 s73, 88, s73\n"
  "s_getpc_b64 s[78:79]\n"
  "s_add_u32 s78, s78, s73\n"
  "s_addc_u32 s79, s79, 0\n"
  "s_setpc_b64 s[78:79]\n"
  "s_nop 1\n"
  "s_nop 1\n"
  "s_nop 1\n"
  "s_nop 1\n"
  "s_nop 1\n"
  "s_nop 1\n"
  "s_nop 1\n"
  "s_nop 1\n"
  "s_nop 1\n"
  "s_nop 1\n"
  "s_nop 1\n"
  "s_nop 1\n"
  "s_nop 1\n"
  "s_nop 1\n"
  "s_nop 1\n"
  "s_nop 1\n"
  "s_nop 1\n"
  "s_nop 1\n"
  "s_nop 1\n"
  "Lc6a_ge_%=:\n"
  "s_barrier\n"
  "buffer_load_dwordx4 v[16:19], v202, s[40:43], s45 offen offset:0 sc1\n"
  "s_nop 7\n"
  "buffer_load_dwordx4 v[20:23], v203, s[40:43], s45 offen offset:0 sc1\n"
  "s_nop 15\n"
  "s_nop 7\n"
  "buffer_load_dwordx4 v[8:11], v200, s[40:43], s45 offen offset:0 sc1\n"
  "s_nop 8\n"
  "buffer_load_dwordx4 v[12:15], v201, s[40:43], s45 offen offset:0 sc1\n"
  "s_nop 6\n"
  "s_waitcnt vmcnt(4)\n"
  "s_add_u32 s52, s44, 2\n"
  "v_cmp_gt_u32 vcc, s52, v66\n"
  "s_cbranch_vccnz Lc6a_fv_%=\n"
  "Lc6a_fvb_%=:\n"
  "s_nop 5\n"
  "s_nop 2\n"
  "v_mov_b32 v28, 0\n"
  "s_nop 2\n"
  "v_mov_b32 v28, 0\n"
  "s_nop 2\n"
  "v_mov_b32 v28, 0\n"
  "s_nop 2\n"
  "v_mov_b32 v28, 0\n"
  "s_nop 1\n"
  "v_mov_b32 v28, 0\n"
  "buffer_load_dword v40, v204, s[40:43], s45 offen offset:0 sc1\n"
  "v_mov_b32 v28, 0\n"
  "buffer_load_dword v41, v204, s[40:43], s45 offen offset:64 sc1\n"
  "v_mov_b32 v28, 0\n"
  "buffer_load_dword v42, v204, s[40:43], s45 offen offset:128 sc1\n"
  "v_mov_b32 v28, 0\n"
  "buffer_load_dword v43, v204, s[40:43], s45 offen offset:192 sc1\n"
  "v_mov_b32 v28, 0\n"
  "v_mov_b32 v28, 0\n"
  "s_barrier\n"
  "v_mov_b32 v28, 0\n"
  "s_nop 2\n"
  "v_mov_b32 v28, 0\n"
  "s_nop 0\n"
  "v_mov_b32 v28, 0\n"
  "buffer_load_dwordx4 v[92:95], v205, s[40:43], s48 offen offset:0 sc1\n"
  "v_mov_b32 v28, 0\n"
  "s_nop 2\n"
  "v_mov_b32 v28, 0\n"
  "s_nop 1\n"
  "v_mov_b32 v28, 0\n"
  "buffer_load_dwordx4 v[96:99], v205, s[40:43], s48 offen offset:2048 sc1\n"
  "v_mov_b32 v28, 0\n"
  "s_nop 2\n"
  "v_mov_b32 v28, 0\n"
  "s_nop 2\n"
  "v_mov_b32 v28, 0\n"
  "buffer_load_dwordx4 v[100:103], v205, s[40:43], s48 offen offset:512 sc1\n"
  "v_mov_b32 v28, 0\n"
  "s_nop 2\n"
  "v_mov_b32 v28, 0\n"
  "s_nop 0\n"
  "v_mov_b32 v28, 0\n"
  "s_waitcnt vmcnt(9)\n"
  "v_max3_f32 v58, v16, v17, v18\n"
  "v_max3_f32 v58, v58, v19, v20\n"
  "v_max3_f32 v58, v58, v21, v22\n"
  "v_max_f32 v58, v58, v23\n"
  "v_pk_add_f32 v[0:1], v[16:17], v[58:59] op_sel_hi:[1,0] neg_lo:[0,1] neg_hi:[0,1]\n"
  "v_pk_add_f32 v[2:3], v[18:19], v[58:59] op_sel_hi:[1,0] neg_lo:[0,1] neg_hi:[0,1]\n"
  "v_pk_add_f32 v[4:5], v[20:21], v[58:59] op_sel_hi:[1,0] neg_lo:[0,1] neg_hi:[0,1]\n"
  "v_pk_add_f32 v[6:7], v[22:23], v[58:59] op_sel_hi:[1,0] neg_lo:[0,1] neg_hi:[0,1]\n"
  "v_exp_f32 v0, v0\n"
  "v_exp_f32 v1, v1\n"
  "v_exp_f32 v2, v2\n"
  "v_exp_f32 v3, v3\n"
  "v_exp_f32 v4, v4\n"
  "v_exp_f32 v5, v5\n"
  "v_exp_f32 v6, v6\n"
  "v_exp_f32 v7, v7\n"
  "v_pk_add_f32 v[52:53], v[0:1], v[2:3]\n"
  "v_pk_add_f32 v[54:55], v[4:5], v[6:7]\n"
  "v_pk_add_f32 v[52:53], v[52:53], v[54:55]\n"
  "v_add_f32 v59, v52, v53\n"
  "s_waitcnt vmcnt(8)\n"
  "v_add_f32_dpp v52, v10, v8 row_ror:8 row_mask:0xf bank_mask:0xf\n"
  "v_add_f32_dpp v53, v11, v9 row_ror:8 row_mask:0xf bank_mask:0xf\n"
  "s_nop 0\n"
  "v_mov_b32 v28, 0\n"
  "s_waitcnt vmcnt(7)\n"
  "v_add_f32_dpp v54, v14, v12 row_ror:8 row_mask:0xf bank_mask:0xf\n"
  "v_add_f32_dpp v55, v15, v13 row_ror:8 row_mask:0xf bank_mask:0xf\n"
  "v_pk_add_f32 v[52:53], v[52:53], v[54:55]\n"
  "v_max3_f32 v56, v52, v53, v58\n"
  "s_nop 1\n"
  "v_max_f32_dpp v56, v56, v56 quad_perm:[1,0,3,2] row_mask:0xf bank_mask:0xf\n"
  "s_nop 1\n"
  "v_max_f32_dpp v56, v56, v56 quad_perm:[2,3,0,1] row_mask:0xf bank_mask:0xf\n"
  "v_sub_f32 v54, v58, v56\n"
  "v_pk_add_f32 v[52:53], v[52:53], v[56:57] op_sel_hi:[1,0] neg_lo:[0,1] neg_hi:[0,1]\n"
  "v_exp_f32 v54, v54\n"
  "v_exp_f32 v52, v52\n"
  "v_exp_f32 v53, v53\n"
  "v_fma_f32 v56, v54, v59, v52\n"
  "v_add_f32 v56, v56, v53\n"
  "s_nop 1\n"
  "v_add_f32_dpp v56, v56, v56 quad_perm:[1,0,3,2] row_mask:0xf bank_mask:0xf\n"
  "s_nop 1\n"
  "v_add_f32_dpp v56, v56, v56 quad_perm:[2,3,0,1] row_mask:0xf bank_mask:0xf\n"
  "v_rcp_f32 v56, v56\n"
  "s_nop 0\n"
  "v_mul_f32 v54, v54, v56\n"
  "v_pk_mul_f32 v[0:1], v[0:1], v[54:55] op_sel_hi:[1,0]\n"
  "v_pk_mul_f32 v[2:3], v[2:3], v[54:55] op_sel_hi:[1,0]\n"
  "v_pk_mul_f32 v[4:5], v[4:5], v[54:55] op_sel_hi:[1,0]\n"
  "v_pk_mul_f32 v[6:7], v[6:7], v[54:55] op_sel_hi:[1,0]\n"
  "v_cvt_pk_bf16_f32 v46, v0, v1\n"
  "v_cvt_pk_bf16_f32 v48, v2, v3\n"
  "v_cvt_pk_bf16_f32 v47, v4, v5\n"
  "v_cvt_pk_bf16_f32 v49, v6, v7\n"
  "v_pk_mul_f32 v[52:53], v[52:53], v[56:57] op_sel_hi:[1,0]\n"
  "v_cvt_pk_bf16_f32 v50, v52, v53\n"
  "v_smfmac_f32_16x16x64_bf16 v[32:35], v[46:49], v[76:83], v208\n"
  "v_smfmac_f32_16x16x64_bf16 v[36:39], v[46:49], v[84:91], v208\n"
  "s_waitcnt vmcnt(3)\n"
  "v_smfmac_f32_16x16x32_bf16 v[32:35], v[50:51], v[40:43], v208\n"
  "v_smfmac_f32_16x16x32_bf16 v[36:39], v[50:51], v[42:45], v208\n"
  "s_nop 3\n"
  "v_add_f32 v52, v32, v33\n"
  "s_nop 2\n"
  "v_add_f32 v53, v38, v39\n"
  "v_add_f32 v52, v52, v53\n"
  "ds_write_b32 v206, v52\n"
  "v_cmp_u_f32 vcc, v52, v52\n"
  "s_waitcnt lgkmcnt(0)\n"
  "s_barrier\n"
  "s_cbranch_vccnz Lc6a_sb_%=\n"
  "Lc6a_sbb_%=:\n"
  "s_waitcnt lgkmcnt(0)\n"
  "s_barrier\n"
  "ds_read_b32 v25, v24 offset:7424\n"
  "s_waitcnt lgkmcnt(0)\n"
  "v_cmp_ne_u32 vcc, 0, v25\n"
  "s_cbranch_vccnz Lc6a_rt_%=\n"
  "s_add_u32 s44, s44, 1\n"
  "s_mov_b32 s52, s45\n"
  "s_mov_b32 s45, s46\n"
  "s_mov_b32 s46, s47\n"
  "s_mov_b32 s47, s72\n"
  "s_mov_b32 s72, s52\n"
  "s_cmp_ge_u32 s44, 63\n"
  "s_cbranch_scc1 Lc6_out_%=\n"
  "s_nop 15\n"
  "s_nop 13\n"
  "buffer_load_dwordx4 v[104:107], v205, s[40:43], s48 offen offset:2560 sc1\n"
  "buffer_load_dword v66, v207, s[40:43], 0 offen offset:0 sc1\n"
  "s_add_u32 s48, s48, 2097152\n"
  "s_xor_b32 s48, s48, 0x2000\n"
  "Lc6b_nxt_%=:\n"
  "Lc6b_top_%=:\n"
  "v_mov_b32 v32, 0\n"
  "v_mov_b32 v33, 0\n"
  "v_mov_b32 v34, 0\n"
  "v_mov_b32 v35, 0\n"
  "v_mov_b32 v36, 0\n"
  "v_mov_b32 v37, 0\n"
  "v_mov_b32 v38, 0\n"
  "v_mov_b32 v39, 0\n"
  "s_cmp_lg_u32 s50, 3\n"
  "s_cbranch_scc1 Lc6b_gx_%=\n"
  "Lc6b_gn_%=:\n"
  "s_add_u32 s71, s71, s66\n"
  "s_memrealtime s[76:77]\n"
  "s_waitcnt lgkmcnt(0)\n"
  "s_sub_i32 s73, s71, s76\n"
  "s_cmp_lt_i32 s73, 4\n"
  "s_cbranch_scc1 Lc6b_gs_%=\n"
  "v_mov_b32 v25, s74\n"
  "s_mov_b32 s74, 0\n"
  "ds_write_b32 v24, v25 offset:7428\n"
  "Lc6b_gl_%=:\n"
  "s_memrealtime s[76:77]\n"
  "s_waitcnt lgkmcnt(0)\n"
  "s_sub_i32 s73, s71, s76\n"
  "s_cmp_gt_i32 s73, 3\n"
  "s_cbranch_scc1 Lc6b_gl_%=\n"
  "s_nop 13\n"
  "s_memrealtime s[78:79]\n"
  "s_nop 0\n"
  "s_memrealtime s[80:81]\n"
  "s_nop 0\n"
  "s_memrealtime s[82:83]\n"
  "s_nop 0\n"
  "s_memrealtime s[84:85]\n"
  "s_nop 0\n"
  "s_memrealtime s[86:87]\n"
  "s_nop 0\n"
  "s_memrealtime s[88:89]\n"
  "s_nop 0\n"
  "s_memrealtime s[90:91]\n"
  "s_nop 0\n"
  "s_memrealtime s[92:93]\n"
  "s_nop 0\n"
  "s_memrealtime s[94:95]\n"
  "s_nop 0\n"
  "s_memrealtime s[96:97]\n"
  "s_nop 0\n"
  "s_sub_i32 s73, s73, 0\n"
  "s_max_i32 s73, s73, 0\n"
  "s_mul_i32 s73, s73, 3\n"
  "s_add_u32 s76, s76, 8\n"
  "s_waitcnt lgkmcnt(0)\n"
  "s_cmp_lg_u32 s78, s76\n"
  "s_addc_u32 s73, s73, 0\n"
  "s_cmp_lg_u32 s80, s76\n"
  "s_addc_u32 s73, s73, 0\n"
  "s_cmp_lg_u32 s82, s76\n"
  "s_addc_u32 s73, s73, 0\n"
  "s_cmp_lg_u32 s84, s76\n"
  "s_addc_u32 s73, s73, 0\n"
  "s_cmp_lg_u32 s86, s76\n"
  "s_addc_u32 s73, s73, 0\n"
  "s_cmp_lg_u32 s88, s76\n"
  "s_addc_u32 s73, s73, 0\n"
  "s_cmp_lg_u32 s90, s76\n"
  "s_addc_u32 s73, s73, 0\n"
  "s_cmp_lg_u32 s92, s76\n"
  "s_addc_u32 s73, s73, 0\n"
  "s_cmp_lg_u32 s94, s76\n"
  "s_addc_u32 s73, s73, 0\n"
  "s_cmp_lg_u32 s96, s76\n"
  "s_addc_u32 s73, s73, 0\n"
  "s_lshl_b32 s73, s73, 2\n"
  "s_sub_u32 s73, 88, s73\n"
  "s_getpc_b64 s[78:79]\n"
  "s_add_u32 s78, s78, s73\n"
  "s_addc_u32 s79, s79, 0\n"
  "s_setpc_b64 s[78:79]\n"
  "s_nop 1\n"
  "s_nop 1\n"
  "s_nop 1\n"
  "s_nop 1\n"
  "s_nop 1\n"
  "s_nop 1\n"
  "s_nop 1\n"
  "s_nop 1\n"
  "s_nop 1\n"
  "s_nop 1\n"
  "s_nop 1\n"
  "s_nop 1\n"
  "s_nop 1\n"
  "s_nop 1\n"
  "s_nop 1\n"
  "s_nop 1\n"
  "s_nop 1\n"
  "s_nop 1\n"
  "s_nop 1\n"
  "Lc6b_ge_%=:\n"
  "s_barrier\n"
  "buffer_load_dwordx4 v[16:19], v202, s[40:43], s45 offen offset:0 sc1\n"
  "s_nop 7\n"
  "buffer_load_dwordx4 v[20:23], v203, s[40:43], s45 offen offset:0 sc1\n"
  "s_nop 15\n"
  "s_nop 7\n"
  "buffer_load_dwordx4 v[8:11], v200, s[40:43], s45 offen offset:0 sc1\n"
  "s_nop 8\n"
  "buffer_load_dwordx4 v[12:15], v201, s[40:43], s45 offen offset:0 sc1\n"
  "s_nop 6\n"
  "s_waitcnt vmcnt(4)\n"
  "s_add_u32 s52, s44, 2\n"
  "v_cmp_gt_u32 vcc, s52, v66\n"
  "s_cbranch_vccnz Lc6b_fv_%=\n"
  "Lc6b_fvb_%=:\n"
  "s_nop 5\n"
  "s_nop 2\n"
  "v_mov_b32 v28, 0\n"
  "s_nop 2\n"
  "v_mov_b32 v28, 0\n"
  "s_nop 2\n"
  "v_mov_b32 v28, 0\n"
  "s_nop 2\n"
  "v_mov_b32 v28, 0\n"
  "s_nop 1\n"
  "v_mov_b32 v28, 0\n"
  "buffer_load_dword v40, v204, s[40:43], s45 offen offset:0 sc1\n"
  "v_mov_b32 v28, 0\n"
  "buffer_load_dword v41, v204, s[40:43], s45 offen offset:64 sc1\n"
  "v_mov_b32 v28, 0\n"
  "buffer_load_dword v42, v204, s[40:43], s45 offen offset:128 sc1\n"
  "v_mov_b32 v28, 0\n"
  "buffer_load_dword v43, v204, s[40:43], s45 offen offset:192 sc1\n"
  "v_mov_b32 v28, 0\n"
  "v_mov_b32 v28, 0\n"
  "s_barrier\n"
  "v_mov_b32 v28, 0\n"
  "s_nop 2\n"
  "v_mov_b32 v28, 0\n"
  "s_nop 0\n"
  "v_mov_b32 v28, 0\n"
  "buffer_load_dwordx4 v[76:79], v205, s[40:43], s48 offen offset:0 sc1\n"
  "v_mov_b32 v28, 0\n"
  "s_nop 2\n"
  "v_mov_b32 v28, 0\n"
  "s_nop 1\n"
  "v_mov_b32 v28, 0\n"
  "buffer_load_dwordx4 v[80:83], v205, s[40:43], s48 offen offset:2048 sc1\n"
  "v_mov_b32 v28, 0\n"
  "s_nop 2\n"
  "v_mov_b32 v28, 0\n"
  "s_nop 2\n"
  "v_mov_b32 v28, 0\n"
  "buffer_load_dwordx4 v[84:87], v205, s[40:43], s48 offen offset:512 sc1\n"
  "v_mov_b32 v28, 0\n"
  "s_nop 2\n"
  "v_mov_b32 v28, 0\n"
  "s_nop 0\n"
  "v_mov_b32 v28, 0\n"
  "s_waitcnt vmcnt(9)\n"
  "v_max3_f32 v58, v16, v17, v18\n"
  "v_max3_f32 v58, v58, v19, v20\n"
  "v_max3_f32 v58, v58, v21, v22\n"
  "v_max_f32 v58, v58, v23\n"
  "v_pk_add_f32 v[0:1], v[16:17], v[58:59] op_sel_hi:[1,0] neg_lo:[0,1] neg_hi:[0,1]\n"
  "v_pk_add_f32 v[2:3], v[18:19], v[58:59] op_sel_hi:[1,0] neg_lo:[0,1] neg_hi:[0,1]\n"
  "v_pk_add_f32 v[4:5], v[20:21], v[58:59] op_sel_hi:[1,0] neg_lo:[0,1] neg_hi:[0,1]\n"
  "v_pk_add_f32 v[6:7], v[22:23], v[58:59] op_sel_hi:[1,0] neg_lo:[0,1] neg_hi:[0,1]\n"
  "v_exp_f32 v0, v0\n"
  "v_exp_f32 v1, v1\n"
  "v_exp_f32 v2, v2\n"
  "v_exp_f32 v3, v3\n"
  "v_exp_f32 v4, v4\n"
  "v_exp_f32 v5, v5\n"
  "v_exp_f32 v6, v6\n"
  "v_exp_f32 v7, v7\n"
  "v_pk_add_f32 v[52:53], v[0:1], v[2:3]\n"
  "v_pk_add_f32 v[54:55], v[4:5], v[6:7]\n"
  "v_pk_add_f32 v[52:53], v[52:53], v[54:55]\n"
  "v_add_f32 v59, v52, v53\n"
  "s_waitcnt vmcnt(8)\n"
  "v_add_f32_dpp v52, v10, v8 row_ror:8 row_mask:0xf bank_mask:0xf\n"
  "v_add_f32_dpp v53, v11, v9 row_ror:8 row_mask:0xf bank_mask:0xf\n"
  "s_nop 0\n"
  "v_mov_b32 v28, 0\n"
  "s_waitcnt vmcnt(7)\n"
  "v_add_f32_dpp v54, v14, v12 row_ror:8 row_mask:0xf bank_mask:0xf\n"
  "v_add_f32_dpp v55, v15, v13 row_ror:8 row_mask:0xf bank_mask:0xf\n"
  "v_pk_add_f32 v[52:53], v[52:53], v[54:55]\n"
  "v_max3_f32 v56, v52, v53, v58\n"
  "s_nop 1\n"
  "v_max_f32_dpp v56, v56, v56 quad_perm:[1,0,3,2] row_mask:0xf bank_mask:0xf\n"
  "s_nop 1\n"
  "v_max_f32_dpp v56, v56, v56 quad_perm:[2,3,0,1] row_mask:0xf bank_mask:0xf\n"
  "v_sub_f32 v54, v58, v56\n"
  "v_pk_add_f32 v[52:53], v[52:53], v[56:57] op_sel_hi:[1,0] neg_lo:[0,1] neg_hi:[0,1]\n"
  "v_exp_f32 v54, v54\n"
  "v_exp_f32 v52, v52\n"
  "v_exp_f32 v53, v53\n"
  "v_fma_f32 v56, v54, v59, v52\n"
  "v_add_f32 v56, v56, v53\n"
  "s_nop 1\n"
  "v_add_f32_dpp v56, v56, v56 quad_perm:[1,0,3,2] row_mask:0xf bank_mask:0xf\n"
  "s_nop 1\n"
  "v_add_f32_dpp v56, v56, v56 quad_perm:[2,3,0,1] row_mask:0xf bank_mask:0xf\n"
  "v_rcp_f32 v56, v56\n"
  "s_nop 0\n"
  "v_mul_f32 v54, v54, v56\n"
  "v_pk_mul_f32 v[0:1], v[0:1], v[54:55] op_sel_hi:[1,0]\n"
  "v_pk_mul_f32 v[2:3], v[2:3], v[54:55] op_sel_hi:[1,0]\n"
  "v_pk_mul_f32 v[4:5], v[4:5], v[54:55] op_sel_hi:[1,0]\n"
  "v_pk_mul_f32 v[6:7], v[6:7], v[54:55] op_sel_hi:[1,0]\n"
  "v_cvt_pk_bf16_f32 v46, v0, v1\n"
  "v_cvt_pk_bf16_f32 v48, v2, v3\n"
  "v_cvt_pk_bf16_f32 v47, v4, v5\n"
  "v_cvt_pk_bf16_f32 v49, v6, v7\n"
  "v_pk_mul_f32 v[52:53], v[52:53], v[56:57] op_sel_hi:[1,0]\n"
  "v_cvt_pk_bf16_f32 v50, v52, v53\n"
  "v_smfmac_f32_16x16x64_bf16 v[32:35], v[46:49], v[92:99], v208\n"
  "v_smfmac_f32_16x16x64_bf16 v[36:39], v[46:49], v[100:107], v208\n"
  "s_waitcnt vmcnt(3)\n"
  "v_smfmac_f32_16x16x32_bf16 v[32:35], v[50:51], v[40:43], v208\n"
  "v_smfmac_f32_16x16x32_bf16 v[36:39], v[50:51], v[42:45], v208\n"
  "s_nop 3\n"
  "v_add_f32 v52, v32, v33\n"
  "s_nop 2\n"
  "v_add_f32 v53, v38, v39\n"
  "v_add_f32 v52, v52, v53\n"
  "ds_write_b32 v206, v52\n"
  "v_cmp_u_f32 vcc, v52, v52\n"
  "s_waitcnt lgkmcnt(0)\n"
  "s_barrier\n"
  "s_cbranch_vccnz Lc6b_sb_%=\n"
  "Lc6b_sbb_%=:\n"
  "s_waitcnt lgkmcnt(0)\n"
  "s_barrier\n"
  "ds_read_b32 v25, v24 offset:7424\n"
  "s_waitcnt lgkmcnt(0)\n"
  "v_cmp_ne_u32 vcc, 0, v25\n"
  "s_cbranch_vccnz Lc6b_rt_%=\n"
  "s_add_u32 s44, s44, 1\n"
  "s_mov_b32 s52, s45\n"
  "s_mov_b32 s45, s46\n"
  "s_mov_b32 s46, s47\n"
  "s_mov_b32 s47, s72\n"
  "s_mov_b32 s72, s52\n"
  "s_cmp_ge_u32 s44, 63\n"
  "s_cbranch_scc1 Lc6_out_%=\n"
  "s_nop 15\n"
  "s_nop 13\n"
  "buffer_load_dwordx4 v[88:91], v205, s[40:43], s48 offen offset:2560 sc1\n"
  "buffer_load_dword v66, v207, s[40:43], 0 offen offset:0 sc1\n"
  "s_add_u32 s48, s48, 2097152\n"
  "s_xor_b32 s48, s48, 0x2000\n"
  "s_branch Lc6a_nxt_%=\n"
  "Lc6_out_%=:\n"
  "s_branch Lc_end_%=\n"
  "Lc6_v0_%=:\n"
  "s_cmp_eq_u32 s53, 0\n"
  "s_cbranch_scc1 Lc6_v0b_%=\n"
  "s_sub_u32 s53, s53, 1\n"
  "buffer_load_dword v66, v207, s[40:43], 0 offen offset:0 sc1\n"
  "s_waitcnt vmcnt(0)\n"
  "v_cmp_gt_u32 vcc, s52, v66\n"
  "s_cbranch_vccnz Lc6_v0_%=\n"
  "s_branch Lc6_v0b_%=\n"
  "Lc6_v1_%=:\n"
  "s_cmp_eq_u32 s53, 0\n"
  "s_cbranch_scc1 Lc6_v1b_%=\n"
  "s_sub_u32 s53, s53, 1\n"
  "buffer_load_dword v66, v207, s[40:43], 0 offen offset:0 sc1\n"
  "s_waitcnt vmcnt(0)\n"
  "v_cmp_gt_u32 vcc, s52, v66\n"
  "s_cbranch_vccnz Lc6_v1_%=\n"
  "s_branch Lc6_v1b_%=\n"
  "Lc6a_gx_%=:\n"
  "s_memrealtime s[76:77]\n"
  "s_waitcnt lgkmcnt(0)\n"
  "s_mov_b32 s71, s76\n"
  "s_branch Lc6a_gn_%=\n"
  "Lc6a_gs_%=:\n"
  "s_mov_b32 s74, 1\n"
  "s_branch Lc6a_gn_%=\n"
  "Lc6a_fv_%=:\n"
  "s_cmp_eq_u32 s53, 0\n"
  "s_cbranch_scc1 Lc6a_fvb_%=\n"
  "s_sub_u32 s53, s53, 1\n"
  "buffer_load_dword v66, v207, s[40:43], 0 offen offset:0 sc1\n"
  "s_waitcnt vmcnt(0)\n"
  "v_cmp_gt_u32 vcc, s52, v66\n"
  "s_cbranch_vccnz Lc6a_fv_%=\n"
  "s_branch Lc6a_fvb_%=\n"
  "Lc6a_sb_%=:\n"
  "s_cmp_eq_u32 s53, 0\n"
  "s_cbranch_scc1 Lc6a_sbb_%=\n"
  "s_sub_u32 s53, s53, 1\n"
  "ds_write_b32 v24, v64 offset:7424\n"
  "s_branch Lc6a_sbb_%=\n"
  "Lc6a_rt_%=:\n"
  "s_mov_b32 s74, 1\n"
  "s_waitcnt lgkmcnt(0)\n"
  "s_barrier\n"
  "ds_write_b32 v24, v24 offset:7424\n"
  "s_waitcnt lgkmcnt(0)\n"
  "s_nop 7\n"
  "s_cmp_eq_u32 s50, 0\n"
  "s_cbranch_scc1 Lc6b_top_%=\n"
  "s_branch Lc6a_top_%=\n"
  "Lc6b_gx_%=:\n"
  "s_memrealtime s[76:77]\n"
  "s_waitcnt lgkmcnt(0)\n"
  "s_mov_b32 s71, s76\n"
  "s_branch Lc6b_gn_%=\n"
  "Lc6b_gs_%=:\n"
  "s_mov_b32 s74, 1\n"
  "s_branch Lc6b_gn_%=\n"
  "Lc6b_fv_%=:\n"
  "s_cmp_eq_u32 s53, 0\n"
  "s_cbranch_scc1 Lc6b_fvb_%=\n"
  "s_sub_u32 s53, s53, 1\n"
  "buffer_load_dword v66, v207, s[40:43], 0 offen offset:0 sc1\n"
  "s_waitcnt vmcnt(0)\n"
  "v_cmp_gt_u32 vcc, s52, v66\n"
  "s_cbranch_vccnz Lc6b_fv_%=\n"
  "s_branch Lc6b_fvb_%=\n"
  "Lc6b_sb_%=:\n"
  "s_cmp_eq_u32 s53, 0\n"
  "s_cbranch_scc1 Lc6b_sbb_%=\n"
  "s_sub_u32 s53, s53, 1\n"
  "ds_write_b32 v24, v64 offset:7424\n"
  "s_branch Lc6b_sbb_%=\n"
  "Lc6b_rt_%=:\n"
  "s_mov_b32 s74, 1\n"
  "s_waitcnt lgkmcnt(0)\n"
  "s_barrier\n"
  "ds_write_b32 v24, v24 offset:7424\n"
  "s_waitcnt lgkmcnt(0)\n"
  "s_nop 7\n"
  "s_cmp_eq_u32 s50, 0\n"
  "s_cbranch_scc1 Lc6_s2_%=\n"
  "s_branch Lc6b_top_%=\n"
  "Lc7_start_%=:\n"
  "v_mov_b32 v66, 0\n"
  "v_mov_b32 v64, -1\n"
  "v_mov_b32 v24, 0\n"
  "v_mov_b32 v44, 0\n"
  "v_mov_b32 v45, 0\n"
  "v_mov_b32 v51, 0\n"
  "v_mov_b32 v54, 2155520\n"
  "Lc7_cw_%=:\n"
  "buffer_load_dword v52, v54, s[40:43], 0 offen sc1\n"
  "s_waitcnt vmcnt(0)\n"
  "v_readfirstlane_b32 s73, v52\n"
  "s_nop 3\n"
  "s_cmp_lg_u32 s73, 0\n"
  "s_cbranch_scc1 Lc7_cg_%=\n"
  "s_sub_u32 s53, s53, 1\n"
  "s_cmp_lg_u32 s53, 0\n"
  "s_sleep 2\n"
  "s_cbranch_scc1 Lc7_cw_%=\n"
  "Lc7_cg_%=:\n"
  "s_lshl_b32 s75, s73, 12\n"
  "s_andn2_b32 s75, 0x2000, s75\n"
  "v_bfe_u32 v52, v200, 12, 2\n"
  "v_lshlrev_b32 v52, 2, v52\n"
  "v_lshrrev_b32 v52, v52, s73\n"
  "v_and_b32 v52, 7, v52\n"
  "v_and_b32 v200, 0xffff8fff, v200\n"
  "v_lshl_or_b32 v200, v52, 12, v200\n"
  "v_bfe_u32 v52, v201, 12, 2\n"
  "v_lshlrev_b32 v52, 2, v52\n"
  "v_lshrrev_b32 v52, v52, s73\n"
  "v_and_b32 v52, 7, v52\n"
  "v_and_b32 v201, 0xffff8fff, v201\n"
  "v_lshl_or_b32 v201, v52, 12, v201\n"
  "v_bfe_u32 v52, v202, 12, 2\n"
  "v_lshlrev_b32 v52, 2, v52\n"
  "v_lshrrev_b32 v52, v52, s73\n"
  "v_and_b32 v52, 7, v52\n"
  "v_and_b32 v202, 0xffff8fff, v202\n"
  "v_lshl_or_b32 v202, v52, 12, v202\n"
  "v_bfe_u32 v52, v203, 12, 2\n"
  "v_lshlrev_b32 v52, 2, v52\n"
  "v_lshrrev_b32 v52, v52, s73\n"
  "v_and_b32 v52, 7, v52\n"
  "v_and_b32 v203, 0xffff8fff, v203\n"
  "v_lshl_or_b32 v203, v52, 12, v203\n"
  "v_bfe_u32 v52, v204, 12, 2\n"
  "v_lshlrev_b32 v52, 2, v52\n"
  "v_lshrrev_b32 v52, v52, s73\n"
  "v_and_b32 v52, 7, v52\n"
  "v_and_b32 v204, 0xffff8fff, v204\n"
  "v_lshl_or_b32 v204, v52, 12, v204\n"
  "v_bfe_u32 v52, v209, 12, 2\n"
  "v_lshlrev_b32 v52, 2, v52\n"
  "v_lshrrev_b32 v52, v52, s73\n"
  "v_and_b32 v52, 7, v52\n"
  "v_and_b32 v209, 0xffff8fff, v209\n"
  "v_lshl_or_b32 v209, v52, 12, v209\n"
  "s_mov_b32 s48, s99\n"
  "v_mov_b32 v66, -1\n"
  "v_mov_b32 v76, -1\n"
  "v_mov_b32 v77, -1\n"
  "v_mov_b32 v78, -1\n"
  "v_mov_b32 v79, -1\n"
  "v_mov_b32 v80, -1\n"
  "v_mov_b32 v81, -1\n"
  "v_mov_b32 v82, -1\n"
  "v_mov_b32 v83, -1\n"
  "v_mov_b32 v84, -1\n"
  "v_mov_b32 v85, -1\n"
  "v_mov_b32 v86, -1\n"
  "v_mov_b32 v87, -1\n"
  "v_mov_b32 v88, -1\n"
  "v_mov_b32 v89, -1\n"
  "v_mov_b32 v90, -1\n"
  "v_mov_b32 v91, -1\n"
  "v_mov_b32 v92, -1\n"
  "v_mov_b32 v93, -1\n"
  "v_mov_b32 v94, -1\n"
  "v_mov_b32 v95, -1\n"
  "v_mov_b32 v96, -1\n"
  "v_mov_b32 v97, -1\n"
  "v_mov_b32 v98, -1\n"
  "v_mov_b32 v99, -1\n"
  "v_mov_b32 v100, -1\n"
  "v_mov_b32 v101, -1\n"
  "v_mov_b32 v102, -1\n"
  "v_mov_b32 v103, -1\n"
  "v_mov_b32 v104, -1\n"
  "v_mov_b32 v105, -1\n"
  "v_mov_b32 v106, -1\n"
  "v_mov_b32 v107, -1\n"
  "s_branch Lc7a_top_%=\n"
  "Lc7_s2_%=:\n"
  "s_mov_b32 s48, s75\n"
  "v_mov_b32 v66, 0\n"
  "s_add_u32 s52, s44, 1\n"
  "v_cmp_gt_u32 vcc, s52, v66\n"
  "s_cbranch_vccnz Lc7_v0_%=\n"
  "Lc7_v0b_%=:\n"
  "buffer_load_dwordx4 v[76:79], v205, s[40:43], s48 offen offset:0 sc1\n"
  "buffer_load_dwordx4 v[80:83], v205, s[40:43], s48 offen offset:2048 sc1\n"
  "buffer_load_dwordx4 v[84:87], v205, s[40:43], s48 offen offset:512 sc1\n"
  "buffer_load_dwordx4 v[88:91], v205, s[40:43], s48 offen offset:2560 sc1\n"
  "s_add_u32 s48, s48, 2097152\n"
  "s_xor_b32 s48, s48, 0x2000\n"
  "s_mov_b32 s50, 3\n"
  "s_add_u32 s52, s44, 2\n"
  "v_cmp_gt_u32 vcc, s52, v66\n"
  "s_cbranch_vccnz Lc7_v1_%=\n"
  "Lc7_v1b_%=:\n"
  "Lc7a_nxt_%=:\n"
  "Lc7a_top_%=:\n"
  "v_mov_b32 v32, 0\n"
  "v_mov_b32 v33, 0\n"
  "v_mov_b32 v34, 0\n"
  "v_mov_b32 v35, 0\n"
  "v_mov_b32 v36, 0\n"
  "v_mov_b32 v37, 0\n"
  "v_mov_b32 v38, 0\n"
  "v_mov_b32 v39, 0\n"
  "Lc7a_ge_%=:\n"
  "s_barrier\n"
  "buffer_load_dwordx4 v[16:19], v202, s[40:43], s45 offen offset:0 sc1\n"
  "s_nop 7\n"
  "buffer_load_dwordx4 v[20:23], v203, s[40:43], s45 offen offset:0 sc1\n"
  "s_nop 15\n"
  "s_nop 7\n"
  "buffer_load_dwordx4 v[8:11], v200, s[40:43], s45 offen offset:0 sc1\n"
  "s_nop 8\n"
  "buffer_load_dwordx4 v[12:15], v201, s[40:43], s45 offen offset:0 sc1\n"
  "s_nop 6\n"
  "s_waitcnt vmcnt(4)\n"
  "s_add_u32 s52, s44, 2\n"
  "v_cmp_gt_u32 vcc, s52, v66\n"
  "s_cbranch_vccnz Lc7a_fv_%=\n"
  "Lc7a_fvb_%=:\n"
  "s_nop 5\n"
  "s_nop 1\n"
  "v_mov_b32 v28, 0\n"
  "buffer_load_dword v40, v204, s[40:43], s45 offen offset:0 sc1\n"
  "v_mov_b32 v28, 0\n"
  "buffer_load_dword v41, v204, s[40:43], s45 offen offset:64 sc1\n"
  "v_mov_b32 v28, 0\n"
  "buffer_load_dword v42, v204, s[40:43], s45 offen offset:128 sc1\n"
  "v_mov_b32 v28, 0\n"
  "buffer_load_dword v43, v204, s[40:43], s45 offen offset:192 sc1\n"
  "v_mov_b32 v28, 0\n"
  "v_mov_b32 v28, 0\n"
  "s_nop 2\n"
  "v_mov_b32 v28, 0\n"
  "s_nop 2\n"
  "v_mov_b32 v28, 0\n"
  "s_nop 2\n"
  "v_mov_b32 v28, 0\n"
  "s_nop 2\n"
  "v_mov_b32 v28, 0\n"
  "s_barrier\n"
  "v_mov_b32 v28, 0\n"
  "s_nop 2\n"
  "v_mov_b32 v28, 0\n"
  "s_nop 2\n"
  "v_mov_b32 v28, 0\n"
  "buffer_load_dwordx4 v[92:95], v205, s[40:43], s48 offen offset:0 sc1\n"
  "v_mov_b32 v28, 0\n"
  "s_nop 2\n"
  "v_mov_b32 v28, 0\n"
  "s_nop 2\n"
  "v_mov_b32 v28, 0\n"
  "buffer_load_dwordx4 v[96:99], v205, s[40:43], s48 offen offset:2048 sc1\n"
  "v_mov_b32 v28, 0\n"
  "s_nop 2\n"
  "v_mov_b32 v28, 0\n"
  "s_nop 2\n"
  "v_mov_b32 v28, 0\n"
  "buffer_load_dwordx4 v[100:103], v205, s[40:43], s48 offen offset:512 sc1\n"
  "v_mov_b32 v28, 0\n"
  "s_nop 1\n"
  "v_mov_b32 v28, 0\n"
  "s_waitcnt vmcnt(9)\n"
  "v_max3_f32 v58, v16, v17, v18\n"
  "v_max3_f32 v58, v58, v19, v20\n"
  "v_max3_f32 v58, v58, v21, v22\n"
  "v_max_f32 v58, v58, v23\n"
  "v_pk_add_f32 v[0:1], v[16:17], v[58:59] op_sel_hi:[1,0] neg_lo:[0,1] neg_hi:[0,1]\n"
  "v_pk_add_f32 v[2:3], v[18:19], v[58:59] op_sel_hi:[1,0] neg_lo:[0,1] neg_hi:[0,1]\n"
  "v_pk_add_f32 v[4:5], v[20:21], v[58:59] op_sel_hi:[1,0] neg_lo:[0,1] neg_hi:[0,1]\n"
  "v_pk_add_f32 v[6:7], v[22:23], v[58:59] op_sel_hi:[1,0] neg_lo:[0,1] neg_hi:[0,1]\n"
  "v_exp_f32 v0, v0\n"
  "v_exp_f32 v1, v1\n"
  "v_exp_f32 v2, v2\n"
  "v_exp_f32 v3, v3\n"
  "v_exp_f32 v4, v4\n"
  "v_exp_f32 v5, v5\n"
  "v_exp_f32 v6, v6\n"
  "v_exp_f32 v7, v7\n"
  "v_pk_add_f32 v[52:53], v[0:1], v[2:3]\n"
  "v_pk_add_f32 v[54:55], v[4:5], v[6:7]\n"
  "v_pk_add_f32 v[52:53], v[52:53], v[54:55]\n"
  "v_add_f32 v59, v52, v53\n"
  "s_waitcnt vmcnt(8)\n"
  "v_add_f32_dpp v52, v10, v8 row_ror:8 row_mask:0xf bank_mask:0xf\n"
  "v_add_f32_dpp v53, v11, v9 row_ror:8 row_mask:0xf bank_mask:0xf\n"
  "s_nop 0\n"
  "v_mov_b32 v28, 0\n"
  "s_waitcnt vmcnt(7)\n"
  "v_add_f32_dpp v54, v14, v12 row_ror:8 row_mask:0xf bank_mask:0xf\n"
  "v_add_f32_dpp v55, v15, v13 row_ror:8 row_mask:0xf bank_mask:0xf\n"
  "v_pk_add_f32 v[52:53], v[52:53], v[54:55]\n"
  "v_max3_f32 v56, v52, v53, v58\n"
  "s_nop 1\n"
  "v_max_f32_dpp v56, v56, v56 quad_perm:[1,0,3,2] row_mask:0xf bank_mask:0xf\n"
  "s_nop 1\n"
  "v_max_f32_dpp v56, v56, v56 quad_perm:[2,3,0,1] row_mask:0xf bank_mask:0xf\n"
  "v_sub_f32 v54, v58, v56\n"
  "v_pk_add_f32 v[52:53], v[52:53], v[56:57] op_sel_hi:[1,0] neg_lo:[0,1] neg_hi:[0,1]\n"
  "v_exp_f32 v54, v54\n"
  "v_exp_f32 v52, v52\n"
  "v_exp_f32 v53, v53\n"
  "v_fma_f32 v56, v54, v59, v52\n"
  "v_add_f32 v56, v56, v53\n"
  "s_nop 1\n"
  "v_add_f32_dpp v56, v56, v56 quad_perm:[1,0,3,2] row_mask:0xf bank_mask:0xf\n"
  "s_nop 1\n"
  "v_add_f32_dpp v56, v56, v56 quad_perm:[2,3,0,1] row_mask:0xf bank_mask:0xf\n"
  "v_rcp_f32 v56, v56\n"
  "s_nop 0\n"
  "v_mul_f32 v54, v54, v56\n"
  "v_pk_mul_f32 v[0:1], v[0:1], v[54:55] op_sel_hi:[1,0]\n"
  "v_pk_mul_f32 v[2:3], v[2:3], v[54:55] op_sel_hi:[1,0]\n"
  "v_pk_mul_f32 v[4:5], v[4:5], v[54:55] op_sel_hi:[1,0]\n"
  "v_pk_mul_f32 v[6:7], v[6:7], v[54:55] op_sel_hi:[1,0]\n"
  "v_cvt_pk_bf16_f32 v46, v0, v1\n"
  "v_cvt_pk_bf16_f32 v48, v2, v3\n"
  "v_cvt_pk_bf16_f32 v47, v4, v5\n"
  "v_cvt_pk_bf16_f32 v49, v6, v7\n"
  "v_pk_mul_f32 v[52:53], v[52:53], v[56:57] op_sel_hi:[1,0]\n"
  "v_cvt_pk_bf16_f32 v50, v52, v53\n"
  "v_smfmac_f32_16x16x64_bf16 v[32:35], v[46:49], v[76:83], v208\n"
  "v_smfmac_f32_16x16x64_bf16 v[36:39], v[46:49], v[84:91], v208\n"
  "s_waitcnt vmcnt(3)\n"
  "v_smfmac_f32_16x16x32_bf16 v[32:35], v[50:51], v[40:43], v208\n"
  "v_smfmac_f32_16x16x32_bf16 v[36:39], v[50:51], v[42:45], v208\n"
  "s_nop 3\n"
  "v_add_f32 v52, v32, v33\n"
  "s_nop 2\n"
  "v_add_f32 v53, v38, v39\n"
  "v_add_f32 v52, v52, v53\n"
  "ds_write_b32 v206, v52\n"
  "v_cmp_u_f32 vcc, v52, v52\n"
  "s_waitcnt lgkmcnt(0)\n"
  "s_barrier\n"
  "s_cbranch_vccnz Lc7a_sb_%=\n"
  "Lc7a_sbb_%=:\n"
  "s_waitcnt lgkmcnt(0)\n"
  "s_barrier\n"
  "ds_read_b32 v25, v24 offset:7424\n"
  "s_waitcnt lgkmcnt(0)\n"
  "v_cmp_ne_u32 vcc, 0, v25\n"
  "s_cbranch_vccnz Lc7a_rt_%=\n"
  "s_add_u32 s44, s44, 1\n"
  "s_mov_b32 s52, s45\n"
  "s_mov_b32 s45, s46\n"
  "s_mov_b32 s46, s47\n"
  "s_mov_b32 s47, s72\n"
  "s_mov_b32 s72, s52\n"
  "s_cmp_ge_u32 s44, 63\n"
  "s_cbranch_scc1 Lc7_out_%=\n"
  "s_nop 15\n"
  "s_nop 13\n"
  "buffer_load_dwordx4 v[104:107], v205, s[40:43], s48 offen offset:2560 sc1\n"
  "buffer_load_dword v66, v207, s[40:43], 0 offen offset:0 sc1\n"
  "s_add_u32 s48, s48, 2097152\n"
  "s_xor_b32 s48, s48, 0x2000\n"
  "Lc7b_nxt_%=:\n"
  "Lc7b_top_%=:\n"
  "v_mov_b32 v32, 0\n"
  "v_mov_b32 v33, 0\n"
  "v_mov_b32 v34, 0\n"
  "v_mov_b32 v35, 0\n"
  "v_mov_b32 v36, 0\n"
  "v_mov_b32 v37, 0\n"
  "v_mov_b32 v38, 0\n"
  "v_mov_b32 v39, 0\n"
  "Lc7b_ge_%=:\n"
  "s_barrier\n"
  "buffer_load_dwordx4 v[16:19], v202, s[40:43], s45 offen offset:0 sc1\n"
  "s_nop 7\n"
  "buffer_load_dwordx4 v[20:23], v203, s[40:43], s45 offen offset:0 sc1\n"
  "s_nop 15\n"
  "s_nop 7\n"
  "buffer_load_dwordx4 v[8:11], v200, s[40:43], s45 offen offset:0 sc1\n"
  "s_nop 8\n"
  "buffer_load_dwordx4 v[12:15], v201, s[40:43], s45 offen offset:0 sc1\n"
  "s_nop 6\n"
  "s_waitcnt vmcnt(4)\n"
  "s_add_u32 s52, s44, 2\n"
  "v_cmp_gt_u32 vcc, s52, v66\n"
  "s_cbranch_vccnz Lc7b_fv_%=\n"
  "Lc7b_fvb_%=:\n"
  "s_nop 5\n"
  "s_nop 1\n"
  "v_mov_b32 v28, 0\n"
  "buffer_load_dword v40, v204, s[40:43], s45 offen offset:0 sc1\n"
  "v_mov_b32 v28, 0\n"
  "buffer_load_dword v41, v204, s[40:43], s45 offen offset:64 sc1\n"
  "v_mov_b32 v28, 0\n"
  "buffer_load_dword v42, v204, s[40:43], s45 offen offset:128 sc1\n"
  "v_mov_b32 v28, 0\n"
  "buffer_load_dword v43, v204, s[40:43], s45 offen offset:192 sc1\n"
  "v_mov_b32 v28, 0\n"
  "v_mov_b32 v28, 0\n"
  "s_nop 2\n"
  "v_mov_b32 v28, 0\n"
  "s_nop 2\n"
  "v_mov_b32 v28, 0\n"
  "s_nop 2\n"
  "v_mov_b32 v28, 0\n"
  "s_nop 2\n"
  "v_mov_b32 v28, 0\n"
  "s_barrier\n"
  "v_mov_b32 v28, 0\n"
  "s_nop 2\n"
  "v_mov_b32 v28, 0\n"
  "s_nop 2\n"
  "v_mov_b32 v28, 0\n"
  "buffer_load_dwordx4 v[76:79], v205, s[40:43], s48 offen offset:0 sc1\n"
  "v_mov_b32 v28, 0\n"
  "s_nop 2\n"
  "v_mov_b32 v28, 0\n"
  "s_nop 2\n"
  "v_mov_b32 v28, 0\n"
  "buffer_load_dwordx4 v[80:83], v205, s[40:43], s48 offen offset:2048 sc1\n"
  "v_mov_b32 v28, 0\n"
  "s_nop 2\n"
  "v_mov_b32 v28, 0\n"
  "s_nop 2\n"
  "v_mov_b32 v28, 0\n"
  "buffer_load_dwordx4 v[84:87], v205, s[40:43], s48 offen offset:512 sc1\n"
  "v_mov_b32 v28, 0\n"
  "s_nop 1\n"
  "v_mov_b32 v28, 0\n"
  "s_waitcnt vmcnt(9)\n"
  "v_max3_f32 v58, v16, v17, v18\n"
  "v_max3_f32 v58, v58, v19, v20\n"
  "v_max3_f32 v58, v58, v21, v22\n"
  "v_max_f32 v58, v58, v23\n"
  "v_pk_add_f32 v[0:1], v[16:17], v[58:59] op_sel_hi:[1,0] neg_lo:[0,1] neg_hi:[0,1]\n"
  "v_pk_add_f32 v[2:3], v[18:19], v[58:59] op_sel_hi:[1,0] neg_lo:[0,1] neg_hi:[0,1]\n"
  "v_pk_add_f32 v[4:5], v[20:21], v[58:59] op_sel_hi:[1,0] neg_lo:[0,1] neg_hi:[0,1]\n"
  "v_pk_add_f32 v[6:7], v[22:23], v[58:59] op_sel_hi:[1,0] neg_lo:[0,1] neg_hi:[0,1]\n"
  "v_exp_f32 v0, v0\n"
  "v_exp_f32 v1, v1\n"
  "v_exp_f32 v2, v2\n"
  "v_exp_f32 v3, v3\n"
  "v_exp_f32 v4, v4\n"
  "v_exp_f32 v5, v5\n"
  "v_exp_f32 v6, v6\n"
  "v_exp_f32 v7, v7\n"
  "v_pk_add_f32 v[52:53], v[0:1], v[2:3]\n"
  "v_pk_add_f32 v[54:55], v[4:5], v[6:7]\n"
  "v_pk_add_f32 v[52:53], v[52:53], v[54:55]\n"
  "v_add_f32 v59, v52, v53\n"
  "s_waitcnt vmcnt(8)\n"
  "v_add_f32_dpp v52, v10, v8 row_ror:8 row_mask:0xf bank_mask:0xf\n"
  "v_add_f32_dpp v53, v11, v9 row_ror:8 row_mask:0xf bank_mask:0xf\n"
  "s_nop 0\n"
  "v_mov_b32 v28, 0\n"
  "s_waitcnt vmcnt(7)\n"
  "v_add_f32_dpp v54, v14, v12 row_ror:8 row_mask:0xf bank_mask:0xf\n"
  "v_add_f32_dpp v55, v15, v13 row_ror:8 row_mask:0xf bank_mask:0xf\n"
  "v_pk_add_f32 v[52:53], v[52:53], v[54:55]\n"
  "v_max3_f32 v56, v52, v53, v58\n"
  "s_nop 1\n"
  "v_max_f32_dpp v56, v56, v56 quad_perm:[1,0,3,2] row_mask:0xf bank_mask:0xf\n"
  "s_nop 1\n"
  "v_max_f32_dpp v56, v56, v56 quad_perm:[2,3,0,1] row_mask:0xf bank_mask:0xf\n"
  "v_sub_f32 v54, v58, v56\n"
  "v_pk_add_f32 v[52:53], v[52:53], v[56:57] op_sel_hi:[1,0] neg_lo:[0,1] neg_hi:[0,1]\n"
  "v_exp_f32 v54, v54\n"
  "v_exp_f32 v52, v52\n"
  "v_exp_f32 v53, v53\n"
  "v_fma_f32 v56, v54, v59, v52\n"
  "v_add_f32 v56, v56, v53\n"
  "s_nop 1\n"
  "v_add_f32_dpp v56, v56, v56 quad_perm:[1,0,3,2] row_mask:0xf bank_mask:0xf\n"
  "s_nop 1\n"
  "v_add_f32_dpp v56, v56, v56 quad_perm:[2,3,0,1] row_mask:0xf bank_mask:0xf\n"
  "v_rcp_f32 v56, v56\n"
  "s_nop 0\n"
  "v_mul_f32 v54, v54, v56\n"
  "v_pk_mul_f32 v[0:1], v[0:1], v[54:55] op_sel_hi:[1,0]\n"
  "v_pk_mul_f32 v[2:3], v[2:3], v[54:55] op_sel_hi:[1,0]\n"
  "v_pk_mul_f32 v[4:5], v[4:5], v[54:55] op_sel_hi:[1,0]\n"
  "v_pk_mul_f32 v[6:7], v[6:7], v[54:55] op_sel_hi:[1,0]\n"
  "v_cvt_pk_bf16_f32 v46, v0, v1\n"
  "v_cvt_pk_bf16_f32 v48, v2, v3\n"
  "v_cvt_pk_bf16_f32 v47, v4, v5\n"
  "v_cvt_pk_bf16_f32 v49, v6, v7\n"
  "v_pk_mul_f32 v[52:53], v[52:53], v[56:57] op_sel_hi:[1,0]\n"
  "v_cvt_pk_bf16_f32 v50, v52, v53\n"
  "v_smfmac_f32_16x16x64_bf16 v[32:35], v[46:49], v[92:99], v208\n"
  "v_smfmac_f32_16x16x64_bf16 v[36:39], v[46:49], v[100:107], v208\n"
  "s_waitcnt vmcnt(3)\n"
  "v_smfmac_f32_16x16x32_bf16 v[32:35], v[50:51], v[40:43], v208\n"
  "v_smfmac_f32_16x16x32_bf16 v[36:39], v[50:51], v[42:45], v208\n"
  "s_nop 3\n"
  "v_add_f32 v52, v32, v33\n"
  "s_nop 2\n"
  "v_add_f32 v53, v38, v39\n"
  "v_add_f32 v52, v52, v53\n"
  "ds_write_b32 v206, v52\n"
  "v_cmp_u_f32 vcc, v52, v52\n"
  "s_waitcnt lgkmcnt(0)\n"
  "s_barrier\n"
  "s_cbranch_vccnz Lc7b_sb_%=\n"
  "Lc7b_sbb_%=:\n"
  "s_waitcnt lgkmcnt(0)\n"
  "s_barrier\n"
  "ds_read_b32 v25, v24 offset:7424\n"
  "s_waitcnt lgkmcnt(0)\n"
  "v_cmp_ne_u32 vcc, 0, v25\n"
  "s_cbranch_vccnz Lc7b_rt_%=\n"
  "s_add_u32 s44, s44, 1\n"
  "s_mov_b32 s52, s45\n"
  "s_mov_b32 s45, s46\n"
  "s_mov_b32 s46, s47\n"
  "s_mov_b32 s47, s72\n"
  "s_mov_b32 s72, s52\n"
  "s_cmp_ge_u32 s44, 63\n"
  "s_cbranch_scc1 Lc7_out_%=\n"
  "s_nop 15\n"
  "s_nop 13\n"
  "buffer_load_dwordx4 v[88:91], v205, s[40:43], s48 offen offset:2560 sc1\n"
  "buffer_load_dword v66, v207, s[40:43], 0 offen offset:0 sc1\n"
  "s_add_u32 s48, s48, 2097152\n"
  "s_xor_b32 s48, s48, 0x2000\n"
  "s_branch Lc7a_nxt_%=\n"
  "Lc7_out_%=:\n"
  "s_branch Lc_end_%=\n"
  "Lc7_v0_%=:\n"
  "s_cmp_eq_u32 s53, 0\n"
  "s_cbranch_scc1 Lc7_v0b_%=\n"
  "s_sub_u32 s53, s53, 1\n"
  "buffer_load_dword v66, v207, s[40:43], 0 offen offset:0 sc1\n"
  "s_waitcnt vmcnt(0)\n"
  "v_cmp_gt_u32 vcc, s52, v66\n"
  "s_cbranch_vccnz Lc7_v0_%=\n"
  "s_branch Lc7_v0b_%=\n"
  "Lc7_v1_%=:\n"
  "s_cmp_eq_u32 s53, 0\n"
  "s_cbranch_scc1 Lc7_v1b_%=\n"
  "s_sub_u32 s53, s53, 1\n"
  "buffer_load_dword v66, v207, s[40:43], 0 offen offset:0 sc1\n"
  "s_waitcnt vmcnt(0)\n"
  "v_cmp_gt_u32 vcc, s52, v66\n"
  "s_cbranch_vccnz Lc7_v1_%=\n"
  "s_branch Lc7_v1b_%=\n"
  "Lc7a_fv_%=:\n"
  "s_cmp_eq_u32 s53, 0\n"
  "s_cbranch_scc1 Lc7a_fvb_%=\n"
  "s_sub_u32 s53, s53, 1\n"
  "buffer_load_dword v66, v207, s[40:43], 0 offen offset:0 sc1\n"
  "s_waitcnt vmcnt(0)\n"
  "v_cmp_gt_u32 vcc, s52, v66\n"
  "s_cbranch_vccnz Lc7a_fv_%=\n"
  "s_branch Lc7a_fvb_%=\n"
  "Lc7a_sb_%=:\n"
  "s_cmp_eq_u32 s53, 0\n"
  "s_cbranch_scc1 Lc7a_sbb_%=\n"
  "s_sub_u32 s53, s53, 1\n"
  "ds_write_b32 v24, v64 offset:7424\n"
  "s_branch Lc7a_sbb_%=\n"
  "Lc7a_rt_%=:\n"
  "s_mov_b32 s74, 1\n"
  "s_waitcnt lgkmcnt(0)\n"
  "s_barrier\n"
  "ds_write_b32 v24, v24 offset:7424\n"
  "s_waitcnt lgkmcnt(0)\n"
  "s_nop 7\n"
  "s_cmp_eq_u32 s50, 0\n"
  "s_cbranch_scc1 Lc7b_top_%=\n"
  "s_branch Lc7a_top_%=\n"
  "Lc7b_fv_%=:\n"
  "s_cmp_eq_u32 s53, 0\n"
  "s_cbranch_scc1 Lc7b_fvb_%=\n"
  "s_sub_u32 s53, s53, 1\n"
  "buffer_load_dword v66, v207, s[40:43], 0 offen offset:0 sc1\n"
  "s_waitcnt vmcnt(0)\n"
  "v_cmp_gt_u32 vcc, s52, v66\n"
  "s_cbranch_vccnz Lc7b_fv_%=\n"
  "s_branch Lc7b_fvb_%=\n"
  "Lc7b_sb_%=:\n"
  "s_cmp_eq_u32 s53, 0\n"
  "s_cbranch_scc1 Lc7b_sbb_%=\n"
  "s_sub_u32 s53, s53, 1\n"
  "ds_write_b32 v24, v64 offset:7424\n"
  "s_branch Lc7b_sbb_%=\n"
  "Lc7b_rt_%=:\n"
  "s_mov_b32 s74, 1\n"
  "s_waitcnt lgkmcnt(0)\n"
  "s_barrier\n"
  "ds_write_b32 v24, v24 offset:7424\n"
  "s_waitcnt lgkmcnt(0)\n"
  "s_nop 7\n"
  "s_cmp_eq_u32 s50, 0\n"
  "s_cbranch_scc1 Lc7_s2_%=\n"
  "s_branch Lc7b_top_%=\n"
  "Lc_end_%=:\n"
  :: [r0] "s"((unsigned)b), [r1] "s"((unsigned)(b >> 32) & 0xffffu), [r2] "s"(W_END),
     [wave] "s"(__builtin_amdgcn_readfirstlane((threadIdx.x >> 6) + (isA ? 0 : 4)))
  : "v0", "v1", "v2", "v3", "v4", "v5", "v6", "v7", "v8", "v9", "v10", "v11", "v12", "v13", "v14", "v15", "v16", "v17", "v18", "v19", "v20", "v21", "v22", "v23", "v24", "v25", "v26", "v27", "v28", "v29", "v30", "v31", "v32", "v33", "v34", "v35", "v36", "v37", "v38", "v39", "v40", "v41", "v42", "v43", "v44", "v45", "v46", "v47", "v48", "v49", "v50", "v51", "v52", "v53", "v54", "v55", "v56", "v57", "v58", "v59", "v60", "v61", "v62", "v63", "v64", "v65", "v66", "v67", "v68", "v69", "v70", "v71", "v72", "v73", "v74", "v75", "v76", "v77", "v78", "v79", "v80", "v81", "v82", "v83", "v84", "v85", "v86", "v87", "v88", "v89", "v90", "v91", "v92", "v93", "v94", "v95", "v96", "v97", "v98", "v99", "v100", "v101", "v102", "v103", "v104", "v105", "v106", "v107", "v108", "v109", "v110", "v111", "v112", "v113", "v114", "v115", "v116", "v117", "v118", "v119", "v120", "v121", "v122", "v123", "v124", "v125", "v126", "v127", "v128", "v129", "v130", "v131", "v132", "v133", "v134", "v135", "v136", "v137", "v138", "v139", "v140", "v141", "v142", "v143", "v144", "v145", "v146", "v147", "v148", "v149", "v150", "v151", "v152", "v153", "v154", "v155", "v156", "v157", "v158", "v159", "v160", "v161", "v162", "v163", "v164", "v165", "v166", "v167", "v168", "v169", "v170", "v171", "v172", "v173", "v174", "v175", "v176", "v177", "v178", "v179", "v180", "v181", "v182", "v183", "v184", "v185", "v186", "v187", "v188", "v189", "v190", "v191", "v192", "v193", "v194", "v195", "v196", "v197", "v198", "v199", "v200", "v201", "v202", "v203", "v204", "v205", "v206", "v207", "v208", "v209", "v210", "v211", "v212", "v213", "v214", "v215", "v216", "v217", "v218", "v219", "v255", "a0", "a1", "a2", "a3", "a4", "a5", "a6", "a7", "a8", "a9", "a10", "a11", "a12", "a13", "a14", "a15", "a16", "a17", "a18", "a19", "a20", "a21", "a22", "a23", "a24", "a25", "a26", "a27", "a28", "a29", "a30", "a31", "a32", "a33", "a34", "a35", "a36", "a37", "a38", "a39", "a40", "a41", "a42", "a43", "a44", "a45", "a46", "a47", "a48", "a49", "a50", "a51", "a52", "a53", "a54", "a55", "a56", "a57", "a58", "a59", "a60", "a61", "a62", "a63", "a64", "a65", "a66", "a67", "a68", "a69", "a70", "a71", "a72", "a73", "a74", "a75", "a76", "a77", "a78", "a79", "a80", "a81", "a82", "a83", "a84", "a85", "a86", "a87", "a88", "a89", "a90", "a91", "a92", "a93", "a94", "a95", "a96", "a97", "a98", "a99", "a100", "a101", "a102", "a103", "a104", "a105", "a106", "a107", "a108", "a109", "a110", "a111", "a112", "a113", "a114", "a115", "a116", "a117", "a118", "a119", "a120", "a121", "a122", "a123", "a124", "a125", "a126", "a127", "a128", "a129", "a130", "a131", "a132", "a133", "a134", "a135", "a136", "a137", "a138", "a139", "a140", "a141", "a142", "a143", "a144", "a145", "a146", "a147", "a148", "a149", "a150", "a151", "a152", "a153", "a154", "a155", "a156", "a157", "a158", "a159", "a160", "a161", "a162", "a163", "a164", "a165", "a166", "a167", "a168", "a169", "a170", "a171", "a172", "a173", "a174", "a175", "a176", "a177", "a178", "a179", "a180", "a181", "a182", "a183", "a184", "a185", "a186", "a187", "a188", "a189", "a190", "a191", "a192", "a193", "a194", "a195", "a196", "a197", "a198", "a199", "a200", "a201", "a202", "a203", "a204", "a205", "a206", "a207", "a208", "a209", "a210", "a211", "a212", "a213", "a214", "a215", "a216", "a217", "a218", "a219", "a220", "a221", "a222", "a223", "s40", "s41", "s42", "s43", "s44", "s45", "s46", "s47", "s48", "s49", "s50", "s51", "s52", "s53", "s54", "s55", "s56", "s57", "s58", "s59", "s60", "s61", "s62", "s63", "s64", "s65", "s66", "s67", "s68", "s69", "s70", "s71", "s72", "s73", "s74", "s75", "s76", "s77", "s78", "s79", "s80", "s81", "s82", "s83", "s84", "s85", "s86", "s87", "s88", "s89", "s90", "s91", "s92", "s93", "s94", "s95", "s96", "s97", "s98", "s99", "vcc", "scc", "memory");
}

extern "C" void kernel_launch(void* const* d_in, const int* in_sizes, int n_in, void* d_out, int out_size, void* d_ws, size_t ws_size, hipStream_t stream) {
  Args a;
  a.x = (const u16*)d_in[0]; a.mem0 = (const u16*)d_in[1];
  a.wq = (const u16*)d_in[2]; a.wk = (const u16*)d_in[3]; a.wv = (const u16*)d_in[4]; a.wo = (const u16*)d_in[5];
  a.wg = (const u16*)d_in[8]; a.wu = (const u16*)d_in[9]; a.wd = (const u16*)d_in[10];
  a.out = (u16*)d_out;
  a.ws = (unsigned char*)d_ws;
  constexpr int LDS_PRE = PRE_LN + 8192, LDS_MAIN = 4 * 28672;
  (void)hipFuncSetAttribute((const void*)k_pre, hipFuncAttributeMaxDynamicSharedMemorySize, LDS_PRE);
  (void)hipFuncSetAttribute((const void*)k_main, hipFuncAttributeMaxDynamicSharedMemorySize, LDS_MAIN);
  k_pre<<<256, 256, LDS_PRE, stream>>>(a);
  k_main<<<256, 256, LDS_MAIN, stream>>>(a);
}
```
